```python
import torch

def _softmax(x, axis=-1):
    return torch.softmax(x, dim=axis)

def _gelu(x):
    return torch.nn.functional.gelu(x, approximate='tanh')

def _silu(x):
    return torch.nn.functional.silu(x)

def _sigmoid(x):
    return torch.sigmoid(x)

def _layernorm(x, axis=-1, eps=1e-05):
    m = x.mean(dim=axis, keepdim=True)
    v = x.var(dim=axis, keepdim=True, unbiased=False)
    return (x - m) / torch.sqrt(v + eps)

def _scatter_add(tgt, idx, src, axis=0):
    return tgt.clone().index_add_(axis, idx, src)

def _segment_sum(src, seg, n):
    out = torch.zeros((n,) + src.shape[1:], dtype=src.dtype)
    return out.index_add_(0, seg, src)

def _pad(x, pw):
    flat = tuple((v for pair in reversed(pw) for v in pair))
    return torch.nn.functional.pad(x, flat)
kernel_name = 'adaLN_cond__memory_aug_recurrent_mem_tr__glu_gated_geglu__prompt'

def setup_inputs():

    _w = lambda a: (lambda t: t.to(torch.bfloat16) if t.is_floating_point() else t)(torch.as_tensor(a))

    def _A_setup():
        B, S, Dm = (1, 2048, 640)
        H, Dh = (32, 192)
        M, seg = (8, 32)
        seg = min(seg, S)
        S = S // seg * seg
        return {'x': _w((lambda *s: torch.randn(*s, dtype=torch.bfloat16) * 0.05)(B, S, Dm)), 'mem0': _w((lambda *s: torch.randn(*s, dtype=torch.bfloat16) * 0.8)(B, M, Dm)), 'Wq': _w((lambda *s: torch.randn(*s, dtype=torch.bfloat16) * 0.8)(Dm, H, Dh) * Dm ** (-0.5)), 'Wk': _w((lambda *s: torch.randn(*s, dtype=torch.bfloat16) * 0.8)(Dm, H, Dh) * Dm ** (-0.5)), 'Wv': _w((lambda *s: torch.randn(*s, dtype=torch.bfloat16) * 0.8)(Dm, H, Dh) * Dm ** (-0.5)), 'Wo': _w((lambda *s: torch.randn(*s, dtype=torch.bfloat16) * 0.8)(H, Dh, Dm) * (H * Dh) ** (-0.5)), 'seg_len': seg}
    _A_in = _A_setup()

    def _B_setup():
        B, S, Dm, Df = (1, 2048, 640, 1536)
        return {'x': _w((lambda *s: torch.randn(*s, dtype=torch.bfloat16) * 0.05)(B, S, Dm)), 'w_gate': _w((lambda *s: torch.randn(*s, dtype=torch.bfloat16) * 0.4)(Dm, Df) * Dm ** (-0.5)), 'w_up': _w((lambda *s: torch.randn(*s, dtype=torch.bfloat16) * 0.4)(Dm, Df) * Dm ** (-0.5)), 'w_down': _w((lambda *s: torch.randn(*s, dtype=torch.bfloat16) * 0.4)(Df, Dm) * Df ** (-0.5))}
    _B_in = _B_setup()
    return {'A_x': _A_in['x'], 'A_mem0': _A_in['mem0'], 'A_Wq': _A_in['Wq'], 'A_Wk': _A_in['Wk'], 'A_Wv': _A_in['Wv'], 'A_Wo': _A_in['Wo'], 'A_seg_len': _A_in['seg_len'], 'B_x': _B_in['x'], 'B_w_gate': _B_in['w_gate'], 'B_w_up': _B_in['w_up'], 'B_w_down': _B_in['w_down'], 'c': _w((lambda *s: torch.randn(*s, dtype=torch.bfloat16) * 0.2)(1, 640) * 8.0), 'w_mod': _w((lambda *s: torch.randn(*s, dtype=torch.bfloat16) * 0.2)(640, 6 * 640) * 640 ** (-0.5) * 8.0)}

def reference(A_x, A_mem0, A_Wq, A_Wk, A_Wv, A_Wo, A_seg_len, B_x, B_w_gate, B_w_up, B_w_down, c, w_mod):

    def _A_ref(x, mem0, Wq, Wk, Wv, Wo, seg_len):
        B, S, Dm = x.shape
        H, Dh = (Wq.shape[1], Wq.shape[2])
        M = mem0.shape[1]
        scale = Dh ** (-0.5)
        n_seg = S // seg_len
        L = M + seg_len
        idx = torch.arange(L)
        mask = (idx[:, None] < M) | (idx[:, None] >= idx[None, :])
        mem = mem0
        outs = []
        for s in range(n_seg):
            xs = x[:, s * seg_len:(s + 1) * seg_len, :].clone()
            h = torch.concatenate([mem, xs], axis=1)
            q = torch.einsum('btd,dhe->bhte', h, Wq)
            k = torch.einsum('btd,dhe->bhte', h, Wk)
            v = torch.einsum('btd,dhe->bhte', h, Wv)
            scores = torch.einsum('bhqe,bhke->bhqk', q, k) * scale
            scores = torch.where(mask[None, None], scores, -1000000000.0)
            p = _softmax(scores, axis=-1)
            o = torch.einsum('bhqk,bhke->bhqe', p, v)
            o = torch.einsum('bhte,hed->btd', o, Wo)
            mem = o[:, :M, :].clone()
            outs.append(o[:, M:, :])
        return torch.concatenate(outs, axis=1)

    def _B_ref(x, w_gate, w_up, w_down):
        g = torch.einsum('bsd,df->bsf', x, w_gate)
        u = torch.einsum('bsd,df->bsf', x, w_up)
        h = _gelu(g) * u
        return torch.einsum('bsf,fd->bsd', h, w_down)
    x = A_x
    mod = (_silu(c) @ w_mod).reshape(c.shape[0], 6, 1, -1)
    sa, ca, ga, sb, cb, gb = (mod[:, 0].clone(), mod[:, 1].clone(), mod[:, 2].clone(), mod[:, 3].clone(), mod[:, 4].clone(), mod[:, 5].clone())
    h = _layernorm(x, axis=-1) * (1 + ca) + sa
    x = x + ga * _A_ref(h, A_mem0, A_Wq, A_Wk, A_Wv, A_Wo, A_seg_len)
    h = _layernorm(x, axis=-1) * (1 + cb) + sb
    x = x + gb * _B_ref(h, B_w_gate, B_w_up, B_w_down)
    return x
```

```cpp
#include <hip/hip_runtime.h>
#include <hip/hip_bf16.h>
#include <type_traits>
#include <cstdint>

#define LAS __attribute__((address_space(3)))
typedef unsigned u32x2 __attribute__((ext_vector_type(2)));
typedef unsigned u32x4 __attribute__((ext_vector_type(4)));
typedef unsigned u32x8 __attribute__((ext_vector_type(8)));
typedef float f32x2 __attribute__((ext_vector_type(2)));
typedef float f32x4 __attribute__((ext_vector_type(4)));
typedef short s16x4 __attribute__((ext_vector_type(4)));
typedef __bf16 bf16x2 __attribute__((ext_vector_type(2)));
typedef __bf16 bf16x8 __attribute__((ext_vector_type(8)));
typedef __bf16 bf16x16 __attribute__((ext_vector_type(16)));
typedef __amdgpu_buffer_rsrc_t rsrc_t;

namespace {

constexpr int DM = 640, NH = 32, DH = 192, NSEG = 64, DFF = 1536;
constexpr unsigned SENT = 0xFFFFFFFFu;

constexpr unsigned FS = 128;
constexpr unsigned FL_ABORT = 0;
constexpr unsigned OFF_MOD = 1024;
constexpr unsigned FL_H = 16384;
constexpr unsigned FL_KM = 24576, FL_K = 28672, FL_V = 32768, FL_U = 36864, FL_PX = 40960;
constexpr unsigned FL_X1 = 45056;
constexpr unsigned FL_ACT = 49152;
constexpr unsigned INIT_BYTES = 57344;
constexpr unsigned OFF_XR = 65536;
constexpr unsigned MEM_SZ = 10240, P_SZ = 20480;
constexpr unsigned L_MEM = 0, L_P = 64 * MEM_SZ, L_END = L_P + 64 * P_SZ;
constexpr unsigned OFF_CAL = OFF_XR + 2 * L_END;
constexpr unsigned OFF_M = OFF_CAL + 65536;
constexpr unsigned M_SZ = 40 * 16384;
constexpr unsigned OFF_HB = OFF_M + 64 * M_SZ;
constexpr unsigned HB_SZ = 40960;
constexpr unsigned OFF_KX = OFF_HB + 64 * HB_SZ;
constexpr unsigned KX_SZ = 12288;
constexpr unsigned OFF_VX = OFF_KX + 64 * 32 * KX_SZ;
constexpr unsigned OFF_U = OFF_VX + 64 * 32 * KX_SZ;
constexpr unsigned U_SZ = 40 * 32 * 1024;
constexpr unsigned OFF_KM = OFF_U + 64 * U_SZ;
constexpr unsigned OFF_PX = OFF_KM + 64 * 32 * 3072;
constexpr unsigned PX_SZ = 81920;
constexpr unsigned OFF_X1 = OFF_PX + 64 * PX_SZ;
constexpr unsigned OFF_ACT = OFF_X1 + 2048 * 640 * 2;
constexpr unsigned ACT_SZ = 48 * 2 * 1024;
constexpr unsigned OFF_DBG = OFF_ACT + 64 * ACT_SZ;
constexpr unsigned WS_BYTES = OFF_DBG + (1u << 20);
static_assert(WS_BYTES + (2u << 20) <= (256u << 20), "workspace");
static_assert(OFF_XR % 16384 == 0 && OFF_CAL % 16384 == 0 && OFF_M % 16384 == 0, "alignment");

constexpr int LDS_BYTES = 160 * 1024;
constexpr unsigned SPIN_CAP = 1u << 21;
#define PZ_SLEEP 24
#define PZ_TWO_PHASE 1
#define PZ_GAP 0
#define UST_AUX SC1
#define UST_VO ((unsigned)c.lane * 16)
#define H_SLEEP 12
#define V_SLEEP 16

struct Params {
  const void* x; const void* mem0; const void* Wq; const void* Wk; const void* Wv; const void* Wo;
  const void* wg; const void* wu; const void* wd; const void* c; const void* wmod;
  void* out; void* ws;
  unsigned mask; unsigned pad;
};

template <int I, int N, class F>
__device__ __forceinline__ void sfor(F&& f) {
  if constexpr (I < N) { f(std::integral_constant<int, I>{}); sfor<I + 1, N>(f); }
}
__device__ __forceinline__ rsrc_t mk_rsrc(const void* p, unsigned bytes) {
  return __builtin_amdgcn_make_buffer_rsrc(const_cast<void*>(p), 0, bytes, 0x00020000);
}
template <int AUX> __device__ __forceinline__ u32x4 ld16(rsrc_t r, unsigned vo, unsigned so) { return __builtin_amdgcn_raw_buffer_load_b128(r, vo, so, AUX); }
template <int AUX> __device__ __forceinline__ u32x2 ld8(rsrc_t r, unsigned vo, unsigned so) { return __builtin_amdgcn_raw_buffer_load_b64(r, vo, so, AUX); }
template <int AUX> __device__ __forceinline__ unsigned ld4(rsrc_t r, unsigned vo, unsigned so) { return __builtin_amdgcn_raw_buffer_load_b32(r, vo, so, AUX); }
template <int AUX> __device__ __forceinline__ void st16(rsrc_t r, unsigned vo, unsigned so, u32x4 v) { __builtin_amdgcn_raw_buffer_store_b128(v, r, vo, so, AUX); }
template <int AUX> __device__ __forceinline__ void st8(rsrc_t r, unsigned vo, unsigned so, u32x2 v) { __builtin_amdgcn_raw_buffer_store_b64(v, r, vo, so, AUX); }
template <int AUX> __device__ __forceinline__ void st4(rsrc_t r, unsigned vo, unsigned so, unsigned v) { __builtin_amdgcn_raw_buffer_store_b32(v, r, vo, so, AUX); }
constexpr int SC1 = 16;
template <int AUX> __device__ __forceinline__ void dma16(rsrc_t r, LAS unsigned char* dst, unsigned vo, unsigned so) {
  __builtin_amdgcn_raw_ptr_buffer_load_lds(r, (LAS void*)dst, 16, vo, so, 0, AUX);
}
template <int N> __device__ __forceinline__ void wait_vm() { asm volatile("s_waitcnt vmcnt(%0)" ::"n"(N) : "memory"); }
__device__ __forceinline__ void wait_lgkm() { asm volatile("s_waitcnt lgkmcnt(0)" ::: "memory"); }
__device__ __forceinline__ void bar() { asm volatile("s_waitcnt lgkmcnt(0)\n\ts_barrier" ::: "memory"); }

__device__ __forceinline__ unsigned pk(float a, float b) {
  f32x2 v = {a, b};
  return __builtin_bit_cast(unsigned, __builtin_convertvector(v, bf16x2));
}
__device__ __forceinline__ unsigned pks(float a, float b) { return min(pk(a, b), 0xFFFFFFFEu); }
__device__ __forceinline__ float bf_lo(unsigned u) { return __builtin_bit_cast(float, u << 16); }
__device__ __forceinline__ float bf_hi(unsigned u) { return __builtin_bit_cast(float, u & 0xFFFF0000u); }
__device__ __forceinline__ float rbf(float a) { return bf_lo(pk(a, a)); }
__device__ __forceinline__ float f_(unsigned u) { return __builtin_bit_cast(float, u); }
__device__ __forceinline__ unsigned u_(float f) { return __builtin_bit_cast(unsigned, f); }
__device__ __forceinline__ unsigned umax3(unsigned a, unsigned b, unsigned c) { return max(max(a, b), c); }
__device__ __forceinline__ unsigned umax4(unsigned m, u32x4 v) { return umax3(umax3(m, v.x, v.y), v.z, v.w); }

__device__ __forceinline__ float dot2(unsigned a, unsigned b, float c) {
  return __builtin_amdgcn_fdot2_f32_bf16(__builtin_bit_cast(bf16x2, a), __builtin_bit_cast(bf16x2, b), c, false);
}
__device__ __forceinline__ f32x4 mfma32(u32x4 a, u32x4 b, f32x4 c) {
  return __builtin_amdgcn_mfma_f32_16x16x32_bf16(__builtin_bit_cast(bf16x8, a), __builtin_bit_cast(bf16x8, b), c, 0, 0, 0);
}
__device__ __forceinline__ f32x4 mfma16(u32x2 a, u32x2 b, f32x4 c) {
  return __builtin_amdgcn_mfma_f32_16x16x16bf16_1k(__builtin_bit_cast(s16x4, a), __builtin_bit_cast(s16x4, b), c, 0, 0, 0);
}
__device__ __forceinline__ f32x4 smfma(u32x4 a, u32x8 b, f32x4 c, int idx) {
  return __builtin_amdgcn_smfmac_f32_16x16x64_bf16(__builtin_bit_cast(bf16x8, a), __builtin_bit_cast(bf16x16, b), c, idx, 0, 0);
}
__device__ __forceinline__ u32x2 tr8(LAS unsigned char* p) {
  return __builtin_bit_cast(u32x2, __builtin_amdgcn_ds_read_tr16_b64_v4i16((LAS s16x4*)p));
}
__device__ __forceinline__ u32x4 lds16(LAS unsigned char* p) { return *(LAS u32x4*)p; }
__device__ __forceinline__ u32x4 cat(u32x2 a, u32x2 b) { return u32x4{a.x, a.y, b.x, b.y}; }
__device__ __forceinline__ u32x8 cat(u32x4 a, u32x4 b) { return u32x8{a.x, a.y, a.z, a.w, b.x, b.y, b.z, b.w}; }
__device__ __forceinline__ float halfsum(float x, float y) {
  auto r = __builtin_amdgcn_permlane32_swap(u_(x), u_(y), false, false);
  return f_(r[0]) + f_(r[1]);
}
__device__ __forceinline__ float halfsum1(float x, float y) {
  auto r = __builtin_amdgcn_permlane32_swap(u_(x), u_(y), false, false);
  float o;
  asm("v_add_f32 %0, %1, %2" : "=v"(o) : "v"(r[0]), "v"(r[1]));
  return o;
}
template <int CTRL> __device__ __forceinline__ float dpp(float x) {
  return f_((unsigned)__builtin_amdgcn_update_dpp(0, (int)u_(x), CTRL, 0xf, 0xf, false));
}

template <class T> __device__ __forceinline__ void pin_a(T& v) { asm volatile("" : "+a"(v)); }
template <class T> __device__ __forceinline__ void pin_v(T& v) { asm volatile("" : "+v"(v)); }

__device__ __forceinline__ void sb() { __builtin_amdgcn_sched_barrier(0); }

struct Ctx {
  rsrc_t ws;
  LAS unsigned char* lds;
  int tid, lane, wave;
};

__device__ __forceinline__ void stamp(const Ctx&, int, int = 0) {}
__device__ __forceinline__ void stampc(const Ctx&, int, int = 0) {}
__device__ __forceinline__ void stampf(const Ctx&, int, int = 0) {}
#define CYC(x)
#define CYCW(x)
#define CYCOUT(...)
__device__ __forceinline__ void spin_check(const Ctx& c, unsigned& n) {
  asm volatile("" ::: "memory");
  if (__builtin_expect(((++n) & 1023u) == 0u, 0)) {
    if (n > SPIN_CAP) st4<SC1>(c.ws, FL_ABORT, 0, 1u);
    if (n > SPIN_CAP || __builtin_amdgcn_readfirstlane(ld4<SC1>(c.ws, FL_ABORT, 0)) != SENT) __builtin_amdgcn_endpgm();
  }
}
template <int SLEEP>
__device__ __forceinline__ int wait_flags(const Ctx& c, unsigned fl, int n, int need) {
  unsigned sp = 0;
  const unsigned vo = fl + FS * (unsigned)min(c.lane, n - 1);
  for (;;) {
    int v = (int)ld4<SC1>(c.ws, vo, 0);
    if (!__any(v < need)) {
      int got = need;
      if (!__any(v < need + 2)) got = need + 2;
      if (!__any(v < need + 6)) got = need + 6;
      if (!__any(v < need + 16)) got = need + 16;
      return got;
    }
    __builtin_amdgcn_s_sleep(SLEEP);
    spin_check(c, sp);
  }
}
template <int SLEEP>
__device__ __forceinline__ int wait_flags_blk(const Ctx& c, unsigned fl, int n, int need, LAS int* w) {
  if (c.wave == 0) { const int r = wait_flags<SLEEP>(c, fl, n, need); if (c.lane == 0) *w = r; }
  bar();
  const int r = __builtin_amdgcn_readfirstlane(*w);
  bar();
  return r;
}
__device__ __forceinline__ void publish(const Ctx& c, unsigned fl, int val) {
  wait_vm<0>();
  bar();
  if (c.tid == 0) st4<SC1>(c.ws, fl, 0, (unsigned)val);
}

template <int SLEEP>
__device__ __forceinline__ void gate(const Ctx& c, unsigned vo, unsigned so) {
  unsigned sp = 0;
  while (__any(ld4<SC1>(c.ws, vo, so) == SENT)) { __builtin_amdgcn_s_sleep(SLEEP); spin_check(c, sp); }
}
__device__ __forceinline__ u32x4 nosent(u32x4 v) { return u32x4{min(v.x, 0xFFFFFFFEu), min(v.y, 0xFFFFFFFEu), min(v.z, 0xFFFFFFFEu), min(v.w, 0xFFFFFFFEu)}; }
__device__ __forceinline__ u32x2 nosent(u32x2 v) { return u32x2{min(v.x, 0xFFFFFFFEu), min(v.y, 0xFFFFFFFEu)}; }

__device__ __forceinline__ unsigned side_bit(unsigned b, unsigned side) { return b | (((side ^ (b >> 21)) & 1u) << 13); }
__device__ __forceinline__ unsigned xr_phys(unsigned o, unsigned side) { return side_bit(OFF_XR + ((o >> 13) << 14) + (o & 8191u), side); }
__device__ __forceinline__ unsigned m_phys(unsigned s, unsigned j, unsigned side) { return side_bit(OFF_M + (s * 40u + j) * 16384u, side); }
__device__ __forceinline__ unsigned my_iod() { return (((unsigned)__builtin_amdgcn_s_getreg(20 | (3 << 11))) >> 2) & 1u; }
struct Sides { unsigned me; unsigned long long light; };
__device__ __forceinline__ void get_sides(const Ctx& c, Sides& o) {
  unsigned sp = 0, v;
  for (;;) {
    v = ld4<SC1>(c.ws, FL_H + FS * (unsigned)c.lane, 0);
    if (!__any(v == SENT)) break;
    __builtin_amdgcn_s_sleep(8);
    spin_check(c, sp);
  }
  const unsigned cc = __popcll(__ballot(v & 1u) >> 32) > 16 ? 1u : 0u;
  const unsigned long long iod = __ballot(v & 2u);
  o.me = cc ^ my_iod();
  o.light = cc ? ~iod : iod;
}
__device__ __forceinline__ unsigned pz_side(const Sides& sd, int j) { return (unsigned)(sd.light >> j) & 1u; }
__device__ __forceinline__ unsigned calibrate(const Ctx& c, int slot) {
  const unsigned base = OFF_CAL + (unsigned)slot * 256u;
  st16<SC1>(c.ws, base + (unsigned)(c.lane >> 3) * 8192u + (unsigned)(c.lane & 7) * 16u, 0, u32x4{0u, 0u, 0u, 0u});
  wait_vm<0>();
  unsigned best[2] = {~0u, ~0u};
#pragma unroll
  for (int i = 0; i < 8; ++i) {
    const unsigned long long t0 = __builtin_amdgcn_s_memtime();
    wait_lgkm();
    unsigned v = ld4<SC1>(c.ws, base, (unsigned)i * 8192u);
    asm volatile("s_waitcnt vmcnt(0)" : "+v"(v)::"memory");
    const unsigned long long t1 = __builtin_amdgcn_s_memtime();
    wait_lgkm();
    const unsigned par = (((OFF_CAL + i * 8192u) >> 13) ^ ((OFF_CAL + i * 8192u) >> 21)) & 1u;
    best[par] = min(best[par], (unsigned)(t1 - t0));
  }
  return best[1] < best[0] ? 1u : 0u;
}

template <int MODE> __device__ __forceinline__ int rowfn(int g, int j) {
  if (MODE == 0) return 8 * g + j;
  if (MODE == 1) return 16 * (g >> 1) + 8 * ((j >> 1) & 1) + 4 * (g & 1) + 2 * ((j >> 2) & 1) + (j & 1);
  if (MODE == 2) return 16 * (j >> 2) + 4 * g + (j & 3);
  return 64 * (j >> 2) + 4 * g + (j & 3);
}
template <int MODE> __device__ __forceinline__ unsigned stage_src(const Ctx& c, int ldw) {
  const int pos = c.lane >> 3, cp = c.lane & 7;
  const int u = (cp >> 1) ^ ((pos >> 1) & 3);
  const int ch = 2 * u + (cp & 1);
  const int g = 2 * (c.wave >> 1) + (pos >> 2), j = 4 * (c.wave & 1) + (pos & 3);
  return (unsigned)((rowfn<MODE>(g, j) * ldw + ch * 8) * 2);
}
__device__ __forceinline__ unsigned stage_rd(int lane, int u) {
  const int g = lane >> 4, q = (lane & 15) >> 2, p = lane & 3, pos = 4 * (g & 1) + q;
  return (unsigned)((g >> 1) * 2048 + pos * 128 + ((u ^ ((pos >> 1) & 3)) * 32) + p * 8);
}
__device__ __forceinline__ u32x4 stage_frag(LAS unsigned char* chunk_plus_rd) { return cat(tr8(chunk_plus_rd), tr8(chunk_plus_rd + 1024)); }

template <int NB, int NRES, int BATCH, int NBUF, class Issue, class Consume, class Pin>
__device__ __forceinline__ void stage_weights(const Ctx& c, Issue&& issue, Consume&& consume, Pin&& pin) {
  constexpr int SLOT = BATCH * 4096;
  bar();
  sfor<0, (NBUF < NB ? NBUF : NB)>([&](auto b) {
    sfor<0, BATCH>([&](auto i) { issue(std::integral_constant<int, b * BATCH + i>{}, c.lds + b * SLOT + i * 4096 + c.wave * 1024); });
  });
  sfor<0, NB - NRES>([&](auto b) {
    constexpr int inflight = ((b + NBUF < NB ? b + NBUF : NB) - (b + 1)) * BATCH;
    wait_vm<inflight>();
    bar();
    sfor<0, BATCH>([&](auto i) { consume(std::integral_constant<int, b * BATCH + i>{}, c.lds + (b % NBUF) * SLOT + i * 4096); });
    sb();
    sfor<0, BATCH>([&](auto i) { pin(std::integral_constant<int, b * BATCH + i>{}); });
    if constexpr (b + NBUF < NB) {
      bar();
      sfor<0, BATCH>([&](auto i) {
        issue(std::integral_constant<int, (b + NBUF) * BATCH + i>{}, c.lds + (b % NBUF) * SLOT + i * 4096 + c.wave * 1024);
      });
    }
  });
  wait_vm<0>();
  bar();
}

__device__ __forceinline__ float ln_stats(const Ctx& c, const u32x4 (&raw)[10], f32x2 (&cv)[40], LAS float* red) {
  const int tau = c.wave & 1, kh = c.wave >> 1, g = c.lane >> 4, rho = c.lane & 15, row = tau * 16 + rho;
  float s0 = 0.f, s1 = 0.f;
  unsigned ones_u = 0x3F803F80u;
  pin_v(ones_u);
#pragma unroll
  for (int i = 0; i < 10; ++i) {
    s0 = dot2(raw[i].x, ones_u, s0); s1 = dot2(raw[i].y, ones_u, s1);
    s0 = dot2(raw[i].z, ones_u, s0); s1 = dot2(raw[i].w, ones_u, s1);
  }
  float s = s0 + s1;
  s += __shfl_xor(s, 16); s += __shfl_xor(s, 32);
  if (g == 0) red[kh * 32 + row] = s;
  bar();
  const float mean = (red[row] + red[32 + row]) * (1.f / DM);
  f32x2 v2 = {0.f, 0.f};
#pragma unroll
  for (int i = 0; i < 10; ++i)
#pragma unroll
    for (int d = 0; d < 4; ++d) {
      const f32x2 t = f32x2{bf_lo(raw[i][d]), bf_hi(raw[i][d])} - mean;
      cv[4 * i + d] = t;
      v2 += t * t;
    }
  float v = v2[0] + v2[1];
  v += __shfl_xor(v, 16); v += __shfl_xor(v, 32);
  if (g == 0) red[64 + kh * 32 + row] = v;
  bar();
  return 1.f / sqrtf((red[64 + row] + red[96 + row]) * (1.f / DM) + 1e-5f);
}
template <class Store>
__device__ __forceinline__ void ln_apply(const Ctx& c, const f32x2 (&cv)[40], float rstd, LAS float* shiftv, LAS float* scalev, Store&& store) {
  const int tau = c.wave & 1, kh = c.wave >> 1, g = c.lane >> 4;
#pragma unroll
  for (int i = 0; i < 10; ++i) {
    const int kap = kh * 10 + i, d0 = (4 * kap + g) * 8;
    const f32x4 sc0 = *(LAS f32x4*)(scalev + d0), sc1 = *(LAS f32x4*)(scalev + d0 + 4);
    const f32x4 sh0 = *(LAS f32x4*)(shiftv + d0), sh1 = *(LAS f32x4*)(shiftv + d0 + 4);
    const f32x2 r0 = (cv[4 * i + 0] * rstd) * f32x2{sc0[0], sc0[1]} + f32x2{sh0[0], sh0[1]};
    const f32x2 r1 = (cv[4 * i + 1] * rstd) * f32x2{sc0[2], sc0[3]} + f32x2{sh0[2], sh0[3]};
    const f32x2 r2 = (cv[4 * i + 2] * rstd) * f32x2{sc1[0], sc1[1]} + f32x2{sh1[0], sh1[1]};
    const f32x2 r3 = (cv[4 * i + 3] * rstd) * f32x2{sc1[2], sc1[3]} + f32x2{sh1[2], sh1[3]};
    store(kap * 2 + tau, u32x4{pk(r0[0], r0[1]), pk(r1[0], r1[1]), pk(r2[0], r2[1]), pk(r3[0], r3[1])});
  }
}
__device__ __forceinline__ void load_mod(const Ctx& c, int f0, int nfl, LAS float* dst, bool add1) {
  for (int ch = c.tid; ch < ((nfl / 4 + 255) & ~255); ch += 256) {
    const int cc = min(ch, nfl / 4 - 1);
    unsigned sp = 0;
    u32x4 v;
    for (;;) {
      v = ld16<SC1>(c.ws, OFF_MOD + (unsigned)(f0 * 4 + cc * 16), 0);
      if (!__any(umax4(0u, v) == SENT)) break;
      __builtin_amdgcn_s_sleep(1);
      spin_check(c, sp);
    }
    f32x4 f = __builtin_bit_cast(f32x4, v);
    if (add1) { f[0] = rbf(1.f + f[0]); f[1] = rbf(1.f + f[1]); f[2] = rbf(1.f + f[2]); f[3] = rbf(1.f + f[3]); }
    *(LAS f32x4*)(dst + cc * 4) = f;
  }
}

__device__ __forceinline__ void role_pre(const Params& p, const Ctx& c, int li, int jpz) {
  const unsigned iod = my_iod();
  unsigned tabw = iod << 1;
  if (li >= 32 && c.wave == 0) tabw |= calibrate(c, li - 32) ^ iod;
  if (jpz >= 0) {
    const u32x4 sv = {SENT, SENT, SENT, SENT};
#pragma unroll
    for (int i = 0; i < 8; ++i) st16<SC1>(c.ws, (unsigned)c.tid * 16u, OFF_M + (unsigned)((i >> 2) * 40 + jpz) * 16384u + (unsigned)(i & 3) * 4096u, sv);
    if (c.tid < 128) st16<SC1>(c.ws, xr_phys(L_P + (unsigned)jpz * 1024u + (unsigned)(c.tid & 63) * 16u, c.tid >> 6), 0, sv);
    else if (c.tid < 192) st16<SC1>(c.ws, xr_phys(L_MEM + (unsigned)(1 + ((c.tid >> 4) & 1)) * MEM_SZ + (unsigned)jpz * 256u + (unsigned)(c.tid & 15) * 16u, (c.tid >> 5) & 1), 0, sv);
  }
  LAS float* sc = (LAS float*)c.lds;
  LAS float* red = (LAS float*)(c.lds + 4096);
  rsrc_t rx = mk_rsrc(p.x, 2048 * DM * 2);
  u32x4 raw[10];
  {
    const int tau = c.wave & 1, kh = c.wave >> 1, g = c.lane >> 4, rho = c.lane & 15;
    const unsigned vo = (unsigned)(((li * 32 + tau * 16 + rho) * DM + (40 * kh + g) * 8) * 2);
#pragma unroll
    for (int i = 0; i < 10; ++i) raw[i] = ld16<0>(rx, vo + i * 64, 0);
  }
  if (li < 60) {
    rsrc_t rw = mk_rsrc(p.wmod, DM * 3840 * 2);
    const int cg = c.tid & 7, rg = c.tid >> 3;
    u32x4 w[20];
#pragma unroll
    for (int i = 0; i < 20; ++i) w[i] = ld16<0>(rw, (unsigned)(((rg * 20 + i) * 3840 + li * 64 + cg * 8) * 2), 0);
    sb();
    const unsigned short* cp = (const unsigned short*)p.c;
    for (int i = c.tid; i < DM; i += 256) { float v = bf_lo(cp[i]); sc[i] = rbf(v / (1.f + __expf(-v))); }
    bar();
    float a[8] = {};
#pragma unroll
    for (int i = 0; i < 20; ++i) {
      const float s = sc[rg * 20 + i];
#pragma unroll
      for (int d = 0; d < 4; ++d) { a[2 * d] += s * bf_lo(w[i][d]); a[2 * d + 1] += s * bf_hi(w[i][d]); }
    }
    *(LAS f32x4*)(red + rg * 64 + cg * 8) = f32x4{a[0], a[1], a[2], a[3]};
    *(LAS f32x4*)(red + rg * 64 + cg * 8 + 4) = f32x4{a[4], a[5], a[6], a[7]};
    bar();
    if (c.tid < 64) {
      float s = 0.f;
#pragma unroll
      for (int r = 0; r < 32; ++r) s += red[r * 64 + c.tid];
      st4<SC1>(c.ws, OFF_MOD + (unsigned)(li * 64 + c.tid) * 4, 0, min(u_(rbf(s)), 0xFFFFFFFEu));
    }
  }
  LAS float* shiftv = (LAS float*)(c.lds + 16384);
  LAS float* scalev = (LAS float*)(c.lds + 20480);
  LAS float* red2 = (LAS float*)(c.lds + 24576);
  f32x2 cv[40];
  const float rstd = ln_stats(c, raw, cv, red2);
  load_mod(c, 0, DM, shiftv, false);
  load_mod(c, DM, DM, scalev, true);
  bar();
  ln_apply(c, cv, rstd, shiftv, scalev, [&](int fr, u32x4 o) { st16<SC1>(c.ws, (unsigned)c.lane * 16 + (unsigned)fr * 1024, OFF_HB + (unsigned)li * HB_SZ, o); });
  publish(c, FL_H + FS * li, (int)tabw);
  stamp(c, 1);
}

__device__ __forceinline__ void gather_mem(const Params& p, const Ctx& c, int s, bool perm, unsigned side, LAS unsigned char* A) {
  int r = c.lane & 7;
  if (perm) r = (r & 1) | ((r & 2) << 1) | ((r & 4) >> 1);
  const int t0 = 2 * c.wave, t2 = 8 + (c.wave & 1);
  u32x4 v0, v1, v2;
  if (s == 0) {
    rsrc_t rm = mk_rsrc(p.mem0, 8 * DM * 2);
    const unsigned vo = (unsigned)(r * DM * 2 + (c.lane >> 3) * 16);
    v0 = ld16<0>(rm, vo, t0 * 128); v1 = ld16<0>(rm, vo, t0 * 128 + 128); v2 = ld16<0>(rm, vo, t2 * 128);
  } else {
    const unsigned vo = (unsigned)(((c.lane & ~7) + r) * 16);
    const unsigned lo = L_MEM + (unsigned)s * MEM_SZ;
    const unsigned s0 = xr_phys(lo + t0 * 1024, side), s1 = xr_phys(lo + t0 * 1024 + 1024, side), s2 = xr_phys(lo + t2 * 1024, side);
    unsigned sp = 0;
    if (c.wave < 2) {
      for (;;) {
        v0 = ld16<SC1>(c.ws, vo, s0); v1 = ld16<SC1>(c.ws, vo, s1); v2 = ld16<SC1>(c.ws, vo, s2);
        if (!__any(umax4(umax4(umax4(0u, v0), v1), v2) == SENT)) break;
        spin_check(c, sp);
      }
    } else {
      for (;;) {
        v0 = ld16<SC1>(c.ws, vo, s0); v1 = ld16<SC1>(c.ws, vo, s1);
        if (!__any(umax4(umax4(0u, v0), v1) == SENT)) break;
        spin_check(c, sp);
      }
      v2 = v1;
    }
  }
  *(LAS u32x4*)(A + t0 * 1024 + c.lane * 16) = v0;
  *(LAS u32x4*)(A + t0 * 1024 + 1024 + c.lane * 16) = v1;
  if (c.wave < 2) *(LAS u32x4*)(A + t2 * 1024 + c.lane * 16) = v2;
  bar();
}
struct ResW { LAS unsigned char* lo; LAS unsigned char* hi; };
__device__ __forceinline__ ResW res_base(const Ctx& c) {
  ResW r; r.lo = c.lds + 81920 + stage_rd(c.lane, c.wave); r.hi = r.lo + 40960;
  pin_v(r.lo); pin_v(r.hi);
  return r;
}
__device__ __forceinline__ u32x8 res_frag(const ResW& w, int t) {
  LAS unsigned char* b = t < 5 ? w.lo + t * 8192 : w.hi + (t - 5) * 8192;
  return cat(stage_frag(b), stage_frag(b + 4096));
}
template <int CTRL> __device__ __forceinline__ unsigned dppu(unsigned x) { return (unsigned)__builtin_amdgcn_update_dpp(0, (int)x, CTRL, 0xf, 0xf, true); }
__device__ __forceinline__ float row16_max(float m) {
  asm volatile("s_nop 1\n\tv_max_f32_dpp %0, %0, %0 row_ror:8 row_mask:0xf bank_mask:0xf\n\t"
               "s_nop 1\n\tv_max_f32_dpp %0, %0, %0 row_ror:4 row_mask:0xf bank_mask:0xf\n\t"
               "s_nop 1\n\tv_max_f32_dpp %0, %0, %0 row_ror:2 row_mask:0xf bank_mask:0xf\n\t"
               "s_nop 1\n\tv_max_f32_dpp %0, %0, %0 row_ror:1 row_mask:0xf bank_mask:0xf" : "+v"(m));
  return m;
}
__device__ __forceinline__ float row16_sum(float m) {
  asm volatile("s_nop 1\n\tv_add_f32_dpp %0, %0, %0 row_ror:8 row_mask:0xf bank_mask:0xf\n\t"
               "s_nop 1\n\tv_add_f32_dpp %0, %0, %0 row_ror:4 row_mask:0xf bank_mask:0xf\n\t"
               "s_nop 1\n\tv_add_f32_dpp %0, %0, %0 row_ror:2 row_mask:0xf bank_mask:0xf\n\t"
               "s_nop 1\n\tv_add_f32_dpp %0, %0, %0 row_ror:1 row_mask:0xf bank_mask:0xf" : "+v"(m));
  return m;
}

__device__ __forceinline__ void role_H(const Params& p, const Ctx& c, int h) {
  u32x8 W[10][5];
  const unsigned kmvo = (c.lane & 8) ? (unsigned)((c.lane >> 4) * 64 + (c.lane & 7) * 8 + c.wave * 256) : 0x80000000u;
#pragma unroll
  for (int i = 0; i < 6; ++i) st8<SC1>(c.ws, kmvo, OFF_KM + (unsigned)((i / 3) * NH + h) * 3072 + (unsigned)(i % 3) * 1024, u32x2{SENT, SENT});
  {
    rsrc_t rq = mk_rsrc(p.Wq, DM * NH * DH * 2), rk = mk_rsrc(p.Wk, DM * NH * DH * 2);
    const unsigned src = stage_src<1>(c, NH * DH), rd = stage_rd(c.lane, c.wave);
    stage_weights<12, 2, 10, 4>(c,
      [&](auto ci, LAS unsigned char* dst) {
        constexpr int id = ci;
        if constexpr (id < 100) {
          constexpr int t = id / 10, pn = (id % 10) / 2, a = id % 2;
          constexpr int col = (pn < 3 ? pn : pn - 3) * 64;
          dma16<0>(pn < 3 ? rq : rk, dst, src, (unsigned)(((64 * t + 32 * a) * NH * DH + h * DH + col) * 2));
        } else {
          constexpr int sp = id - 100;
          dma16<0>(rk, dst, src, (unsigned)(((32 * sp) * NH * DH + h * DH + 128) * 2));
        }
      },
      [&](auto ci, LAS unsigned char* chunk) {
        constexpr int id = ci, t = id / 10, pn = (id % 10) / 2, a = id % 2;
        if constexpr (a == 1) {
          W[t][pn] = cat(stage_frag(chunk - 4096 + rd), stage_frag(chunk + rd));
        }
      },
      [&](auto ci) {
        constexpr int id = ci, t = id / 10, pn = (id % 10) / 2, a = id % 2;
        if constexpr (a == 1) { if constexpr (t * 5 + pn < 32) pin_a(W[t][pn]); else pin_v(W[t][pn]); }
      });
  }
  if (c.tid == 0) st4<SC1>(c.ws, FL_KM + FS * h, 0, 0u);
  Sides sd;
  get_sides(c, sd);
  LAS unsigned char* A = c.lds + c.lane * 16;
  const int g = c.lane >> 4, n = c.lane & 15;
  LAS unsigned char* TW = c.lds + 10240 + c.wave * 1536 + n * 32 + g * 8;
  LAS unsigned char* TR = c.lds + 10240 + c.wave * 1536 + c.lane * 8;
  LAS unsigned char* PART = c.lds + 16384;
  const ResW WR = res_base(c);
  const int idx = (c.lane & 8) ? 0xEEEE : 0x4444;
  const int q = (c.wave & 1) * 4 + g, it = min(n, 9);
  LAS unsigned char* PR = PART + (it >> 2) * 512 + q * 64 + (it < 8 ? (it & 3) : it - 6) * 16;
  const float lmask = n < 10 ? 1.f : 0.f;
  const unsigned plo = it < 8 ? (unsigned)(h * 512 + (it >> 1) * 128 + q * 16) : (unsigned)(16384 + h * 128 + q * 16);
  LAS unsigned char* KX = c.lds + 24576;
  int kready = -1;
  auto dma_kx = [&](int s) {
    if (kready < s) kready = wait_flags<2>(c, FL_K + FS * h, 1, s);
#pragma unroll
    for (int i = 0; i < 6; ++i)
      dma16<SC1>(c.ws, KX + (s & 1) * KX_SZ + (c.wave * 6 + i) * 1024, (unsigned)c.lane * 16, OFF_KX + (unsigned)(s * NH + h) * KX_SZ + (unsigned)(c.wave * 6 + i) * 1024);
  };
  stamp(c, 1);
  if (c.wave < 2) dma_kx(0);
  wait_vm<0>();
  for (int s = 0; s < NSEG; ++s) {
    unsigned pvo = xr_phys(L_P + (unsigned)s * P_SZ + plo, n & 1);
    if (n >= 10) pvo = 0x80000000u;
    f32x4 acc[6] = {};
    u32x8 wl[10];
    wl[0] = res_frag(WR, 0); wl[1] = res_frag(WR, 1);
#pragma unroll
    for (int i = 0; i < 6; ++i) pin_v(acc[i]);
    pin_v(wl[0]); pin_v(wl[1]); pin_v(pvo);
    gather_mem(p, c, s, false, sd.me, c.lds);
    stampf(c, 4 + 4 * s, 128);
    CYC(t0)
    u32x4 a[10], kx[3];
    a[0] = lds16(A); a[1] = lds16(A + 1024); a[2] = lds16(A + 2048);
    sb();
#pragma unroll
    for (int t = 0; t < 7; ++t) {
      LAS unsigned char* wb = t + 2 < 5 ? WR.lo + (t + 2) * 8192 : WR.hi + (t + 2 - 5) * 8192;
      acc[5] = smfma(a[t], wl[t], acc[5], idx); sb();
      a[t + 3] = lds16(A + (t + 3) * 1024); sb();
      acc[0] = smfma(a[t], W[t][0], acc[0], idx); sb();
      const u32x2 w0 = tr8(wb); sb();
      acc[1] = smfma(a[t], W[t][1], acc[1], idx); sb();
      const u32x2 w1 = tr8(wb + 1024); sb();
      acc[2] = smfma(a[t], W[t][2], acc[2], idx); sb();
      const u32x2 w2 = tr8(wb + 4096); sb();
      acc[3] = smfma(a[t], W[t][3], acc[3], idx); sb();
      const u32x2 w3 = tr8(wb + 5120); sb();
      acc[4] = smfma(a[t], W[t][4], acc[4], idx); sb();
      wl[t + 2] = cat(cat(w0, w1), cat(w2, w3));
    }
    CYC(t1)
    f32x4 sx0, sx1, sm;
    u32x2 F[3];
    float v[4];
#define H_S(i, t) acc[i] = smfma(a[t], W[t][i], acc[i], idx); sb();
#define H_L(t) acc[5] = smfma(a[t], wl[t], acc[5], idx); sb();
#define H_W(i, r) { auto r_ = __builtin_amdgcn_permlane32_swap(u_(acc[i][r]), u_(acc[3 + i][r]), false, false); acc[i][r] = f_(r_[0]); acc[3 + i][r] = f_(r_[1]); } sb();
#define H_A(i, r) asm("v_add_f32 %0, %1, %2" : "=v"(v[r]) : "v"(acc[i][r]), "v"(acc[3 + i][r])); sb();
#define H_K(i) kx[i] = lds16(KX + (s & 1) * KX_SZ + (4 * i + c.wave) * 1024 + c.lane * 16); sb();
#define H_P2(i) *(LAS u32x2*)(TW + i * 512) = u32x2{pk(v[0], v[1]), pk(v[2], v[3])}; F[i] = tr8(TR + i * 512); sb();
#define H_MZ(d, x, i) asm volatile("v_mfma_f32_16x16x16_bf16 %0, %1, %2, 0" : "=&v"(d) : "v"(x), "v"(F[i])); sb();
#define H_MA(d, x, i) asm volatile("v_mfma_f32_16x16x16_bf16 %0, %1, %2, %0" : "+v"(d) : "v"(x), "v"(F[i])); sb();
#define KLO(i) (u32x2{kx[i].x, kx[i].y})
#define KHI(i) (u32x2{kx[i].z, kx[i].w})
    {
      LAS unsigned char* wb = WR.hi + 4 * 8192;
      H_S(1, 7) const u32x2 w0 = tr8(wb); sb(); H_S(4, 7) const u32x2 w1 = tr8(wb + 1024); sb(); H_S(1, 8) const u32x2 w2 = tr8(wb + 4096); sb();
      H_S(4, 8) const u32x2 w3 = tr8(wb + 5120); sb(); H_S(1, 9) H_K(1) H_S(4, 9) H_K(2)
      wl[9] = cat(cat(w0, w1), cat(w2, w3));
    }
    H_L(7) H_K(0) H_S(2, 7) H_W(1, 0) H_W(1, 1) H_L(8) H_W(1, 2) H_W(1, 3) H_S(2, 8) H_A(1, 0) H_A(1, 1) H_L(9) H_A(1, 2) H_A(1, 3) H_S(2, 9) H_P2(1)
    H_S(0, 7) H_S(3, 7) H_W(2, 0) H_W(2, 1) H_S(0, 8) H_W(2, 2) H_W(2, 3) H_S(3, 8) H_A(2, 0) H_A(2, 1) H_S(0, 9) H_A(2, 2) H_A(2, 3) H_S(3, 9) H_P2(2)
    H_MZ(sx0, KLO(1), 1)
    stampf(c, 5 + 4 * s, 128);
    CYC(t2)
    H_W(0, 0) H_W(0, 1) H_W(0, 2) H_W(0, 3) H_MZ(sx1, KHI(1), 1) H_A(0, 0) H_A(0, 1) H_A(0, 2) H_A(0, 3) H_P2(0) H_MZ(sm, F[1], 1)
    H_MA(sx0, KLO(2), 2) H_MA(sx1, KHI(2), 2) H_MA(sm, F[2], 2)
    H_MA(sx0, KLO(0), 0) H_MA(sx1, KHI(0), 0) H_MA(sm, F[0], 0)
    asm volatile("s_nop 5" : "+v"(sx0), "+v"(sx1));
    CYC(t3)
    if (s < NSEG - 1) {
      if (n < 8) {
        LAS unsigned char* pp = PART + c.wave * 1536 + n * 64 + g * 16;
        *(LAS f32x4*)(pp) = sx0; sb(); *(LAS f32x4*)(pp + 512) = sx1; sb(); *(LAS f32x4*)(pp + 1024) = sm;
      }
      bar();
      CYC(t4)
      if (c.wave >= 2) {
        const f32x4 p0 = *(LAS f32x4*)PR, p1 = *(LAS f32x4*)(PR + 1536), p2 = *(LAS f32x4*)(PR + 3072), p3 = *(LAS f32x4*)(PR + 4608);
        const f32x4 x = (p0 + p1) + (p2 + p3);
        const float cs = 0.07216878364870322f * 1.4426950408889634f;
        const float m = row16_max(fmaxf(fmaxf(x[0], x[1]), fmaxf(x[2], x[3]))) * cs;
        float e[4];
#pragma unroll
        for (int r = 0; r < 4; ++r) e[r] = __builtin_amdgcn_exp2f(__builtin_fmaf(x[r], cs, -m));
        const float l = row16_sum(((e[0] + e[1]) + (e[2] + e[3])) * lmask);
        const float inv = __builtin_amdgcn_rcpf(l);
        const unsigned d0 = pks(e[0] * inv, e[1] * inv), d1 = pks(e[2] * inv, e[3] * inv);
        const u32x4 o = {dppu<0xA0>(d0), dppu<0xA0>(d1), dppu<0xF5>(d0), dppu<0xF5>(d1)};
        st16<SC1>(c.ws, pvo, 0, o);
        stampf(c, 6 + 4 * s, 128);
        CYC(t5)
        if (n < 10 && s + 2 < NSEG - 1) st16<SC1>(c.ws, xr_phys(L_P + (unsigned)(s + 2) * P_SZ + plo, n & 1), 0, u32x4{SENT, SENT, SENT, SENT});
      } else {
        dma_kx(s + 1);
      }
    }
#pragma unroll
    for (int i = 0; i < 3; ++i) st8<SC1>(c.ws, kmvo, OFF_KM + (unsigned)(s * NH + h) * 3072 + (unsigned)i * 1024, nosent(F[i]));
    if (s + 2 < NSEG) {
#pragma unroll
      for (int i = 0; i < 3; ++i) st8<SC1>(c.ws, kmvo, OFF_KM + (unsigned)((s + 2) * NH + h) * 3072 + (unsigned)i * 1024, u32x2{SENT, SENT});
    }
    __builtin_amdgcn_s_sleep(H_SLEEP);
  }
}

__device__ __forceinline__ void role_V(const Params& p, const Ctx& c, int h) {
  u32x8 Wv[10][2];
  u32x8 Wo[10][3];
  {
    rsrc_t rv = mk_rsrc(p.Wv, DM * NH * DH * 2), ro = mk_rsrc(p.Wo, DM * NH * DH * 2);
    const unsigned srcv = stage_src<1>(c, NH * DH), srco = stage_src<1>(c, DM), rd = stage_rd(c.lane, c.wave);
    stage_weights<12, 2, 10, 4>(c,
      [&](auto ci, LAS unsigned char* dst) {
        constexpr int id = ci;
        if constexpr (id < 40) {
          constexpr int t = id / 4, pn = (id % 4) / 2, a = id % 2;
          dma16<0>(rv, dst, srcv, (unsigned)(((64 * t + 32 * a) * NH * DH + h * DH + pn * 64) * 2));
        } else if constexpr (id < 100) {
          constexpr int f = (id - 40) / 2, a = id % 2, t = f / 10, pn = f % 10;
          dma16<0>(ro, dst, srco, (unsigned)(((h * DH + 64 * t + 32 * a) * DM + pn * 64) * 2));
        } else {
          constexpr int sp = id - 100;
          dma16<0>(rv, dst, srcv, (unsigned)(((32 * sp) * NH * DH + h * DH + 128) * 2));
        }
      },
      [&](auto ci, LAS unsigned char* chunk) {
        constexpr int id = ci;
        if constexpr (id % 2 == 1) {
          u32x8 f = cat(stage_frag(chunk - 4096 + rd), stage_frag(chunk + rd));
          if constexpr (id < 40) Wv[id / 4][(id % 4) / 2] = f;
          else Wo[((id - 40) / 2) % 10][((id - 40) / 2) / 10] = f;
        }
      },
      [&](auto ci) {
        constexpr int id = ci;
        if constexpr (id % 2 == 1) {
          if constexpr (id < 36) pin_v(Wv[id / 4][(id % 4) / 2]);
          else if constexpr (id < 40) pin_a(Wv[id / 4][(id % 4) / 2]);
          else pin_a(Wo[((id - 40) / 2) % 10][((id - 40) / 2) / 10]);
        }
      });
  }
  Sides sd;
  get_sides(c, sd);
  LAS unsigned char* A = c.lds + c.lane * 16;
  LAS unsigned char* TV = c.lds + 10240;
  const ResW WR = res_base(c);
  const int idx = (c.lane & 8) ? 0xEEEE : 0x4444;
  const int g = c.lane >> 4, n = c.lane & 15;
  unsigned mj[3], ms[3];
#pragma unroll
  for (int u = 0; u < 3; ++u) {
    const int pn = u < 2 ? 4 * u + ((g & 1) * 2 + (g >> 1)) : 8 + (g >> 1);
    mj[u] = (unsigned)(4 * pn + c.wave);
    ms[u] = pz_side(sd, (int)mj[u]);
  }
  stamp(c, 1);
  for (int s = 0; s < NSEG; ++s) {
    unsigned mvo[3];
#pragma unroll
    for (int u = 0; u < 3; ++u) {
      mvo[u] = m_phys((unsigned)s, mj[u], ms[u]) + (unsigned)(h * 256 + n * 16);
      if (u == 2 && (g & 1)) mvo[u] = 0x80000000u;
      pin_v(mvo[u]);
    }
    f32x4 acc[3] = {};
    u32x8 wl[10];
    wl[0] = res_frag(WR, 0); wl[1] = res_frag(WR, 1); wl[2] = res_frag(WR, 2);
#pragma unroll
    for (int i = 0; i < 3; ++i) { pin_v(acc[i]); pin_v(wl[i]); }
    gather_mem(p, c, s, true, sd.me, c.lds);
    stampf(c, 4 + 4 * s);
    u32x4 a[10];
#pragma unroll
    for (int t = 0; t < 4; ++t) a[t] = lds16(A + t * 1024);
    sb();
#pragma unroll
    for (int t = 0; t < 10; ++t) {
      acc[2] = smfma(a[t], wl[t], acc[2], idx);
      acc[0] = smfma(a[t], Wv[t][0], acc[0], idx);
      sb();
      if (t + 3 < 10) wl[t + 3] = res_frag(WR, t + 3);
      sb();
      acc[1] = smfma(a[t], Wv[t][1], acc[1], idx);
      sb();
      if (t + 4 < 10) a[t + 4] = lds16(A + (t + 4) * 1024);
      sb();
    }
    f32x4 m[10] = {};
    {
      const float a0 = halfsum(acc[2][0], acc[2][2]), a1 = halfsum(acc[2][1], acc[2][3]);
      *(LAS unsigned*)(TV + ((8 + c.wave) * 16 + n) * 16 + (g & 1) * 8 + (g >> 1) * 4) = pk(a0, a1);
      float v[4];
#pragma unroll
      for (int r = 0; r < 4; ++r) v[r] = halfsum(acc[0][r], acc[1][r]);
      *(LAS u32x2*)(TV + ((c.wave + 4 * (g >> 1)) * 16 + n) * 16 + (g & 1) * 8) = u32x2{pk(v[0], v[1]), pk(v[2], v[3])};
    }
    sb();
#pragma unroll
    for (int i = 0; i < 4; ++i) pin_v(m[i]);
    bar();
    u32x4 a2[3];
    {
      const int q = n >> 2, pp = n & 3;
      LAS unsigned char* tp = TV + (16 * g + 8 * (pp >> 1) + q) * 16 + (pp & 1) * 8;
#pragma unroll
      for (int t = 0; t < 3; ++t) a2[t] = cat(tr8(tp + t * 1024), tr8(tp + t * 1024 + 64));
    }
    sb();
#pragma unroll
    for (int i = 4; i < 10; ++i) pin_v(m[i]);
    sb();
    auto grp = [&](int u) {
#pragma unroll
      for (int t = 0; t < 3; ++t)
#pragma unroll
        for (int i = 4 * u; i < (u < 2 ? 4 * u + 4 : 10); ++i) m[i] = smfma(a2[t], Wo[i][t], m[i], idx);
    };
    auto post = [&](int u) {
      unsigned x[2], y[2];
#pragma unroll
      for (int d = 0; d < 2; ++d) {
        x[d] = pks(halfsum(m[4 * u][2 * d], m[4 * u + 1][2 * d]), halfsum(m[4 * u][2 * d + 1], m[4 * u + 1][2 * d + 1]));
        y[d] = u < 2 ? pks(halfsum(m[(4 * u + 2) % 10][2 * d], m[(4 * u + 3) % 10][2 * d]), halfsum(m[(4 * u + 2) % 10][2 * d + 1], m[(4 * u + 3) % 10][2 * d + 1])) : x[d];
      }
      auto r0 = __builtin_amdgcn_permlane16_swap(x[0], y[0], false, false);
      auto r1 = __builtin_amdgcn_permlane16_swap(x[1], y[1], false, false);
      st16<SC1>(c.ws, mvo[u], 0, u32x4{r0[0], r1[0], r0[1], r1[1]});
    };
    grp(0);
    sb();
    grp(1); post(0);
#pragma unroll
    for (int i = 0; i < 12; ++i) { __builtin_amdgcn_sched_group_barrier(0x8, 1, 0); __builtin_amdgcn_sched_group_barrier(0x2, 2, 0); }
    sb();
    grp(2); post(1);
#pragma unroll
    for (int i = 0; i < 6; ++i) { __builtin_amdgcn_sched_group_barrier(0x8, 1, 1); __builtin_amdgcn_sched_group_barrier(0x2, 3, 1); }
    sb();
    post(2);
    stampf(c, 6 + 4 * s);
    __builtin_amdgcn_s_sleep(V_SLEEP);
  }
}

__device__ __forceinline__ void role_PZ(const Params& p, const Ctx& c, int j) {
  Sides sd;
  get_sides(c, sd);
  const unsigned mside = pz_side(sd, j);
  LAS unsigned char* R = c.lds;
  const int idx = (c.lane & 8) ? 0xEEEE : 0x4444;
  const int g = c.lane >> 4, n = c.lane & 15;
  int uready = -1;
  const unsigned l16 = (unsigned)c.lane * 16;
  const unsigned pmvo = (unsigned)(2 * g * 128 + (c.lane & 7) * 16 + ((c.lane >> 3) & 1) * 8);
  const u32x4 sv = {SENT, SENT, SENT, SENT};
  LAS float* rw = (LAS float*)(R + c.wave * 512) + (4 * (g & 1) + 2 * (g >> 1)) * 16 + n;
  LAS unsigned char* rp = R + (((c.lane >> 2) & 7) * 16 + (c.lane >> 5) * 8 + (c.lane & 3) * 2) * 4;
  const unsigned olo = (unsigned)(j * 256 + (c.lane >> 2) * 16);
  bar();
  for (int s = 0; s < NSEG - 1; ++s) {
    if (uready < s) uready = wait_flags<2>(c, FL_U + 8 * FS * c.wave, 8, s);
    u32x4 u[8];
#pragma unroll
    for (int i = 0; i < 8; ++i) u[i] = ld16<SC1>(c.ws, l16, OFF_U + (unsigned)s * U_SZ + (unsigned)(j * 32 + 8 * c.wave + i) * 1024);
    u32x4 pa[4], mb[2]; u32x2 pm[2];
    const unsigned lp = L_P + (unsigned)s * P_SZ;
    const unsigned sop = xr_phys(lp + (unsigned)c.wave * 4096, sd.me), som5 = xr_phys(lp + 16384 + (unsigned)c.wave * 1024, sd.me);
    const unsigned som = m_phys((unsigned)s, (unsigned)j, mside) + (unsigned)c.wave * 2048;
    unsigned ovo = xr_phys(L_MEM + (unsigned)(s + 1) * MEM_SZ + olo, c.lane & 1);
    if (c.lane & 2) ovo = 0x80000000u;
    f32x4 acc = {};
    pin_v(ovo); pin_v(acc);
    unsigned sp = 0;
    for (;;) {
      mb[0] = ld16<SC1>(c.ws, l16, som); mb[1] = ld16<SC1>(c.ws, l16 + 1024, som);
      if (!__any(umax4(umax4(0u, mb[0]), mb[1]) == SENT)) break;
      spin_check(c, sp);
    }
    if (PZ_GAP) __builtin_amdgcn_s_sleep(PZ_GAP);
    for (;;) {
#pragma unroll
      for (int i = 0; i < 4; ++i) pa[i] = ld16<SC1>(c.ws, l16 + i * 1024, sop);
      pm[0] = ld8<SC1>(c.ws, pmvo, som5); pm[1] = ld8<SC1>(c.ws, pmvo + 128, som5);
      const unsigned m = umax4(umax4(umax4(umax4(umax4(0u, pa[0]), pa[1]), pa[2]), pa[3]), cat(pm[0], pm[1]));
      if (!__any(m == SENT)) break;
      spin_check(c, sp);
    }
    stampf(c, 4 + 4 * s);
#pragma unroll
    for (int i = 0; i < 4; ++i) acc = smfma(pa[i], cat(u[2 * i], u[2 * i + 1]), acc, idx);
    acc = smfma(cat(pm[0], pm[1]), cat(mb[0], mb[1]), acc, idx);
    const int rb = (s & 1) * 2048;
    rw[rb / 4] = halfsum(acc[0], acc[2]); rw[rb / 4 + 16] = halfsum(acc[1], acc[3]);
    bar();
    if (c.wave == 0) {
      const f32x2 v = (*(LAS f32x2*)(rp + rb) + *(LAS f32x2*)(rp + rb + 512)) + (*(LAS f32x2*)(rp + rb + 1024) + *(LAS f32x2*)(rp + rb + 1536));
      const unsigned d = pks(v[0], v[1]);
      st16<SC1>(c.ws, ovo, 0, u32x4{dppu<0x00>(d), dppu<0x55>(d), dppu<0xAA>(d), dppu<0xFF>(d)});
      stamp(c, 6 + 4 * s);
    }
    __builtin_amdgcn_s_sleep(PZ_SLEEP);
    if (c.wave == 0 && !(c.lane & 2) && s + 3 < NSEG) st16<SC1>(c.ws, xr_phys(L_MEM + (unsigned)(s + 3) * MEM_SZ + olo, c.lane & 1), 0, sv);
    if (s + 2 < NSEG) {
      const unsigned sa = m_phys((unsigned)(s + 2), (unsigned)j, mside) + (unsigned)c.wave * 2048;
      st16<SC1>(c.ws, l16, sa, sv); st16<SC1>(c.ws, l16 + 1024, sa, sv);
    }
  }
}

__device__ __forceinline__ void load_proj_w(const Ctx& c, const void* Wm, int h, u32x4 (&W)[3][20]) {
  rsrc_t rw = mk_rsrc(Wm, DM * NH * DH * 2);
  const unsigned src = stage_src<0>(c, NH * DH), rd = stage_rd(c.lane, c.wave);
  stage_weights<5, 0, 12, 3>(c,
    [&](auto ci, LAS unsigned char* dst) {
      constexpr int id = ci, kap = id / 3, pn = id % 3;
      dma16<0>(rw, dst, src, (unsigned)(((32 * kap) * NH * DH + h * DH + pn * 64) * 2));
    },
    [&](auto ci, LAS unsigned char* chunk) {
      constexpr int id = ci;
      W[id % 3][id / 3] = stage_frag(chunk + rd);
    },
    [&](auto ci) { constexpr int id = ci; pin_a(W[id % 3][id / 3]); });
}
__device__ __forceinline__ void dma_hb(const Ctx& c, int s, LAS unsigned char* slot) {
#pragma unroll
  for (int i = 0; i < 10; ++i)
    dma16<SC1>(c.ws, slot + (c.wave * 10 + i) * 1024, (unsigned)c.lane * 16, OFF_HB + (unsigned)s * HB_SZ + (unsigned)(c.wave * 10 + i) * 1024);
}
template <bool WA, bool WFIRST, bool ZERO>
__device__ __forceinline__ void mfma_v(f32x4& d, const u32x4& w, const u32x4& x) {
  if constexpr (ZERO) {
    if constexpr (WA && WFIRST) asm volatile("v_mfma_f32_16x16x32_bf16 %0, %1, %2, 0" : "=&v"(d) : "a"(w), "v"(x));
    else if constexpr (WA) asm volatile("v_mfma_f32_16x16x32_bf16 %0, %2, %1, 0" : "=&v"(d) : "a"(w), "v"(x));
    else if constexpr (WFIRST) asm volatile("v_mfma_f32_16x16x32_bf16 %0, %1, %2, 0" : "=&v"(d) : "v"(w), "v"(x));
    else asm volatile("v_mfma_f32_16x16x32_bf16 %0, %2, %1, 0" : "=&v"(d) : "v"(w), "v"(x));
  } else {
    if constexpr (WA && WFIRST) asm volatile("v_mfma_f32_16x16x32_bf16 %0, %1, %2, %0" : "+v"(d) : "a"(w), "v"(x));
    else if constexpr (WA) asm volatile("v_mfma_f32_16x16x32_bf16 %0, %2, %1, %0" : "+v"(d) : "a"(w), "v"(x));
    else if constexpr (WFIRST) asm volatile("v_mfma_f32_16x16x32_bf16 %0, %1, %2, %0" : "+v"(d) : "v"(w), "v"(x));
    else asm volatile("v_mfma_f32_16x16x32_bf16 %0, %2, %1, %0" : "+v"(d) : "v"(w), "v"(x));
  }
}
__device__ __forceinline__ void mfma_fence() { sb(); asm volatile("s_nop 7"); sb(); }

template <int NT, int K0, int K1, class Hook>
__device__ __forceinline__ void gemm_part(const u32x4 (&W)[NT][20], LAS unsigned char* hb, f32x4 (&acc)[NT][2], Hook&& hook) {
  u32x4 b[K1 - K0][2];
#pragma unroll
  for (int k = 0; k < 2; ++k) { b[k][0] = lds16(hb + (2 * (K0 + k)) * 1024); b[k][1] = lds16(hb + (2 * (K0 + k) + 1) * 1024); }
  sb();
  sfor<0, K1 - K0>([&](auto kc) {
    constexpr int k = kc, ka = K0 + k;
    sfor<0, 2 * NT>([&](auto ic) {
      constexpr int t = ic / NT, i = ic % NT;
      mfma_v<(ka * NT + i < 64), true, ka == 0>(acc[i][t], W[i][ka], b[k][t]);
      sb();
      if constexpr (ic < 2 && k + 2 < K1 - K0) { b[k + 2][ic] = lds16(hb + (2 * (ka + 2) + ic) * 1024); sb(); }
      if constexpr (ic == 2) { hook(kc); sb(); }
    });
  });
}
__device__ __forceinline__ void dma_hb1(const Ctx& c, int s, LAS unsigned char* slot, int i) {
  dma16<SC1>(c.ws, slot + (c.wave * 10 + i) * 1024, (unsigned)c.lane * 16, OFF_HB + (unsigned)s * HB_SZ + (unsigned)(c.wave * 10 + i) * 1024);
}
__device__ __forceinline__ u32x4 pack2(const f32x4& a, const f32x4& b) { return u32x4{pk(a[0], a[1]), pk(a[2], a[3]), pk(b[0], b[1]), pk(b[2], b[3])}; }

__device__ __forceinline__ void need_hb(const Ctx& c, unsigned long long& mask, int s) {
  if (c.wave != 0 || ((mask >> s) & 1ull)) return;
  unsigned sp = 0;
  for (;;) {
    mask = __ballot(ld4<SC1>(c.ws, FL_H + FS * (unsigned)c.lane, 0) != SENT);
    if ((mask >> s) & 1ull) return;
    __builtin_amdgcn_s_sleep(4);
    spin_check(c, sp);
  }
}

__device__ __forceinline__ void role_KV(const Params& p, const Ctx& c, int h, bool isV) {
  u32x4 W[3][20];
  load_proj_w(c, isV ? p.Wv : p.Wk, h, W);
  stamp(c, 1);
  unsigned long long hm = 0;
  need_hb(c, hm, 0);
  bar();
  stamp(c, 2);
  const unsigned obase = isV ? OFF_VX : OFF_KX, fl = (isV ? FL_V : FL_K) + FS * h;
  dma_hb(c, 0, c.lds);
  need_hb(c, hm, 1);
  bar();
  dma_hb(c, 1, c.lds + HB_SZ);
  auto kst = [&](int s, int i, const f32x4 (&a)[3][2]) {
    st16<SC1>(c.ws, (unsigned)c.lane * 16, obase + (unsigned)(s * NH + h) * KX_SZ + (unsigned)(4 * i + c.wave) * 1024, pack2(a[i][0], a[i][1]));
  };
  f32x4 accA[3][2], accB[3][2];
#pragma unroll 1
  for (int s = 0; s < 2; ++s) {
    need_hb(c, hm, s + 2);
    if (s == 0) wait_vm<10>();
    bar();
    gemm_part<3, 0, 20>(W, c.lds + s * HB_SZ + c.lane * 16, accA, [&](auto k) { if constexpr (k < 10) dma_hb1(c, s + 2, c.lds + (s == 0 ? 2 : 0) * HB_SZ, k); });
    mfma_fence();
#pragma unroll
    for (int i = 0; i < 3; ++i) kst(s, i, accA);
    publish(c, fl, s);
    stamp(c, 4 + s);
  }
  int sl = 2;
  auto seg = [&](int s, f32x4 (&cur)[3][2], const f32x4 (&prev)[3][2], bool hasprev) {
    const int s2 = min(s + 2, NSEG - 1);
    need_hb(c, hm, s2);
    wait_vm<10>();
    bar();
    if (c.tid == 0 && s >= 4) st4<SC1>(c.ws, fl, 0, (unsigned)(s - 2));
    const int sl2 = sl == 0 ? 2 : sl - 1;
    gemm_part<3, 0, 20>(W, c.lds + sl * HB_SZ + c.lane * 16, cur, [&](auto k) {
      if constexpr (k >= 2 && k < 5) { if (hasprev) kst(s - 1, k - 2, prev); }
      if constexpr (k >= 6 && k < 16) dma_hb1(c, s2, c.lds + sl2 * HB_SZ, k - 6);
    });
    stamp(c, 4 + s);
    sl = sl == 2 ? 0 : sl + 1;
  };
#pragma unroll 1
  for (int s = 2; s < NSEG; s += 2) { seg(s, accA, accB, s > 2); seg(s + 1, accB, accA, true); }
  mfma_fence();
#pragma unroll
  for (int i = 0; i < 3; ++i) kst(NSEG - 1, i, accB);
  publish(c, fl, NSEG - 1);
}

__device__ __forceinline__ void role_U(const Params& p, const Ctx& c, int h) {
  u32x4 W[10][6];
  {
    rsrc_t ro = mk_rsrc(p.Wo, DM * NH * DH * 2);
    const unsigned src = stage_src<2>(c, DM), rd = stage_rd(c.lane, c.wave);
    stage_weights<6, 0, 10, 4>(c,
      [&](auto ci, LAS unsigned char* dst) {
        constexpr int id = ci, a = id / 10, pn = id % 10;
        dma16<0>(ro, dst, src, (unsigned)(((h * DH + 32 * a) * DM + pn * 64) * 2));
      },
      [&](auto ci, LAS unsigned char* chunk) {
        constexpr int id = ci;
        W[id % 10][id / 10] = stage_frag(chunk + rd);
      },
      [&](auto ci) { constexpr int id = ci; pin_a(W[id % 10][id / 10]); });
  }
  const int g = c.lane >> 4, rp = c.lane & 15;
  const int gs = rp >> 2, i4 = rp & 3;
  const unsigned aoff = (unsigned)((g * 16 + 8 * (i4 >> 1) + 4 * (gs & 1) + (i4 & 1)) * 16 + (gs >> 1) * 8);
  LAS int* bw = (LAS int*)(c.lds + 3 * KX_SZ);
  int vready = -1, issued = -1;
  stamp(c, 1);
  auto dma_v = [&](int s) {
    LAS unsigned char* slot = c.lds + (s % 3) * KX_SZ;
#pragma unroll
    for (int i = 0; i < 3; ++i)
      dma16<SC1>(c.ws, slot + (c.wave * 3 + i) * 1024, (unsigned)c.lane * 16, OFF_VX + (unsigned)(s * NH + h) * KX_SZ + (unsigned)(c.wave * 3 + i) * 1024);
  };
  for (int s = 0; s < NSEG; ++s) {
    if (issued < s) {
      if (s >= 1) publish(c, FL_U + FS * h, s - 1);
      if (vready < s) { vready = wait_flags_blk<2>(c, FL_V + FS * h, 1, s, bw); }
      dma_v(s); issued = s;
      wait_vm<0>();
      bar();
    } else {
      wait_vm<10>();
      bar();
      if (c.tid == 0 && s >= 2) st4<SC1>(c.ws, FL_U + FS * h, 0, (unsigned)(s - 2));
    }
    if (s + 1 < NSEG && vready >= s + 1) { dma_v(s + 1); issued = s + 1; }
    LAS unsigned char* sa0 = c.lds + (s % 3) * KX_SZ + aoff;
    LAS unsigned char* sa1 = sa0 + 32;
    pin_v(sa0); pin_v(sa1);
    u32x4 a[2][6];
#pragma unroll
    for (int k = 0; k < 6; ++k) {
      a[0][k] = cat(*(LAS u32x2*)(sa0 + (2 * k) * 1024), *(LAS u32x2*)(sa0 + (2 * k + 1) * 1024));
      a[1][k] = cat(*(LAS u32x2*)(sa1 + (2 * k) * 1024), *(LAS u32x2*)(sa1 + (2 * k + 1) * 1024));
    }
    sb();
    f32x4 d[10][2];
    u32x4 o;
    sfor<0, 10>([&](auto pc) {
      constexpr int pn = pc;
      sfor<0, 12>([&](auto ic) {
        constexpr int k = ic / 2, tp = ic % 2;
        mfma_v<true, false, k == 0>(d[pn][tp], W[pn][k], a[tp][k]);
        sb();
        if constexpr (pn > 0 && ic >= 7 && ic < 11) { constexpr int q = ic - 7; o[q] = pk(d[pn - 1][q / 2][2 * (q % 2)], d[pn - 1][q / 2][2 * (q % 2) + 1]); sb(); }
        if constexpr (pn > 0 && ic == 11) {
          st16<SC1>(c.ws, (unsigned)c.lane * 16, OFF_U + (unsigned)s * U_SZ + (unsigned)((4 * (pn - 1) + c.wave) * 32 + h) * 1024, o);
          sb();
        }
      });
    });
    mfma_fence();
    st16<SC1>(c.ws, (unsigned)c.lane * 16, OFF_U + (unsigned)s * U_SZ + (unsigned)((36 + c.wave) * 32 + h) * 1024, pack2(d[9][0], d[9][1]));
    stamp(c, 4 + s);
  }
  publish(c, FL_U + FS * h, NSEG - 1);
}

__device__ __forceinline__ void role_Q(const Params& p, const Ctx& c, int h) {
  const int g = c.lane >> 4, n = c.lane & 15;
  const int qt = c.wave * 8 + (c.lane >> 3), tau = qt >> 4, rho = qt & 15, it0 = c.lane & 7, it = min(it0, 4);
  const int T = it >> 1;
  const bool dead = (it0 > 4) || (tau == 0 && it >= 2 && it < 4);
  const unsigned pvo = dead ? 0x80000000u : it < 4 ? (unsigned)((h * 2 + tau) * 1024 + (it * 16 + rho) * 16)
                                                   : (unsigned)(65536 + ((h >> 2) * 2 + tau) * 1024 + ((h & 3) * 16 + rho) * 16);
  const u32x4 sv = {SENT, SENT, SENT, SENT};
  st16<SC1>(c.ws, pvo, OFF_PX, sv); st16<SC1>(c.ws, pvo, OFF_PX + PX_SZ, sv);
  u32x4 W[3][20];
  load_proj_w(c, p.Wq, h, W);
  if (c.tid == 0) st4<SC1>(c.ws, FL_PX + FS * h, 0, 0u);
  LAS unsigned char* PART = c.lds + 2 * HB_SZ;
  const int tile = it == 4 ? (tau ? 4 : 1) : (tau ? 2 + T : 0);
  const int ga = it == 4 ? 0 : (it & 1), gb = it == 4 ? 1 : (it & 1) + 2;
  const int pao = tile * 1024 + rho * 64 + ga * 16, pbo = tile * 1024 + rho * 64 + gb * 16;
  bool ka[4], kb[4];
#pragma unroll
  for (int r = 0; r < 4; ++r) { ka[r] = dead || (it < 4 && 16 * T + 4 * ga + r > qt); kb[r] = dead || (it < 4 && 16 * T + 4 * gb + r > qt); }
  if (c.wave == 0) wait_flags<8>(c, FL_H, 64, 0);
  if (c.wave == 1) wait_flags<8>(c, FL_KM + FS * h, 1, 0);
  bar();
  dma_hb(c, 0, c.lds); dma_hb(c, 1, c.lds + HB_SZ);
  wait_vm<10>();
  bar();
  u32x4 q[3], kx[3];
  int kready = -1;
  auto load_kx = [&](int s, int i) {
    kx[i] = ld16<SC1>(c.ws, (unsigned)c.lane * 16, OFF_KX + (unsigned)(s * NH + h) * KX_SZ + (unsigned)(4 * i + c.wave) * 1024);
  };
  {
    f32x4 acc[3][2];
    gemm_part<3, 0, 20>(W, c.lds + c.lane * 16, acc, [](auto) {});
    mfma_fence();
#pragma unroll
    for (int i = 0; i < 3; ++i) q[i] = pack2(acc[i][0], acc[i][1]);
    kready = wait_flags<8>(c, FL_K + FS * h, 1, 0);
#pragma unroll
    for (int i = 0; i < 3; ++i) load_kx(0, i);
  }
  const unsigned kmvo = (unsigned)(g * 64 + (n & 7) * 8 + c.wave * 256);
#pragma unroll 1
  for (int s = 0; s < NSEG; ++s) {
    u32x2 km[3];
    unsigned sp = 0;
    for (;;) {
#pragma unroll
      for (int i = 0; i < 3; ++i) km[i] = ld8<SC1>(c.ws, kmvo, OFF_KM + (unsigned)(s * NH + h) * 3072 + (unsigned)i * 1024);
      if (!__any(umax3(umax3(km[0].x, km[0].y, km[1].x), umax3(km[1].y, km[2].x, km[2].y), 0u) == SENT)) break;
      __builtin_amdgcn_s_sleep(3);
      spin_check(c, sp);
    }
    wait_vm<0>();
    f32x4 sc[5] = {};
#pragma unroll
    for (int i = 0; i < 3; ++i) {
      const u32x2 q0 = {q[i].x, q[i].y}, q1 = {q[i].z, q[i].w}, k0 = {kx[i].x, kx[i].y}, k1 = {kx[i].z, kx[i].w};
      sc[0] = mfma16(k0, q0, sc[0]); sc[1] = mfma16(km[i], q0, sc[1]);
      sc[2] = mfma16(k0, q1, sc[2]); sc[3] = mfma16(k1, q1, sc[3]); sc[4] = mfma16(km[i], q1, sc[4]);
    }
    LAS unsigned char* PB = PART + (s & 1) * 20480;
#pragma unroll
    for (int t = 0; t < 5; ++t) *(LAS f32x4*)(PB + c.wave * 5120 + t * 1024 + n * 64 + g * 16) = sc[t];
    bar();
    {
      LAS unsigned char* pa = PB + pao;
      LAS unsigned char* pb = PB + pbo;
      f32x4 va = (*(LAS f32x4*)pa + *(LAS f32x4*)(pa + 5120)) + (*(LAS f32x4*)(pa + 10240) + *(LAS f32x4*)(pa + 15360));
      f32x4 vb = (*(LAS f32x4*)pb + *(LAS f32x4*)(pb + 5120)) + (*(LAS f32x4*)(pb + 10240) + *(LAS f32x4*)(pb + 15360));
      const float NEG = -3.0e38f;
#pragma unroll
      for (int r = 0; r < 4; ++r) { va[r] = ka[r] ? NEG : va[r]; vb[r] = kb[r] ? NEG : vb[r]; }
      float m = fmaxf(fmaxf(fmaxf(va[0], va[1]), fmaxf(va[2], va[3])), fmaxf(fmaxf(vb[0], vb[1]), fmaxf(vb[2], vb[3])));
      m = fmaxf(m, dpp<0xB1>(m)); m = fmaxf(m, dpp<0x4E>(m)); m = fmaxf(m, dpp<0x141>(m));
      const float cs = 0.07216878364870322f * 1.4426950408889634f;
      float l = 0.f;
#pragma unroll
      for (int r = 0; r < 4; ++r) {
        va[r] = ka[r] ? 0.f : __builtin_amdgcn_exp2f((va[r] - m) * cs);
        vb[r] = kb[r] ? 0.f : __builtin_amdgcn_exp2f((vb[r] - m) * cs);
        l += va[r] + vb[r];
      }
      l += dpp<0xB1>(l); l += dpp<0x4E>(l); l += dpp<0x141>(l);
      const float inv = __builtin_amdgcn_rcpf(l);
      st16<SC1>(c.ws, pvo, OFF_PX + (unsigned)s * PX_SZ,
                u32x4{pks(va[0] * inv, va[1] * inv), pks(vb[0] * inv, vb[1] * inv), pks(va[2] * inv, va[3] * inv), pks(vb[2] * inv, vb[3] * inv)});
    }
    stamp(c, 4 + s);
    if (s + 1 < NSEG) {
      if (s + 2 < NSEG) st16<SC1>(c.ws, pvo, OFF_PX + (unsigned)(s + 2) * PX_SZ, sv);
      if (kready < s + 1) kready = wait_flags<8>(c, FL_K + FS * h, 1, s + 1);
      const int s2 = min(s + 2, NSEG - 1);
      f32x4 acc[3][2];
      gemm_part<3, 0, 20>(W, c.lds + ((s + 1) & 1) * HB_SZ + c.lane * 16, acc, [&](auto k) {
        if constexpr (k < 10) dma_hb1(c, s2, c.lds + (s & 1) * HB_SZ, k);
        if constexpr (k >= 10 && k < 13) load_kx(s + 1, k - 10);
      });
      mfma_fence();
#pragma unroll
      for (int i = 0; i < 3; ++i) q[i] = pack2(acc[i][0], acc[i][1]);
    }
  }
}

constexpr int X1_ARM = 6;
__device__ __forceinline__ void role_PX(const Params& p, const Ctx& c, int jx) {
  LAS float* ga = (LAS float*)c.lds;
  LAS unsigned char* R = c.lds + 4096;
  const int g = c.lane >> 4, n = c.lane & 15;
  const unsigned l16 = (unsigned)c.lane * 16;
  const unsigned l16z = c.lane < 32 ? l16 : 0x80000000u;
  const int nt = c.wave & 1, tau = c.wave >> 1;
  const int col = 32 * jx + 16 * nt + 4 * g;
  const unsigned xo = (unsigned)(((tau * 16 + n) * DM + col) * 2);
  const unsigned x1o = (unsigned)((jx * 2 + tau) * 1024 + ((2 * nt + (g >> 1)) * 16 + n) * 16 + (g & 1) * 8);
#pragma unroll
  for (int i = 0; i < X1_ARM; ++i) st8<SC1>(c.ws, x1o, OFF_X1 + (unsigned)i * HB_SZ, u32x2{SENT, SENT});
  publish(c, FL_X1 + FS * jx, 0);
  Sides sd;
  get_sides(c, sd);
  load_mod(c, 2 * DM, DM, ga, false);
  wait_flags<8>(c, FL_PX + 8 * FS * c.wave, 8, 0);
  bar();
  const f32x4 gav = *(LAS f32x4*)(ga + col);
  rsrc_t rx = mk_rsrc(p.x, 2048 * DM * 2);
  const unsigned ms0 = pz_side(sd, 2 * jx), ms1 = pz_side(sd, 2 * jx + 1);
  const int gh = 8 * c.wave + ((c.lane >> 2) & 7), gw = c.lane & 3;
  const unsigned gvo = (unsigned)(65536 + ((gh >> 2) * 2 + (gw >> 1)) * 1024 + ((gh & 3) * 16 + (gw & 1) * 8) * 16);
  int uready = -1;
  auto ld_u = [&](int s, int i) -> u32x4 {
    return ld16<SC1>(c.ws, l16, OFF_U + (unsigned)s * U_SZ + (unsigned)((2 * jx + (i & 1)) * 32 + 8 * c.wave + (i >> 1)) * 1024);
  };
  auto step = [&](int s, const u32x4 (&z)[16], u32x4 (&zn)[16]) {
    const int sn = min(s + 1, NSEG - 1);
    if (uready < sn) uready = wait_flags<8>(c, FL_U + 8 * FS * c.wave, 8, sn);
    const unsigned spx = OFF_PX + (unsigned)s * PX_SZ;
    CYCW(u0)
    gate<4>(c, gvo, spx);
    CYCW(u1)
    const unsigned m0 = m_phys((unsigned)s, (unsigned)(2 * jx), ms0) + (unsigned)c.wave * 2048, m1 = m_phys((unsigned)s, (unsigned)(2 * jx + 1), ms1) + (unsigned)c.wave * 2048;
    u32x4 pa[10][2], zm[2][2];
    f32x4 acc[2][2];
    const u32x2 xv = ld8<0>(rx, xo, (unsigned)s * HB_SZ);
    unsigned sp = 0;
    for (;;) {
#pragma unroll
      for (int i = 0; i < 8; ++i)
#pragma unroll
        for (int t = 0; t < 2; ++t) pa[i][t] = ld16<SC1>(c.ws, t ? l16 : l16z, spx + (unsigned)((8 * c.wave + i) * 2 + t) * 1024);
#pragma unroll
      for (int i = 0; i < 2; ++i) {
#pragma unroll
        for (int t = 0; t < 2; ++t) pa[8 + i][t] = ld16<SC1>(c.ws, l16, spx + 65536 + (unsigned)((2 * c.wave + i) * 2 + t) * 1024);
        zm[i][0] = ld16<SC1>(c.ws, l16 + i * 1024, m0); zm[i][1] = ld16<SC1>(c.ws, l16 + i * 1024, m1);
      }
      sb();
      unsigned m = 0;
      sfor<0, 10>([&](auto kc) {
        constexpr int k = kc;
        sfor<0, 4>([&](auto ic) {
          constexpr int t = ic >> 1, u = ic & 1;
          if constexpr (k < 8) mfma_v<false, true, k == 0>(acc[t][u], z[2 * k + t], pa[k][u]);
          else mfma_v<false, true, false>(acc[t][u], zm[k - 8][t], pa[k][u]);
          sb();
          if constexpr (ic == 0) { m = umax4(m, pa[k][0]); if constexpr (k >= 8) m = umax4(m, zm[k - 8][0]); }
          if constexpr (ic == 2) { m = umax4(m, pa[k][1]); if constexpr (k >= 8) m = umax4(m, zm[k - 8][1]); }
          if constexpr ((ic & 1) && k >= 2) zn[2 * (k - 2) + (ic >> 1)] = ld_u(sn, 2 * (k - 2) + (ic >> 1));
          sb();
        });
      });
      if (!__any(m == SENT)) break;
      __builtin_amdgcn_s_sleep(2);
      spin_check(c, sp);
    }
    mfma_fence();
    CYCW(u2)
    LAS unsigned char* Rb = R + (s & 1) * 16384;
#pragma unroll
    for (int t = 0; t < 2; ++t)
#pragma unroll
      for (int u = 0; u < 2; ++u) *(LAS f32x4*)(Rb + c.wave * 4096 + (u * 2 + t) * 1024 + l16) = acc[t][u];
    bar();
    CYCW(u3)
    LAS unsigned char* rp = Rb + c.wave * 1024 + l16;
    const f32x4 v = (*(LAS f32x4*)rp + *(LAS f32x4*)(rp + 4096)) + (*(LAS f32x4*)(rp + 8192) + *(LAS f32x4*)(rp + 12288));
    const float o0 = bf_lo(xv.x) + rbf(gav[0] * rbf(v[0])), o1 = bf_hi(xv.x) + rbf(gav[1] * rbf(v[1]));
    const float o2 = bf_lo(xv.y) + rbf(gav[2] * rbf(v[2])), o3 = bf_hi(xv.y) + rbf(gav[3] * rbf(v[3]));
    st8<SC1>(c.ws, x1o, OFF_X1 + (unsigned)s * HB_SZ, u32x2{pks(o0, o1), pks(o2, o3)});
    CYCW(u4)
    CYCOUT(s, u0, u1, u2, u3, u4, u4, u4)
    stamp(c, 4 + s);
    if (s + X1_ARM < NSEG) st8<SC1>(c.ws, x1o, OFF_X1 + (unsigned)(s + X1_ARM) * HB_SZ, u32x2{SENT, SENT});
  };
  u32x4 za[16], zb[16];
  uready = wait_flags<8>(c, FL_U + 8 * FS * c.wave, 8, 0);
#pragma unroll
  for (int i = 0; i < 16; ++i) za[i] = ld_u(0, i);
#pragma unroll 1
  for (int s = 0; s < NSEG; s += 2) { step(s, za, zb); step(s + 1, zb, za); }
}

__device__ __forceinline__ void role_F1(const Params& p, const Ctx& c, int rep, int f1) {
  const u32x4 sv = {SENT, SENT, SENT, SENT};
  const unsigned avo = (unsigned)(c.lane * 16 + (f1 * 4 + c.wave) * 2048);
#pragma unroll
  for (int i = 0; i < 4; ++i) st16<SC1>(c.ws, avo + (i & 1) * 1024, OFF_ACT + (unsigned)(rep + 4 * (i >> 1)) * ACT_SZ, sv);
  u32x4 W[4][20];
  {
    rsrc_t rg = mk_rsrc(p.wg, DM * DFF * 2), ru = mk_rsrc(p.wu, DM * DFF * 2);
    const unsigned src = stage_src<0>(c, DFF), rd = stage_rd(c.lane, c.wave);
    stage_weights<10, 0, 8, 5>(c,
      [&](auto ci, LAS unsigned char* dst) {
        constexpr int id = ci, kap = id / 4, pn = id % 4;
        dma16<0>(pn < 2 ? rg : ru, dst, src, (unsigned)(((32 * kap) * DFF + f1 * 128 + (pn & 1) * 64) * 2));
      },
      [&](auto ci, LAS unsigned char* chunk) {
        constexpr int id = ci;
        W[id % 4][id / 4] = stage_frag(chunk + rd);
      },
      [&](auto ci) { constexpr int id = ci; if constexpr (id < 64) pin_a(W[id % 4][id / 4]); else pin_v(W[id % 4][id / 4]); });
  }
  if (c.tid == 0) st4<SC1>(c.ws, FL_ACT + FS * (16 * rep + f1), 0, 0u);
  LAS unsigned char* HB = c.lds;
  LAS float* shiftv = (LAS float*)(c.lds + HB_SZ);
  LAS float* scalev = (LAS float*)(c.lds + HB_SZ + 4096);
  LAS float* red = (LAS float*)(c.lds + HB_SZ + 8192);
  load_mod(c, 3 * DM, DM, shiftv, false);
  load_mod(c, 4 * DM, DM, scalev, true);
  wait_flags<8>(c, FL_X1, 20, 0);
  bar();
  stamp(c, 100);
  const int tau = c.wave & 1, kh = c.wave >> 1;
  const unsigned vo = (unsigned)((20 * kh + tau) * 1024 + c.lane * 16);
  const unsigned gvo = (unsigned)((20 * kh + tau) * 1024 + (min(c.lane, 19) >> 1) * 2048 + (c.lane & 1) * 512);
#pragma unroll 1
  for (int s = rep; s < NSEG; s += 4) {
    {
      const unsigned sx = OFF_X1 + (unsigned)s * HB_SZ;
      CYCW(u0)
      gate<6>(c, gvo, sx);
      CYCW(u1)
      u32x4 raw[10];
      unsigned sp = 0;
      for (;;) {
        unsigned m = 0;
#pragma unroll
        for (int i = 0; i < 10; ++i) raw[i] = ld16<SC1>(c.ws, vo, sx + i * 2048);
        sb();
#pragma unroll
        for (int i = 0; i < 10; ++i) m = umax4(m, raw[i]);
        if (!__any(m == SENT)) break;
        __builtin_amdgcn_s_sleep(2);
        spin_check(c, sp);
      }
      CYCW(u2)
      f32x2 cv[40];
      const float rstd = ln_stats(c, raw, cv, red);
      CYCW(u3)
      ln_apply(c, cv, rstd, shiftv, scalev, [&](int fr, u32x4 o) { *(LAS u32x4*)(HB + fr * 1024 + c.lane * 16) = o; });
    bar();
    CYCW(u4)
    f32x4 acc[4][2];
    gemm_part<4, 0, 20>(W, HB + c.lane * 16, acc, [](auto) {});
    mfma_fence();
    CYCW(u5)
#pragma unroll
    for (int t = 0; t < 2; ++t) {
      float a[8];
#pragma unroll
      for (int i = 0; i < 2; ++i)
#pragma unroll
        for (int r = 0; r < 4; ++r) {
          const float gv = acc[i][t][r], uv = acc[2 + i][t][r];
          const float z2 = gv * __builtin_fmaf(gv * gv, -2.f * 1.4426950408889634f * 0.7978845608028654f * 0.044715f, -2.f * 1.4426950408889634f * 0.7978845608028654f);
          a[4 * i + r] = gv * uv * __builtin_amdgcn_rcpf(1.f + __builtin_amdgcn_exp2f(z2));
        }
      st16<SC1>(c.ws, avo + t * 1024, OFF_ACT + (unsigned)s * ACT_SZ, u32x4{pks(a[0], a[1]), pks(a[2], a[3]), pks(a[4], a[5]), pks(a[6], a[7])});
    }
    CYCW(u6)
    CYCOUT(s, u0, u1, u2, u3, u4, u5, u6)
    }
    stamp(c, 104 + s);
    if (s + 8 < NSEG) { st16<SC1>(c.ws, avo, OFF_ACT + (unsigned)(s + 8) * ACT_SZ, sv); st16<SC1>(c.ws, avo + 1024, OFF_ACT + (unsigned)(s + 8) * ACT_SZ, sv); }
  }
}

__device__ __forceinline__ void role_F2(const Params& p, const Ctx& c, int rep, int f2) {
  u32x4 W[12][4];
  {
    rsrc_t rd_ = mk_rsrc(p.wd, DFF * DM * 2);
    const unsigned src = stage_src<3>(c, DM);
    unsigned rd[4];
#pragma unroll
    for (int u = 0; u < 4; ++u) rd[u] = stage_rd(c.lane, u) + (unsigned)c.wave * 4096;
    stage_weights<4, 0, 12, 3>(c,
      [&](auto ci, LAS unsigned char* dst) {
        constexpr int id = ci;
        dma16<0>(rd_, dst, src, (unsigned)(((128 * (id / 4) + 16 * (id % 4)) * DM + f2 * 64) * 2));
      },
      [&](auto ci, LAS unsigned char* chunk) {
        constexpr int id = ci;
        if constexpr (id % 4 == 0) {
#pragma unroll
          for (int u = 0; u < 4; ++u) W[id / 4][u] = stage_frag(chunk + rd[u]);
        }
      },
      [&](auto ci) { constexpr int id = ci; pin_a(W[id / 4][id % 4]); });
  }
  LAS float* gb = (LAS float*)c.lds;
  LAS unsigned char* R = c.lds + 4096;
  load_mod(c, 5 * DM, DM, gb, false);
  wait_flags<8>(c, FL_ACT + FS * 16 * rep, 12, 0);
  bar();
  const int g = c.lane >> 4, n = c.lane & 15;
  const unsigned l16 = (unsigned)c.lane * 16;
  const int col = 64 * f2 + 16 * c.wave + 4 * g;
  const f32x4 gbv = *(LAS f32x4*)(gb + col);
  rsrc_t ro = mk_rsrc(p.out, 2048 * DM * 2);
  const unsigned gvo = (unsigned)((4 * min(c.lane, 11) + c.wave) * 2048 + 1024);
  const unsigned xo = (unsigned)((n * DM + col) * 2);
  const unsigned x1o = (unsigned)((col >> 5) * 2048 + (((col >> 3) & 3) * 16 + n) * 16 + (g & 1) * 8);
  stamp(c, 100);
  int par = 0;
#pragma unroll 1
  for (int s = rep; s < NSEG; s += 4) {
    const unsigned sa = OFF_ACT + (unsigned)s * ACT_SZ;
    gate<6>(c, gvo, sa);
    u32x4 b[12][2];
    u32x2 xv[2];
    f32x4 acc[4][2];
    unsigned sp = 0;
    for (;;) {
#pragma unroll
      for (int i = 0; i < 12; ++i)
#pragma unroll
        for (int t = 0; t < 2; ++t) b[i][t] = ld16<SC1>(c.ws, l16, sa + (unsigned)((4 * i + c.wave) * 2 + t) * 1024);
#pragma unroll
      for (int t = 0; t < 2; ++t) xv[t] = ld8<SC1>(c.ws, x1o + t * 1024, OFF_X1 + (unsigned)s * HB_SZ);
      sb();
      unsigned m = 0;
#pragma unroll
      for (int u = 0; u < 4; ++u) { acc[u][0] = f32x4{0.f, 0.f, 0.f, 0.f}; acc[u][1] = f32x4{0.f, 0.f, 0.f, 0.f}; }
#pragma unroll
      for (int i = 0; i < 12; ++i) {
        m = umax4(umax4(m, b[i][0]), b[i][1]);
#pragma unroll
        for (int u = 0; u < 4; ++u) { acc[u][0] = mfma32(W[i][u], b[i][0], acc[u][0]); acc[u][1] = mfma32(W[i][u], b[i][1], acc[u][1]); }
      }
      if (!__any(m == SENT)) break;
      __builtin_amdgcn_s_sleep(2);
      spin_check(c, sp);
    }
    LAS unsigned char* Rb = R + par * 32768;
    par ^= 1;
#pragma unroll
    for (int u = 0; u < 4; ++u)
#pragma unroll
      for (int t = 0; t < 2; ++t) *(LAS f32x4*)(Rb + c.wave * 8192 + (u * 2 + t) * 1024 + l16) = acc[u][t];
    bar();
#pragma unroll
    for (int t = 0; t < 2; ++t) {
      LAS unsigned char* rp = Rb + (c.wave * 2 + t) * 1024 + l16;
      const f32x4 v = (*(LAS f32x4*)rp + *(LAS f32x4*)(rp + 8192)) + (*(LAS f32x4*)(rp + 16384) + *(LAS f32x4*)(rp + 24576));
      const float o0 = bf_lo(xv[t].x) + rbf(gbv[0] * rbf(v[0])), o1 = bf_hi(xv[t].x) + rbf(gbv[1] * rbf(v[1]));
      const float o2 = bf_lo(xv[t].y) + rbf(gbv[2] * rbf(v[2])), o3 = bf_hi(xv[t].y) + rbf(gbv[3] * rbf(v[3]));
      st8<0>(ro, xo + t * 16 * DM * 2, (unsigned)s * HB_SZ, u32x2{pk(o0, o1), pk(o2, o3)});
    }
    stamp(c, 104 + s);
  }
}

enum { M_PRE = 1, M_KV = 2, M_U = 4, M_REC = 8, M_Q = 16, M_PX = 32, M_F1 = 64, M_F2 = 128, M_ALL = 255 };

__global__ void __launch_bounds__(256, 1) mega_kernel(Params p) {
  extern __shared__ __attribute__((aligned(16))) unsigned char smem[];
  Ctx c;
  c.ws = mk_rsrc(p.ws, WS_BYTES);
  c.lds = (LAS unsigned char*)smem;
  c.tid = threadIdx.x; c.lane = c.tid & 63; c.wave = __builtin_amdgcn_readfirstlane(c.tid >> 6);
  const int grp = blockIdx.x & 7, ix = blockIdx.x >> 3;
  stamp(c, 0);
  int role, id;
  if (grp < 4) {
    if (ix < 16) { role = ix >> 3; id = grp * 8 + (ix & 7); }
    else if (ix < 26) { role = 6; id = grp * 10 + ix - 16; }
    else if (ix < 31) { role = 6; id = 40 + grp * 5 + ix - 26; }
    else { role = 2; id = 28 + grp; }
  } else {
    const int g4 = grp - 4;
    if (ix < 24) { role = 3 + (ix >> 3); id = g4 * 8 + (ix & 7); }
    else if (ix < 31) { role = 2; id = g4 * 7 + ix - 24; }
    else { role = 6; id = 60 + g4; }
  }
  if (role == 0) { if (p.mask & M_REC) role_H(p, c, id); }
  else if (role == 1) { if (p.mask & M_REC) role_V(p, c, id); }
  else if (role == 2) { if (p.mask & M_Q) role_Q(p, c, id); }
  else if (role < 6) {
    if (role == 5) { if (p.mask & M_U) role_U(p, c, id); }
    else { if (p.mask & M_KV) role_KV(p, c, id, role == 4); }
    if (ix < 12) { if (p.mask & M_F1) role_F1(p, c, grp - 4, ix); }
    else if (ix < 22) { if (p.mask & M_F2) role_F2(p, c, grp - 4, ix - 12); }
  } else {
    if (p.mask & M_PRE) role_pre(p, c, id, id < 40 ? id : -1);
    if (id < 40) { if (p.mask & M_REC) role_PZ(p, c, id); }
    else if (id < 60) { if (p.mask & M_PX) role_PX(p, c, id - 40); }
  }
}

}

#define SPLIT_LAUNCH 0

extern "C" void kernel_launch(void* const* d_in, const int* in_sizes, int n_in, void* d_out, int out_size, void* d_ws,
                              size_t ws_size, hipStream_t stream) {
  Params p;
  p.x = d_in[0]; p.mem0 = d_in[1]; p.Wq = d_in[2]; p.Wk = d_in[3]; p.Wv = d_in[4]; p.Wo = d_in[5];
  p.wg = d_in[8]; p.wu = d_in[9]; p.wd = d_in[10]; p.c = d_in[11]; p.wmod = d_in[12];
  void* wsa = (void*)(((uintptr_t)d_ws + ((2u << 20) - 1)) & ~(uintptr_t)((2u << 20) - 1));
  p.out = d_out; p.ws = wsa; p.mask = M_ALL; p.pad = 0;
  (void)hipFuncSetAttribute((const void*)mega_kernel, hipFuncAttributeMaxDynamicSharedMemorySize, LDS_BYTES);
  (void)hipMemsetAsync(wsa, 0xFF, INIT_BYTES, stream);
  hipLaunchKernelGGL(mega_kernel, dim3(256), dim3(256), LDS_BYTES, stream, p);
}
```

```cpp
#include <hip/hip_runtime.h>
#include <hip/hip_bf16.h>
#include <type_traits>
#include <cstdint>

#define LAS __attribute__((address_space(3)))
typedef unsigned u32x2 __attribute__((ext_vector_type(2)));
typedef unsigned u32x4 __attribute__((ext_vector_type(4)));
typedef unsigned u32x8 __attribute__((ext_vector_type(8)));
typedef float f32x2 __attribute__((ext_vector_type(2)));
typedef float f32x4 __attribute__((ext_vector_type(4)));
typedef short s16x4 __attribute__((ext_vector_type(4)));
typedef __bf16 bf16x2 __attribute__((ext_vector_type(2)));
typedef __bf16 bf16x8 __attribute__((ext_vector_type(8)));
typedef __bf16 bf16x16 __attribute__((ext_vector_type(16)));
typedef __amdgpu_buffer_rsrc_t rsrc_t;

namespace {

constexpr int DM = 640, NH = 32, DH = 192, NSEG = 64, DFF = 1536;
constexpr unsigned SENT = 0xFFFFFFFFu;

constexpr unsigned FS = 128;
constexpr unsigned FL_ABORT = 0;
constexpr unsigned OFF_MOD = 1024;
constexpr unsigned OFF_MODP = 57344;
constexpr unsigned FL_H = 16384;
constexpr unsigned FL_KM = 24576, FL_K = 28672, FL_V = 32768, FL_U = 36864, FL_PX = 40960;
constexpr unsigned FL_X1 = 45056;
constexpr unsigned FL_ACT = 49152;
constexpr unsigned OFF_DB = 81920;
constexpr unsigned INIT_BYTES = OFF_DB + 8192 + 1024;
constexpr unsigned OFF_XR = 98304;
constexpr unsigned MEM_SZ = 10240, P_SZ = 20480;
constexpr unsigned L_MEM = 0, L_P = 64 * MEM_SZ, L_END = L_P + 64 * P_SZ;
constexpr unsigned OFF_CAL = OFF_XR + 2 * L_END;
constexpr unsigned OFF_M = OFF_CAL + 65536;
constexpr unsigned M_SZ = 40 * 16384;
constexpr unsigned OFF_HB = OFF_M + 64 * M_SZ;
constexpr unsigned HB_SZ = 40960;
constexpr unsigned OFF_KX = OFF_HB + 64 * HB_SZ;
constexpr unsigned KX_SZ = 12288;
constexpr unsigned OFF_VX = OFF_KX + 64 * 32 * KX_SZ;
constexpr unsigned OFF_U = OFF_VX + 64 * 32 * KX_SZ;
constexpr unsigned U_SZ = 40 * 32 * 1024;
constexpr unsigned OFF_KM = OFF_U + 64 * U_SZ;
constexpr unsigned OFF_PX = OFF_KM + 64 * 32 * 3072;
constexpr unsigned PX_SZ = 81920;
constexpr unsigned OFF_X1 = OFF_PX + 64 * PX_SZ;
constexpr unsigned OFF_ACT = OFF_X1 + 2048 * 640 * 2;
constexpr unsigned ACT_SZ = 48 * 2 * 1024;
constexpr unsigned OFF_DBG = OFF_ACT + 64 * ACT_SZ;
constexpr unsigned WS_BYTES = OFF_DBG + (1u << 20);
static_assert(WS_BYTES + (2u << 20) <= (256u << 20), "workspace");
static_assert(OFF_XR % 16384 == 0 && OFF_CAL % 16384 == 0 && OFF_M % 16384 == 0, "alignment");

constexpr int LDS_BYTES = 160 * 1024;
constexpr unsigned SPIN_CAP = 1u << 21;
#define PZ_SLEEP 14
#define PZ_GAPD 2
#define PZ_GAP 0
#define UST_AUX SC1
#define UST_VO ((unsigned)c.lane * 16)
#define H_SLEEP 12
#define MAP 1
#define REV 0
#define V_NW 4
#define V_NA 3
#define HV_DELAY 0
#define Q_DELAY 0
#define PZ_MODE 0
#define PZ_THR 16
#define PXT 0
#define PX_SLEEP 4
#define PX_NF 1
#define PX_DELAY 12
#define USE_BELL_P 1
#define USE_BELL_M 1
#define BELL_P_POS 5
#define BELL_P_GAP 3
#define BELL_M_POS 2
#define BELL_M_GAP 0
#define BELL_P_GAP0 0
#define H_D0 0
#define H_D1 8
#define H_D2 0
#define F1_DELAY 4
#define F2_DELAY 2
#define KM_BELL 0
#define KM_DELAY 0
#define TLBWARM 0
#define FFN_PREFETCH 1
#define Q_PREFETCH 0
#define U_EAGER 6
#define V_SLEEP 16

struct Params {
  const void* x; const void* mem0; const void* Wq; const void* Wk; const void* Wv; const void* Wo;
  const void* wg; const void* wu; const void* wd; const void* c; const void* wmod;
  void* out; void* ws;
  unsigned mask; unsigned pad;
};

template <int I, int N, class F>
__device__ __forceinline__ void sfor(F&& f) {
  if constexpr (I < N) { f(std::integral_constant<int, I>{}); sfor<I + 1, N>(f); }
}
__device__ __forceinline__ rsrc_t mk_rsrc(const void* p, unsigned bytes) {
  return __builtin_amdgcn_make_buffer_rsrc(const_cast<void*>(p), 0, bytes, 0x00020000);
}
template <int AUX> __device__ __forceinline__ u32x4 ld16(rsrc_t r, unsigned vo, unsigned so) { return __builtin_amdgcn_raw_buffer_load_b128(r, vo, so, AUX); }
template <int AUX> __device__ __forceinline__ u32x2 ld8(rsrc_t r, unsigned vo, unsigned so) { return __builtin_amdgcn_raw_buffer_load_b64(r, vo, so, AUX); }
template <int AUX> __device__ __forceinline__ unsigned ld4(rsrc_t r, unsigned vo, unsigned so) { return __builtin_amdgcn_raw_buffer_load_b32(r, vo, so, AUX); }
template <int AUX> __device__ __forceinline__ void st16(rsrc_t r, unsigned vo, unsigned so, u32x4 v) { __builtin_amdgcn_raw_buffer_store_b128(v, r, vo, so, AUX); }
template <int AUX> __device__ __forceinline__ void st8(rsrc_t r, unsigned vo, unsigned so, u32x2 v) { __builtin_amdgcn_raw_buffer_store_b64(v, r, vo, so, AUX); }
template <int AUX> __device__ __forceinline__ void st4(rsrc_t r, unsigned vo, unsigned so, unsigned v) { __builtin_amdgcn_raw_buffer_store_b32(v, r, vo, so, AUX); }
constexpr int SC1 = 16;
template <int AUX> __device__ __forceinline__ void dma16(rsrc_t r, LAS unsigned char* dst, unsigned vo, unsigned so) {
  __builtin_amdgcn_raw_ptr_buffer_load_lds(r, (LAS void*)dst, 16, vo, so, 0, AUX);
}
template <int N> __device__ __forceinline__ void wait_vm() { asm volatile("s_waitcnt vmcnt(%0)" ::"n"(N) : "memory"); }
__device__ __forceinline__ void wait_lgkm() { asm volatile("s_waitcnt lgkmcnt(0)" ::: "memory"); }
__device__ __forceinline__ void bar() { asm volatile("s_waitcnt lgkmcnt(0)\n\ts_barrier" ::: "memory"); }

__device__ __forceinline__ unsigned pk(float a, float b) {
  f32x2 v = {a, b};
  return __builtin_bit_cast(unsigned, __builtin_convertvector(v, bf16x2));
}
__device__ __forceinline__ unsigned pks(float a, float b) { return min(pk(a, b), 0xFFFFFFFEu); }
__device__ __forceinline__ float bf_lo(unsigned u) { return __builtin_bit_cast(float, u << 16); }
__device__ __forceinline__ float bf_hi(unsigned u) { return __builtin_bit_cast(float, u & 0xFFFF0000u); }
__device__ __forceinline__ float rbf(float a) { return bf_lo(pk(a, a)); }
__device__ __forceinline__ float f_(unsigned u) { return __builtin_bit_cast(float, u); }
__device__ __forceinline__ unsigned u_(float f) { return __builtin_bit_cast(unsigned, f); }
__device__ __forceinline__ unsigned umax3(unsigned a, unsigned b, unsigned c) { return max(max(a, b), c); }
__device__ __forceinline__ unsigned umax4(unsigned m, u32x4 v) { return umax3(umax3(m, v.x, v.y), v.z, v.w); }

__device__ __forceinline__ float dot2(unsigned a, unsigned b, float c) {
  return __builtin_amdgcn_fdot2_f32_bf16(__builtin_bit_cast(bf16x2, a), __builtin_bit_cast(bf16x2, b), c, false);
}
__device__ __forceinline__ f32x4 mfma32(u32x4 a, u32x4 b, f32x4 c) {
  return __builtin_amdgcn_mfma_f32_16x16x32_bf16(__builtin_bit_cast(bf16x8, a), __builtin_bit_cast(bf16x8, b), c, 0, 0, 0);
}
__device__ __forceinline__ f32x4 mfma16(u32x2 a, u32x2 b, f32x4 c) {
  return __builtin_amdgcn_mfma_f32_16x16x16bf16_1k(__builtin_bit_cast(s16x4, a), __builtin_bit_cast(s16x4, b), c, 0, 0, 0);
}
__device__ __forceinline__ f32x4 smfma(u32x4 a, u32x8 b, f32x4 c, int idx) {
  return __builtin_amdgcn_smfmac_f32_16x16x64_bf16(__builtin_bit_cast(bf16x8, a), __builtin_bit_cast(bf16x16, b), c, idx, 0, 0);
}
__device__ __forceinline__ u32x2 tr8(LAS unsigned char* p) {
  return __builtin_bit_cast(u32x2, __builtin_amdgcn_ds_read_tr16_b64_v4i16((LAS s16x4*)p));
}
__device__ __forceinline__ u32x4 lds16(LAS unsigned char* p) { return *(LAS u32x4*)p; }
__device__ __forceinline__ u32x4 cat(u32x2 a, u32x2 b) { return u32x4{a.x, a.y, b.x, b.y}; }
__device__ __forceinline__ u32x8 cat(u32x4 a, u32x4 b) { return u32x8{a.x, a.y, a.z, a.w, b.x, b.y, b.z, b.w}; }
__device__ __forceinline__ float halfsum(float x, float y) {
  auto r = __builtin_amdgcn_permlane32_swap(u_(x), u_(y), false, false);
  return f_(r[0]) + f_(r[1]);
}
__device__ __forceinline__ float halfsum1(float x, float y) {
  auto r = __builtin_amdgcn_permlane32_swap(u_(x), u_(y), false, false);
  float o;
  asm("v_add_f32 %0, %1, %2" : "=v"(o) : "v"(r[0]), "v"(r[1]));
  return o;
}
template <int CTRL> __device__ __forceinline__ float dpp(float x) {
  return f_((unsigned)__builtin_amdgcn_update_dpp(0, (int)u_(x), CTRL, 0xf, 0xf, false));
}

template <class T> __device__ __forceinline__ void pin_a(T& v) { asm volatile("" : "+a"(v)); }
template <class T> __device__ __forceinline__ void pin_v(T& v) { asm volatile("" : "+v"(v)); }

__device__ __forceinline__ void sb() { __builtin_amdgcn_sched_barrier(0); }

struct Ctx {
  rsrc_t ws;
  u32x4 srd;
  LAS unsigned char* lds;
  int tid, lane, wave;
};

__device__ __forceinline__ void stamp(const Ctx&, int, int = 0) {}
__device__ __forceinline__ void stampc(const Ctx&, int, int = 0) {}
__device__ __forceinline__ void stampl(const Ctx&, int) {}
__device__ __forceinline__ void stampf(const Ctx&, int, int = 0) {}
#define CYC(x)
#define CYCW(x)
#define CYCOUT(...)
__device__ __forceinline__ void spin_check(const Ctx& c, unsigned& n) {
  unsigned ts, tv;
  asm volatile(
      "s_add_u32 %0, %0, 1\n\t"
      "s_and_b32 %1, %0, 0x3ff\n\t"
      "s_cbranch_scc1 1f\n\t"
      "buffer_load_dword %2, off, %3, 0 sc1\n\t"
      "s_waitcnt vmcnt(0)\n\t"
      "v_readfirstlane_b32 %1, %2\n\t"
      "s_nop 0\n\t"
      "s_cmp_lg_u32 %1, -1\n\t"
      "s_cbranch_scc1 2f\n\t"
      "s_cmp_lt_u32 %0, %4\n\t"
      "s_cbranch_scc1 1f\n\t"
      "v_mov_b32 %2, 1\n\t"
      "buffer_store_dword %2, off, %3, 0 sc1\n\t"
      "s_waitcnt vmcnt(0)\n"
      "2:\n\t"
      "s_endpgm\n"
      "1:"
      : "+s"(n), "=&s"(ts), "=&v"(tv)
      : "s"(c.srd), "s"(SPIN_CAP)
      : "memory", "scc");
}
template <class T> __device__ __forceinline__ void pin_s(T& v) { asm volatile("" : "+s"(v)); }
template <int SLEEP>
__device__ __forceinline__ int wait_flags(const Ctx& c, unsigned fl, int n, int need) {
  unsigned sp = 0;
  const unsigned vo = fl + FS * (unsigned)min(c.lane, n - 1);
  for (;;) {
    int v = (int)ld4<SC1>(c.ws, vo, 0);
    if (!__any(v < need)) {
      int got = need;
      if (!__any(v < need + 2)) got = need + 2;
      if (!__any(v < need + 6)) got = need + 6;
      if (!__any(v < need + 16)) got = need + 16;
      return got;
    }
    __builtin_amdgcn_s_sleep(SLEEP);
    spin_check(c, sp);
  }
}
template <int SLEEP>
__device__ __forceinline__ int wait_flags_blk(const Ctx& c, unsigned fl, int n, int need, LAS int* w) {
  if (c.wave == 0) { const int r = wait_flags<SLEEP>(c, fl, n, need); if (c.lane == 0) *w = r; }
  bar();
  const int r = __builtin_amdgcn_readfirstlane(*w);
  bar();
  return r;
}
__device__ __forceinline__ void publish(const Ctx& c, unsigned fl, int val) {
  wait_vm<0>();
  bar();
  if (c.tid == 0) st4<SC1>(c.ws, fl, 0, (unsigned)val);
}

template <int SLEEP, int DELAY>
__device__ __forceinline__ void doorbell(const Ctx& c, unsigned fl, int need) {
  unsigned sp = 0;
  while ((int)__builtin_amdgcn_readfirstlane(ld4<SC1>(c.ws, fl, 0)) < need) { __builtin_amdgcn_s_sleep(SLEEP); spin_check(c, sp); }
  if (__builtin_amdgcn_readfirstlane(sp)) __builtin_amdgcn_s_sleep(DELAY);
}
__device__ __forceinline__ u32x4 nosent(u32x4 v) { return u32x4{min(v.x, 0xFFFFFFFEu), min(v.y, 0xFFFFFFFEu), min(v.z, 0xFFFFFFFEu), min(v.w, 0xFFFFFFFEu)}; }
__device__ __forceinline__ u32x2 nosent(u32x2 v) { return u32x2{min(v.x, 0xFFFFFFFEu), min(v.y, 0xFFFFFFFEu)}; }

__device__ __forceinline__ unsigned side_bit(unsigned b, unsigned side) { return b | (((side ^ (b >> 21)) & 1u) << 13); }
__device__ __forceinline__ unsigned xr_phys(unsigned o, unsigned side) { return side_bit(OFF_XR + ((o >> 13) << 14) + (o & 8191u), side); }
__device__ __forceinline__ unsigned m_phys(unsigned s, unsigned j, unsigned side) { return side_bit(OFF_M + (s * 40u + j) * 16384u, side); }
__device__ __forceinline__ unsigned my_iod() { return (((unsigned)__builtin_amdgcn_s_getreg(20 | (3 << 11))) >> 2) & 1u; }
struct Sides { unsigned me; unsigned long long light; };
__device__ __forceinline__ unsigned ld_table(const Ctx& c) { return ld4<SC1>(c.ws, FL_H + FS * (unsigned)c.lane, 0); }
__device__ __forceinline__ void get_sides(const Ctx& c, Sides& o, unsigned v = SENT) {
  unsigned sp = 0;
  if (__any(v == SENT)) {
    for (;;) {
      v = ld_table(c);
      if (!__any(v == SENT)) break;
      __builtin_amdgcn_s_sleep(8);
      spin_check(c, sp);
    }
  }
  const unsigned cc = __popcll(__ballot(v & 1u) >> 32) > 16 ? 1u : 0u;
  const unsigned long long iod = __ballot(v & 2u);
  o.me = cc ^ my_iod();
  o.light = cc ? ~iod : iod;
}
__device__ __forceinline__ unsigned pz_side(const Sides& sd, int j) { return (unsigned)(sd.light >> j) & 1u; }
__device__ __forceinline__ void ring(const Ctx& c, int bell, int val) {
  if (c.lane < 2) st4<SC1>(c.ws, OFF_DB + (unsigned)bell * FS + (unsigned)c.lane * 8192u, 0, (unsigned)val);
}
__device__ __forceinline__ void wait_bells(const Ctx& c, unsigned vo, int need) {
  unsigned sp = 0;
  while (__any((int)ld4<SC1>(c.ws, vo, 0) < need)) spin_check(c, sp);
}
__device__ __forceinline__ unsigned bell_vo(const Ctx& c, int bell0, unsigned side) { return side_bit(OFF_DB + (unsigned)(bell0 + (c.lane & 3)) * FS, side); }
__device__ __forceinline__ unsigned calibrate(const Ctx& c, int slot) {
  const unsigned base = OFF_CAL + (unsigned)slot * 256u;
  st16<SC1>(c.ws, base + (unsigned)(c.lane >> 3) * 8192u + (unsigned)(c.lane & 7) * 16u, 0, u32x4{0u, 0u, 0u, 0u});
  wait_vm<0>();
  unsigned best[2] = {~0u, ~0u};
#pragma unroll
  for (int i = 0; i < 8; ++i) {
    const unsigned long long t0 = __builtin_amdgcn_s_memtime();
    wait_lgkm();
    unsigned v = ld4<SC1>(c.ws, base, (unsigned)i * 8192u);
    asm volatile("s_waitcnt vmcnt(0)" : "+v"(v)::"memory");
    const unsigned long long t1 = __builtin_amdgcn_s_memtime();
    wait_lgkm();
    const unsigned par = (((OFF_CAL + i * 8192u) >> 13) ^ ((OFF_CAL + i * 8192u) >> 21)) & 1u;
    best[par] = min(best[par], (unsigned)(t1 - t0));
  }
  return best[1] < best[0] ? 1u : 0u;
}

template <int MODE> __device__ __forceinline__ int rowfn(int g, int j) {
  if (MODE == 0) return 8 * g + j;
  if (MODE == 1) return 16 * (g >> 1) + 8 * ((j >> 1) & 1) + 4 * (g & 1) + 2 * ((j >> 2) & 1) + (j & 1);
  if (MODE == 2) return 16 * (j >> 2) + 4 * g + (j & 3);
  return 64 * (j >> 2) + 4 * g + (j & 3);
}
template <int MODE> __device__ __forceinline__ unsigned stage_src(const Ctx& c, int ldw) {
  const int pos = c.lane >> 3, cp = c.lane & 7;
  const int u = (cp >> 1) ^ ((pos >> 1) & 3);
  const int ch = 2 * u + (cp & 1);
  const int g = 2 * (c.wave >> 1) + (pos >> 2), j = 4 * (c.wave & 1) + (pos & 3);
  return (unsigned)((rowfn<MODE>(g, j) * ldw + ch * 8) * 2);
}
__device__ __forceinline__ unsigned stage_rd(int lane, int u) {
  const int g = lane >> 4, q = (lane & 15) >> 2, p = lane & 3, pos = 4 * (g & 1) + q;
  return (unsigned)((g >> 1) * 2048 + pos * 128 + ((u ^ ((pos >> 1) & 3)) * 32) + p * 8);
}
__device__ __forceinline__ u32x4 stage_frag(LAS unsigned char* chunk_plus_rd) { return cat(tr8(chunk_plus_rd), tr8(chunk_plus_rd + 1024)); }

template <int NB, int NRES, int BATCH, int NBUF, class Issue, class Consume, class Pin, class Post>
__device__ __forceinline__ void stage_weights(const Ctx& c, Issue&& issue, Consume&& consume, Pin&& pin, Post&& post) {
  constexpr int SLOT = BATCH * 4096;
  bar();
  sfor<0, (NBUF < NB ? NBUF : NB)>([&](auto b) {
    sfor<0, BATCH>([&](auto i) { issue(std::integral_constant<int, b * BATCH + i>{}, c.lds + b * SLOT + i * 4096 + c.wave * 1024); });
  });
  stampl(c, 300);
  sfor<0, NB - NRES>([&](auto b) {
    constexpr int inflight = ((b + NBUF < NB ? b + NBUF : NB) - (b + 1)) * BATCH;
    wait_vm<inflight>();
    bar();
    stampl(c, 301 + b);
    sfor<0, BATCH>([&](auto i) { consume(std::integral_constant<int, b * BATCH + i>{}, c.lds + (b % NBUF) * SLOT + i * 4096); });
    sb();
    sfor<0, BATCH>([&](auto i) { pin(std::integral_constant<int, b * BATCH + i>{}); });
    if constexpr (b + NBUF < NB) {
      bar();
      sfor<0, BATCH>([&](auto i) {
        issue(std::integral_constant<int, (b + NBUF) * BATCH + i>{}, c.lds + (b % NBUF) * SLOT + i * 4096 + c.wave * 1024);
      });
    }
    sb();
    sfor<0, BATCH>([&](auto i) { post(std::integral_constant<int, b * BATCH + i>{}); });
  });
  wait_vm<0>();
  bar();
}

__device__ __forceinline__ float ln_stats(const Ctx& c, const u32x4 (&raw)[10], f32x2 (&cv)[40], LAS float* red) {
  const int tau = c.wave & 1, kh = c.wave >> 1, g = c.lane >> 4, rho = c.lane & 15, row = tau * 16 + rho;
  float s0 = 0.f, s1 = 0.f;
  unsigned ones_u = 0x3F803F80u;
  pin_v(ones_u);
#pragma unroll
  for (int i = 0; i < 10; ++i) {
    s0 = dot2(raw[i].x, ones_u, s0); s1 = dot2(raw[i].y, ones_u, s1);
    s0 = dot2(raw[i].z, ones_u, s0); s1 = dot2(raw[i].w, ones_u, s1);
  }
  float s = s0 + s1;
  s += __shfl_xor(s, 16); s += __shfl_xor(s, 32);
  if (g == 0) red[kh * 32 + row] = s;
  bar();
  const float mean = (red[row] + red[32 + row]) * (1.f / DM);
  f32x2 v2 = {0.f, 0.f};
#pragma unroll
  for (int i = 0; i < 10; ++i)
#pragma unroll
    for (int d = 0; d < 4; ++d) {
      const f32x2 t = f32x2{bf_lo(raw[i][d]), bf_hi(raw[i][d])} - mean;
      cv[4 * i + d] = t;
      v2 += t * t;
    }
  float v = v2[0] + v2[1];
  v += __shfl_xor(v, 16); v += __shfl_xor(v, 32);
  if (g == 0) red[64 + kh * 32 + row] = v;
  bar();
  return 1.f / sqrtf((red[64 + row] + red[96 + row]) * (1.f / DM) + 1e-5f);
}
template <class Store>
__device__ __forceinline__ void ln_apply(const Ctx& c, const f32x2 (&cv)[40], float rstd, LAS float* shiftv, LAS float* scalev, Store&& store) {
  const int tau = c.wave & 1, kh = c.wave >> 1, g = c.lane >> 4;
#pragma unroll
  for (int i = 0; i < 10; ++i) {
    const int kap = kh * 10 + i, d0 = (4 * kap + g) * 8;
    const f32x4 sc0 = *(LAS f32x4*)(scalev + d0), sc1 = *(LAS f32x4*)(scalev + d0 + 4);
    const f32x4 sh0 = *(LAS f32x4*)(shiftv + d0), sh1 = *(LAS f32x4*)(shiftv + d0 + 4);
    const f32x2 r0 = (cv[4 * i + 0] * rstd) * f32x2{sc0[0], sc0[1]} + f32x2{sh0[0], sh0[1]};
    const f32x2 r1 = (cv[4 * i + 1] * rstd) * f32x2{sc0[2], sc0[3]} + f32x2{sh0[2], sh0[3]};
    const f32x2 r2 = (cv[4 * i + 2] * rstd) * f32x2{sc1[0], sc1[1]} + f32x2{sh1[0], sh1[1]};
    const f32x2 r3 = (cv[4 * i + 3] * rstd) * f32x2{sc1[2], sc1[3]} + f32x2{sh1[2], sh1[3]};
    store(kap * 2 + tau, u32x4{pk(r0[0], r0[1]), pk(r1[0], r1[1]), pk(r2[0], r2[1]), pk(r3[0], r3[1])});
  }
}
__device__ __forceinline__ void load_mod(const Ctx& c, int f0, int nfl, LAS float* dst, bool add1) {
  for (int ch = c.tid; ch < ((nfl / 4 + 255) & ~255); ch += 256) {
    const int cc = min(ch, nfl / 4 - 1);
    unsigned sp = 0;
    u32x4 v;
    for (;;) {
      v = ld16<SC1>(c.ws, OFF_MOD + (unsigned)(f0 * 4 + cc * 16), 0);
      if (!__any(umax4(0u, v) == SENT)) break;
      __builtin_amdgcn_s_sleep(1);
      spin_check(c, sp);
    }
    f32x4 f = __builtin_bit_cast(f32x4, v);
    if (add1) { f[0] = rbf(1.f + f[0]); f[1] = rbf(1.f + f[1]); f[2] = rbf(1.f + f[2]); f[3] = rbf(1.f + f[3]); }
    *(LAS f32x4*)(dst + cc * 4) = f;
  }
}

__device__ __forceinline__ void role_pre(const Params& p, const Ctx& c, int li, int jpz) {
  const unsigned iod = my_iod();
  unsigned tabw = iod << 1;
  if (jpz >= 0) {
    const u32x4 sv = {SENT, SENT, SENT, SENT};
#pragma unroll
    for (int i = 0; i < 8; ++i) st16<SC1>(c.ws, (unsigned)c.tid * 16u, OFF_M + (unsigned)((i >> 2) * 40 + jpz) * 16384u + (unsigned)(i & 3) * 4096u, sv);
    if (c.tid < 128) st16<SC1>(c.ws, xr_phys(L_P + (unsigned)jpz * 1024u + (unsigned)(c.tid & 63) * 16u, c.tid >> 6), 0, sv);
    else if (c.tid < 192) st16<SC1>(c.ws, xr_phys(L_MEM + (unsigned)(1 + ((c.tid >> 4) & 1)) * MEM_SZ + (unsigned)jpz * 256u + (unsigned)(c.tid & 15) * 16u, (c.tid >> 5) & 1), 0, sv);
    else if (c.tid < 224) {
      const u32x4 m0 = ld16<0>(mk_rsrc(p.mem0, 8 * DM * 2), (unsigned)((c.tid & 7) * DM * 2 + jpz * 32 + ((c.tid >> 3) & 1) * 16), 0);
      st16<SC1>(c.ws, xr_phys(L_MEM + (unsigned)jpz * 256u + (unsigned)(c.tid & 15) * 16u, (c.tid >> 4) & 1), 0, nosent(m0));
    }
  }
  LAS float* sc = (LAS float*)c.lds;
  LAS float* red = (LAS float*)(c.lds + 4096);
  rsrc_t rw = mk_rsrc(p.wmod, DM * 3840 * 2);
  const int cg = c.tid & 7, rg = c.tid >> 3;
  u32x4 w[20];
  const unsigned cv3 = ld4<0>(mk_rsrc(p.c, DM * 2), (unsigned)c.tid * 4u, 0), cv4 = ld4<0>(mk_rsrc(p.c, DM * 2), (unsigned)c.tid * 4u + 1024u, 0);
  const int r0 = (li / 20) * 224 + rg * 7;
  if (li < 60) {
#pragma unroll
    for (int i = 0; i < 7; ++i) w[i] = ld16<0>(rw, (unsigned)(((r0 + i) * 3840 + (li % 20) * 64 + cg * 8) * 2), 0);
  }
  rsrc_t rx = mk_rsrc(p.x, 2048 * DM * 2);
  u32x4 raw[10];
  {
    const int tau = c.wave & 1, kh = c.wave >> 1, g = c.lane >> 4, rho = c.lane & 15;
    const unsigned vo = (unsigned)(((li * 32 + tau * 16 + rho) * DM + (40 * kh + g) * 8) * 2);
#pragma unroll
    for (int i = 0; i < 10; ++i) raw[i] = ld16<0>(rx, vo + i * 64, 0);
  }
  sb();
  stampl(c, 300);
  auto silu = [](float v) { return rbf(v / (1.f + __expf(-v))); };
  *(LAS f32x2*)(sc + 2 * c.tid) = f32x2{silu(bf_lo(cv3)), silu(bf_hi(cv3))};
  if (c.tid < 80) *(LAS f32x2*)(sc + 512 + 2 * c.tid) = f32x2{silu(bf_lo(cv4)), silu(bf_hi(cv4))};
  bar();
  stampl(c, 301);
  auto gemv = [&](auto nrow, int row0, unsigned dst, bool round) {
    float a[8] = {};
#pragma unroll
    for (int i = 0; i < nrow; ++i) {
      const float sv = sc[row0 + i];
#pragma unroll
      for (int d = 0; d < 4; ++d) { a[2 * d] += sv * bf_lo(w[i][d]); a[2 * d + 1] += sv * bf_hi(w[i][d]); }
    }
    *(LAS f32x4*)(red + rg * 64 + cg * 8) = f32x4{a[0], a[1], a[2], a[3]};
    *(LAS f32x4*)(red + rg * 64 + cg * 8 + 4) = f32x4{a[4], a[5], a[6], a[7]};
    bar();
    float t = 0.f;
#pragma unroll
    for (int r = 0; r < 8; ++r) t += red[(c.wave * 8 + r) * 64 + c.lane];
    red[2048 + c.tid] = t;
    bar();
    if (c.tid < 64) {
      t = (red[2048 + c.tid] + red[2112 + c.tid]) + (red[2176 + c.tid] + red[2240 + c.tid]);
      st4<SC1>(c.ws, dst + (unsigned)c.tid * 4, 0, min(u_(round ? rbf(t) : t), 0xFFFFFFFEu));
    }
  };
  if (li < 60) gemv(std::integral_constant<int, 7>{}, r0, OFF_MODP + (unsigned)((li / 20) * 1280 + (li % 20) * 64) * 4, false);
  stampl(c, 302);
  LAS float* shiftv = (LAS float*)(c.lds + 16384);
  LAS float* red2 = (LAS float*)(c.lds + 24576);
  f32x2 cv[40];
  const float rstd = ln_stats(c, raw, cv, red2);
  stampl(c, 303);
  for (int ch = c.tid; ch < 512; ch += 256) {
    const int cc = min(ch, 319);
    unsigned sp = 0;
    u32x4 v0, v1, v2;
    for (;;) {
      v0 = ld16<SC1>(c.ws, (unsigned)cc * 16, OFF_MODP); v1 = ld16<SC1>(c.ws, (unsigned)cc * 16, OFF_MODP + 5120); v2 = ld16<SC1>(c.ws, (unsigned)cc * 16, OFF_MODP + 10240);
      if (!__any(umax4(umax4(umax4(0u, v0), v1), v2) == SENT)) break;
      __builtin_amdgcn_s_sleep(2);
      spin_check(c, sp);
    }
    f32x4 f = __builtin_bit_cast(f32x4, v0) + __builtin_bit_cast(f32x4, v1) + __builtin_bit_cast(f32x4, v2);
    const float one = cc >= 160 ? 1.f : 0.f;
#pragma unroll
    for (int d = 0; d < 4; ++d) f[d] = rbf(one + rbf(f[d]));
    *(LAS f32x4*)(shiftv + cc * 4) = f;
  }
  bar();
  stampl(c, 304);
  ln_apply(c, cv, rstd, shiftv, shiftv + DM, [&](int fr, u32x4 o) { st16<SC1>(c.ws, (unsigned)c.lane * 16 + (unsigned)fr * 1024, OFF_HB + (unsigned)li * HB_SZ, o); });
  stampl(c, 305);
  if (li >= 32 && c.wave == 0) tabw |= calibrate(c, li - 32) ^ iod;
  publish(c, FL_H + FS * li, (int)tabw);
  stamp(c, 1);
  if (li >= 24) {
#pragma unroll
    for (int i = 0; i < 20; ++i) w[i] = ld16<0>(rw, (unsigned)(((rg * 20 + i) * 3840 + 1280 + (li - 24) * 64 + cg * 8) * 2), 0);
    sb();
    gemv(std::integral_constant<int, 20>{}, rg * 20, OFF_MOD + (unsigned)(1280 + (li - 24) * 64) * 4, true);
  }
}

__device__ __forceinline__ int sp_row(int lane) { return ((lane >> 1) & 6) | (lane & 1); }
__device__ __forceinline__ int sp_half(int lane) { return (lane >> 1) & 1; }
__device__ __forceinline__ int sp_pos(int row, int half) { return 4 * (row >> 1) + 2 * half + (row & 1); }
__device__ __forceinline__ float add1(float a, float b) { float o; asm("v_add_f32 %0, %1, %2" : "=v"(o) : "v"(a), "v"(b)); return o; }
struct Gath { unsigned vo2, bvo; LAS unsigned char* w0; LAS unsigned char* w2; };
__device__ __forceinline__ Gath gather_setup(const Ctx& c, bool perm, unsigned side) {
  int r = c.lane & 7;
  if (perm) r = (r & 1) | ((r & 2) << 1) | ((r & 4) >> 1);
  const int wpos = ((c.lane & 48) + sp_pos(r, (c.lane >> 3) & 1)) * 16;
  Gath g;
  g.vo2 = c.wave < 2 ? (unsigned)c.lane * 16 : 0x80000000u;
  g.bvo = bell_vo(c, 4, side);
  g.w0 = c.lds + 2 * c.wave * 1024 + wpos;
  g.w2 = c.lds + (c.wave < 2 ? (8 + c.wave) * 1024 : 79872 + (c.wave - 2) * 1024) + wpos;
  return g;
}
__device__ __forceinline__ void gather_mem(const Ctx& c, const Gath& ga, int s, unsigned side, unsigned& nfail) {
  const unsigned lo = L_MEM + (unsigned)s * MEM_SZ;
  unsigned s0 = xr_phys(lo + c.wave * 2048, side), s2 = xr_phys(lo + 8192 + (c.wave & 1) * 1024, side);
  pin_s(s0); pin_s(s2);
  unsigned sp = 0;
  if (s > 0) wait_bells(c, ga.bvo, s);
  if (BELL_M_GAP) __builtin_amdgcn_s_sleep(BELL_M_GAP);
  for (;;) {
    const u32x4 v0 = ld16<SC1>(c.ws, (unsigned)c.lane * 16, s0), v1 = ld16<SC1>(c.ws, (unsigned)c.lane * 16 + 1024, s0), v2 = ld16<SC1>(c.ws, ga.vo2, s2);
    sb();
    *(LAS u32x4*)(ga.w0) = v0;
    unsigned m = umax4(0u, v0);
    sb();
    *(LAS u32x4*)(ga.w0 + 1024) = v1;
    m = umax4(m, v1);
    sb();
    *(LAS u32x4*)(ga.w2) = v2;
    m = umax4(m, v2);
    if (!__any(m == SENT)) break;
    spin_check(c, sp);
  }
  bar();
}
struct ResW { LAS unsigned char* lo; LAS unsigned char* hi; };
__device__ __forceinline__ ResW res_base(const Ctx& c) {
  ResW r; r.lo = c.lds + 81920 + stage_rd(c.lane, c.wave); r.hi = r.lo + 40960;
  pin_v(r.lo); pin_v(r.hi);
  return r;
}
__device__ __forceinline__ u32x8 res_frag(const ResW& w, int t) {
  LAS unsigned char* b = t < 5 ? w.lo + t * 8192 : w.hi + (t - 5) * 8192;
  return cat(stage_frag(b), stage_frag(b + 4096));
}
template <int CTRL> __device__ __forceinline__ unsigned dppu(unsigned x) { return (unsigned)__builtin_amdgcn_update_dpp(0, (int)x, CTRL, 0xf, 0xf, true); }
__device__ __forceinline__ float row16_max(float m) {
  asm volatile("s_nop 1\n\tv_max_f32_dpp %0, %0, %0 row_ror:8 row_mask:0xf bank_mask:0xf\n\t"
               "s_nop 1\n\tv_max_f32_dpp %0, %0, %0 row_ror:4 row_mask:0xf bank_mask:0xf\n\t"
               "s_nop 1\n\tv_max_f32_dpp %0, %0, %0 row_ror:2 row_mask:0xf bank_mask:0xf\n\t"
               "s_nop 1\n\tv_max_f32_dpp %0, %0, %0 row_ror:1 row_mask:0xf bank_mask:0xf" : "+v"(m));
  return m;
}
__device__ __forceinline__ float row16_sum(float m) {
  asm volatile("s_nop 1\n\tv_add_f32_dpp %0, %0, %0 row_ror:8 row_mask:0xf bank_mask:0xf\n\t"
               "s_nop 1\n\tv_add_f32_dpp %0, %0, %0 row_ror:4 row_mask:0xf bank_mask:0xf\n\t"
               "s_nop 1\n\tv_add_f32_dpp %0, %0, %0 row_ror:2 row_mask:0xf bank_mask:0xf\n\t"
               "s_nop 1\n\tv_add_f32_dpp %0, %0, %0 row_ror:1 row_mask:0xf bank_mask:0xf" : "+v"(m));
  return m;
}

constexpr int hmap(int id) { return (REV && id < 100) ? (9 - id / 10) * 10 + id % 10 : id; }
constexpr int vmap(int id) {
  if (!REV || id >= 100) return id;
  const int f = id / 2, a = id % 2;
  const int blk = f / 5, k = f % 5;
  return (k < 2 ? (19 - (2 * blk + k)) * 2 : 40 + (29 - (3 * blk + k - 2)) * 2) + a;
}
__device__ __forceinline__ void role_H(const Params& p, const Ctx& c, int h) {
  u32x8 W[10][5];
  unsigned tabv, kfl;
  const unsigned kmvo = (c.lane & 8) ? (unsigned)((c.lane >> 4) * 64 + (c.lane & 7) * 8 + c.wave * 256) : 0x80000000u;
#pragma unroll
  for (int i = 0; i < 6; ++i) st8<SC1>(c.ws, kmvo, OFF_KM + (unsigned)((i / 3) * NH + h) * 3072 + (unsigned)(i % 3) * 1024, u32x2{SENT, SENT});
  {
    rsrc_t rq = mk_rsrc(p.Wq, DM * NH * DH * 2), rk = mk_rsrc(p.Wk, DM * NH * DH * 2);
    const unsigned src = stage_src<1>(c, NH * DH), rd = stage_rd(c.lane, c.wave);
    stage_weights<12, 2, 10, 4>(c,
      [&](auto ci, LAS unsigned char* dst) {
        constexpr int id = hmap(ci);
        if constexpr (id < 100) {
          constexpr int t = id / 10, pn = (id % 10) / 2, a = id % 2;
          constexpr int col = (pn < 3 ? pn : pn - 3) * 64;
          dma16<0>(pn < 3 ? rq : rk, dst, src, (unsigned)(((64 * t + 32 * a) * NH * DH + h * DH + col) * 2));
        } else {
          constexpr int sp = id - 100;
          dma16<0>(rk, dst, src, (unsigned)(((32 * sp) * NH * DH + h * DH + 128) * 2));
        }
      },
      [&](auto ci, LAS unsigned char* chunk) {
        constexpr int id = hmap(ci), t = id / 10, pn = (id % 10) / 2, a = id % 2;
        if constexpr (a == 1) {
          W[t][pn] = cat(stage_frag(chunk - 4096 + rd), stage_frag(chunk + rd));
        }
      },
      [&](auto ci) {
        constexpr int id = hmap(ci), t = id / 10, pn = (id % 10) / 2, a = id % 2;
        if constexpr (a == 1) { if constexpr (t * 5 + pn < 32) pin_a(W[t][pn]); else pin_v(W[t][pn]); }
      },
      [&](auto ci) { if constexpr (ci == 80) { tabv = ld_table(c); kfl = ld4<SC1>(c.ws, FL_K + FS * h, 0); } });
  }
  if (c.tid == 0) st4<SC1>(c.ws, FL_KM + FS * h, 0, 0u);
  Sides sd;
  get_sides(c, sd, tabv);
  LAS unsigned char* A = c.lds + c.lane * 16;
  const int g = c.lane >> 4, n = c.lane & 15;
  LAS unsigned char* TW = c.lds + 10240 + c.wave * 1920 + n * 40 + g * 4;
  LAS unsigned char* TR = c.lds + 10240 + c.wave * 1920 + (c.lane >> 2) * 40 + (c.lane & 3) * 8;
  LAS unsigned char* PART = c.lds + 49152;
  LAS unsigned char* PW = PART + c.wave * 3328 + n * 208 + g * 16;
  const ResW WR = res_base(c);
  const int idx = (c.lane & 2) ? 0xEEEE : 0x4444;
  const int q = (c.wave & 1) * 4 + g, it = min(n, 9);
  LAS unsigned char* PR = PART + q * 208 + (it < 8 ? it : it + 2) * 16;
  const float lmask = n < 10 ? 1.f : 0.f;
  const unsigned plo = it < 8 ? (unsigned)((h >> 1) * 1024 + (it >> 2) * 512 + (h & 1) * 256 + sp_pos(q, (it >> 1) & 1) * 16) : (unsigned)(16384 + h * 128 + q * 16);
  const bool ringer = USE_BELL_P && (h & 7) == 0 && c.wave == 0;
  const Gath ga = gather_setup(c, false, sd.me);
  const unsigned rvo = (ringer && c.lane < 2) ? OFF_DB + (unsigned)(h >> 3) * FS + (unsigned)c.lane * 8192u : 0x80000000u;
  unsigned nfail = 0;
  LAS unsigned char* KX = c.lds + 24576;
  int kready = (int)__builtin_amdgcn_readfirstlane(kfl);
  auto dma_kx = [&](int s) {
    if (kready < s) kready = wait_flags<2>(c, FL_K + FS * h, 1, s);
#pragma unroll
    for (int i = 0; i < 6; ++i)
      dma16<SC1>(c.ws, KX + (s & 1) * KX_SZ + (c.wave * 6 + i) * 1024, (unsigned)c.lane * 16, OFF_KX + (unsigned)(s * NH + h) * KX_SZ + (unsigned)(c.wave * 6 + i) * 1024);
  };
  stamp(c, 1);
  if (c.wave < 2) dma_kx(0);
  for (int s = 0; s < NSEG; ++s) {
    unsigned pvo = xr_phys(L_P + (unsigned)s * P_SZ + plo, n & 1);
    if (n >= 10) pvo = 0x80000000u;
    f32x4 acc[6] = {};
    u32x8 wl[10];
    wl[0] = res_frag(WR, 0); wl[1] = res_frag(WR, 1);
#pragma unroll
    for (int i = 0; i < 6; ++i) pin_v(acc[i]);
    pin_v(wl[0]); pin_v(wl[1]); pin_v(pvo);
    gather_mem(c, ga, s, sd.me, nfail);
    stampf(c, 4 + 4 * s, 128);
    CYC(t0)
    u32x4 a[10], kx[3];
    a[0] = lds16(A); a[1] = lds16(A + 1024); a[2] = lds16(A + 2048);
    sb();
#pragma unroll
    for (int t = 0; t < 6; ++t) {
      LAS unsigned char* wb = t + 2 < 5 ? WR.lo + (t + 2) * 8192 : WR.hi + (t + 2 - 5) * 8192;
      acc[5] = smfma(a[t], wl[t], acc[5], idx); sb();
      a[t + 3] = lds16(A + (t + 3) * 1024); sb();
      acc[0] = smfma(a[t], W[t][0], acc[0], idx); sb();
      const u32x2 w0 = tr8(wb); sb();
      acc[1] = smfma(a[t], W[t][1], acc[1], idx); sb();
      const u32x2 w1 = tr8(wb + 1024); sb();
      acc[2] = smfma(a[t], W[t][2], acc[2], idx); sb();
      const u32x2 w2 = tr8(wb + 4096); sb();
      acc[3] = smfma(a[t], W[t][3], acc[3], idx); sb();
      const u32x2 w3 = tr8(wb + 5120); sb();
      acc[4] = smfma(a[t], W[t][4], acc[4], idx); sb();
      wl[t + 2] = cat(cat(w0, w1), cat(w2, w3));
    }
    CYC(t1)
    if (BELL_P_POS == 1 && ringer) ring(c, h >> 3, s);
    f32x4 sx0, sx1, sm;
    u32x2 F[3];
    float v[4];
#define H_S(i, t) acc[i] = smfma(a[t], W[t][i], acc[i], idx); sb();
#define H_L(t) acc[5] = smfma(a[t], wl[t], acc[5], idx); sb();
#define H_A(i, r) v[r] = add1(acc[(r >> 1) * 3 + i][r & 1], acc[(r >> 1) * 3 + i][2 + (r & 1)]); sb();
#define H_K(i) kx[i] = lds16(KX + (s & 1) * KX_SZ + (4 * i + c.wave) * 1024 + c.lane * 16); sb();
#define H_MZ(d, x, i) asm volatile("v_mfma_f32_16x16x16_bf16 %0, %1, %2, 0" : "=&v"(d) : "v"(x), "v"(F[i])); sb();
#define H_MA(d, x, i) asm volatile("v_mfma_f32_16x16x16_bf16 %0, %1, %2, %0" : "+v"(d) : "v"(x), "v"(F[i])); sb();
#define KLO(i) (u32x2{kx[i].x, kx[i].y})
#define KHI(i) (u32x2{kx[i].z, kx[i].w})
#define H_C(r) pw[r] = pk(v[2 * r], v[2 * r + 1]); sb();
#define H_D(i) *(LAS unsigned*)(TW + i * 640) = pw[0]; *(LAS unsigned*)(TW + i * 640 + 16) = pw[1]; F[i] = tr8(TR + i * 640); sb();
    unsigned pw[2];
    {
      LAS unsigned char* wb = WR.hi + 3 * 8192;
      H_S(1, 6) a[9] = lds16(A + 9 * 1024); const u32x2 w0 = tr8(wb); sb(); H_S(4, 6) const u32x2 w1 = tr8(wb + 1024); sb();
      H_S(1, 7) const u32x2 w2 = tr8(wb + 4096); sb(); H_S(4, 7) const u32x2 w3 = tr8(wb + 5120); sb();
      wl[8] = cat(cat(w0, w1), cat(w2, w3));
      H_S(1, 8) H_S(4, 8) H_S(1, 9) H_S(4, 9)
      H_L(6) const u32x2 w4 = tr8(wb + 8192), w5 = tr8(wb + 9216); sb(); H_S(2, 6) const u32x2 w6 = tr8(wb + 12288), w7 = tr8(wb + 13312); sb();
      wl[9] = cat(cat(w4, w5), cat(w6, w7));
    }
    H_L(7) H_A(1, 0) H_K(1) H_S(2, 7) H_A(1, 1) H_L(8) H_A(1, 2) H_S(2, 8) H_A(1, 3) H_S(2, 9) H_C(0) H_K(2) H_L(9) H_C(1)
    H_S(0, 6) H_D(1) H_S(3, 6) H_K(0) H_S(0, 7) H_A(2, 0) H_S(3, 7) H_A(2, 1) H_S(0, 8) H_A(2, 2) H_S(3, 8) H_A(2, 3) H_S(0, 9) H_C(0) H_S(3, 9) H_C(1) H_D(2)
    H_MZ(sx0, KLO(1), 1)
    stampf(c, 5 + 4 * s, 128);
    CYC(t2)
    if (BELL_P_POS == 2 && ringer) ring(c, h >> 3, s);
    asm volatile("s_nop 1" : "+v"(acc[0]));
    H_A(0, 0) H_A(0, 1) H_A(0, 2) H_A(0, 3) H_C(0) H_C(1) H_D(0) H_MZ(sx1, KHI(1), 1)
    if (BELL_P_POS == 5) { st4<SC1>(c.ws, rvo, 0, (unsigned)s); sb(); }
    H_MZ(sm, F[1], 1)
    H_MA(sx0, KLO(2), 2) H_MA(sx1, KHI(2), 2) H_MA(sm, F[2], 2)
    H_MA(sx0, KLO(0), 0) H_MA(sx1, KHI(0), 0) H_MA(sm, F[0], 0)
    asm volatile("s_nop 5" : "+v"(sx0), "+v"(sx1));
    CYC(t3)
    if (BELL_P_POS == 3 && ringer) ring(c, h >> 3, s);
    if (s < NSEG - 1) {
      *(LAS f32x4*)(PW) = sx0; sb(); *(LAS f32x4*)(PW + 64) = sx1; sb(); *(LAS f32x4*)(PW + 128) = sm;
      bar();
      CYC(t4)
      if (__builtin_expect(c.wave >= 2, 1)) {
        const f32x4 p0 = *(LAS f32x4*)PR, p1 = *(LAS f32x4*)(PR + 3328), p2 = *(LAS f32x4*)(PR + 6656), p3 = *(LAS f32x4*)(PR + 9984);
        const f32x4 xa = p0 + p1, xb = p2 + p3;
        CYC(t4a)
        u32x4 o;
        float x0, x1, x2, x3, ml, mm, t, d;
        asm volatile(
            "v_add_f32 %4, %12, %16\n\tv_add_f32 %5, %13, %17\n\tv_add_f32 %6, %14, %18\n\tv_add_f32 %7, %15, %19\n\t"
            "v_max3_f32 %8, %4, %5, %6\n\tv_max_f32 %8, %8, %7\n\t"
            "v_mul_f32 %10, %20, %8\n\tv_fma_f32 %4, %4, %20, -%10\n\t"
            "v_max_f32_dpp %9, %8, %8 row_ror:8 row_mask:0xf bank_mask:0xf\n\t"
            "v_fma_f32 %5, %5, %20, -%10\n\tv_fma_f32 %6, %6, %20, -%10\n\t"
            "v_max_f32_dpp %9, %9, %9 row_ror:4 row_mask:0xf bank_mask:0xf\n\t"
            "v_fma_f32 %7, %7, %20, -%10\n\tv_exp_f32 %4, %4\n\t"
            "v_max_f32_dpp %9, %9, %9 row_ror:2 row_mask:0xf bank_mask:0xf\n\t"
            "v_exp_f32 %5, %5\n\tv_exp_f32 %6, %6\n\t"
            "v_max_f32_dpp %9, %9, %9 row_ror:1 row_mask:0xf bank_mask:0xf\n\t"
            "v_exp_f32 %7, %7\n\t"
            "v_fma_f32 %11, %9, -%20, %10\n\t"
            "v_add_f32 %8, %4, %5\n\t"
            "v_exp_f32 %11, %11\n\t"
            "v_add_f32 %9, %6, %7\n\t"
            "v_add_f32 %8, %8, %9\n\t"
            "v_mul_f32 %10, %11, %21\n\t"
            "v_mul_f32 %8, %8, %10\n\t"
            "v_mul_f32 %4, %4, %11\n\tv_mul_f32 %5, %5, %11\n\t"
            "v_add_f32_dpp %8, %8, %8 row_ror:8 row_mask:0xf bank_mask:0xf\n\t"
            "v_mul_f32 %6, %6, %11\n\tv_mul_f32 %7, %7, %11\n\t"
            "v_add_f32_dpp %8, %8, %8 row_ror:4 row_mask:0xf bank_mask:0xf\n\t"
            "s_nop 1\n\t"
            "v_add_f32_dpp %8, %8, %8 row_ror:2 row_mask:0xf bank_mask:0xf\n\t"
            "s_nop 1\n\t"
            "v_add_f32_dpp %8, %8, %8 row_ror:1 row_mask:0xf bank_mask:0xf\n\t"
            "v_rcp_f32 %8, %8\n\t"
            "s_nop 0\n\t"
            "v_mul_f32 %4, %4, %8\n\tv_mul_f32 %5, %5, %8\n\tv_mul_f32 %6, %6, %8\n\tv_mul_f32 %7, %7, %8\n\t"
            "v_cvt_pk_bf16_f32 %9, %4, %5\n\tv_cvt_pk_bf16_f32 %10, %6, %7\n\t"
            "s_nop 0\n\t"
            "v_min_u32_dpp %0, %9, %22 quad_perm:[0,0,2,2] row_mask:0xf bank_mask:0xf\n\t"
            "v_min_u32_dpp %2, %9, %22 quad_perm:[1,1,3,3] row_mask:0xf bank_mask:0xf\n\t"
            "v_min_u32_dpp %1, %10, %22 quad_perm:[0,0,2,2] row_mask:0xf bank_mask:0xf\n\t"
            "v_min_u32_dpp %3, %10, %22 quad_perm:[1,1,3,3] row_mask:0xf bank_mask:0xf"
            : "=&v"(o.x), "=&v"(o.y), "=&v"(o.z), "=&v"(o.w), "=&v"(x0), "=&v"(x1), "=&v"(x2), "=&v"(x3), "=&v"(ml), "=&v"(mm), "=&v"(t), "=&v"(d)
            : "v"(xa[0]), "v"(xa[1]), "v"(xa[2]), "v"(xa[3]), "v"(xb[0]), "v"(xb[1]), "v"(xb[2]), "v"(xb[3]),
              "s"(0.07216878364870322f * 1.4426950408889634f), "v"(lmask), "v"(0xFFFFFFFEu));
        CYC(t4b)
        st16<SC1>(c.ws, pvo, 0, o);
        stampf(c, 6 + 4 * s, 128);
        CYC(t5)
        if (H_D2) __builtin_amdgcn_s_sleep(H_D2);
        if (n < 10 && s + 2 < NSEG - 1) st16<SC1>(c.ws, xr_phys(L_P + (unsigned)(s + 2) * P_SZ + plo, n & 1), 0, u32x4{SENT, SENT, SENT, SENT});
      } else {
        if (BELL_P_POS == 4 && ringer) ring(c, h >> 3, s);
        if (H_D0) __builtin_amdgcn_s_sleep(H_D0);
        dma_kx(s + 1);
        if (H_D1) __builtin_amdgcn_s_sleep(H_D1);
      }
    }
#pragma unroll
    for (int i = 0; i < 3; ++i) st8<SC1>(c.ws, kmvo, OFF_KM + (unsigned)(s * NH + h) * 3072 + (unsigned)i * 1024, nosent(F[i]));
    if (KM_BELL && c.tid == 0) st4<SC1>(c.ws, FL_KM + FS * h, 0, (unsigned)(s + 1));
    if (s + 2 < NSEG) {
#pragma unroll
      for (int i = 0; i < 3; ++i) st8<SC1>(c.ws, kmvo, OFF_KM + (unsigned)((s + 2) * NH + h) * 3072 + (unsigned)i * 1024, u32x2{SENT, SENT});
    }
    __builtin_amdgcn_s_sleep(H_SLEEP);
  }
}

__device__ __forceinline__ void role_V(const Params& p, const Ctx& c, int h) {
  u32x8 Wv[10][2];
  u32x8 Wo[10][3];
  unsigned tabv;
  {
    rsrc_t rv = mk_rsrc(p.Wv, DM * NH * DH * 2), ro = mk_rsrc(p.Wo, DM * NH * DH * 2);
    const unsigned srcv = stage_src<1>(c, NH * DH), srco = stage_src<1>(c, DM), rd = stage_rd(c.lane, c.wave);
    stage_weights<12, 2, 10, 4>(c,
      [&](auto ci, LAS unsigned char* dst) {
        constexpr int id = vmap(ci);
        if constexpr (id < 40) {
          constexpr int t = id / 4, pn = (id % 4) / 2, a = id % 2;
          dma16<0>(rv, dst, srcv, (unsigned)(((64 * t + 32 * a) * NH * DH + h * DH + pn * 64) * 2));
        } else if constexpr (id < 100) {
          constexpr int f = (id - 40) / 2, a = id % 2, t = f / 10, pn = f % 10;
          dma16<0>(ro, dst, srco, (unsigned)(((h * DH + 64 * t + 32 * a) * DM + pn * 64) * 2));
        } else {
          constexpr int sp = id - 100;
          dma16<0>(rv, dst, srcv, (unsigned)(((32 * sp) * NH * DH + h * DH + 128) * 2));
        }
      },
      [&](auto ci, LAS unsigned char* chunk) {
        constexpr int id = vmap(ci);
        if constexpr (id % 2 == 1) {
          u32x8 f = cat(stage_frag(chunk - 4096 + rd), stage_frag(chunk + rd));
          if constexpr (id < 40) Wv[id / 4][(id % 4) / 2] = f;
          else Wo[((id - 40) / 2) % 10][((id - 40) / 2) / 10] = f;
        }
      },
      [&](auto ci) {
        constexpr int id = vmap(ci);
        if constexpr (id % 2 == 1) {
          if constexpr (id < 36) pin_v(Wv[id / 4][(id % 4) / 2]);
          else if constexpr (id < 40) pin_a(Wv[id / 4][(id % 4) / 2]);
          else pin_a(Wo[((id - 40) / 2) % 10][((id - 40) / 2) / 10]);
        }
      },
      [&](auto ci) { if constexpr (ci == 80) tabv = ld_table(c); });
  }
  Sides sd;
  get_sides(c, sd, tabv);
  LAS unsigned char* A = c.lds + c.lane * 16;
  const ResW WR = res_base(c);
  const int idx = (c.lane & 2) ? 0xEEEE : 0x4444;
  const int g = c.lane >> 4, n = c.lane & 15;
  LAS unsigned char* TW = c.lds + 10240 + c.wave * 256 + ((n & 7) * 4 + g) * 8 + (n >> 3) * 4;
  LAS unsigned char* TR = c.lds + 10240 + g * 256 + n * 8;
  unsigned nfail = 0;
  const Gath ga = gather_setup(c, true, sd.me);
  unsigned mj[3], ms[3];
#pragma unroll
  for (int u = 0; u < 3; ++u) {
    const int pn = u < 2 ? 4 * u + g : 8 + (g & 1);
    mj[u] = (unsigned)(4 * pn + c.wave);
    ms[u] = pz_side(sd, (int)mj[u]);
  }
  stamp(c, 1);
  for (int s = 0; s < NSEG; ++s) {
    unsigned mvo[3];
#pragma unroll
    for (int u = 0; u < 3; ++u) {
      mvo[u] = m_phys((unsigned)s, mj[u], ms[u]) + (unsigned)(h * 256 + n * 16);
      if (u == 2 && g >= 2) mvo[u] = 0x80000000u;
      pin_v(mvo[u]);
    }
    f32x4 acc[3] = {};
    constexpr int NW = V_NW, NA = V_NA;
    u32x8 wl[NW];
#pragma unroll
    for (int i = 0; i < NW; ++i) wl[i] = res_frag(WR, i);
#pragma unroll
    for (int i = 0; i < NW; ++i) pin_v(wl[i]);
#pragma unroll
    for (int i = 0; i < 3; ++i) pin_v(acc[i]);
    gather_mem(c, ga, s, sd.me, nfail);
    stampf(c, 4 + 4 * s);
    u32x4 a[NA];
#pragma unroll
    for (int t = 0; t < NA; ++t) a[t] = lds16(A + t * 1024);
    sb();
#pragma unroll
    for (int t = 0; t < 10; ++t) {
      LAS unsigned char* wb = t + NW < 5 ? WR.lo + (t + NW) * 8192 : WR.hi + (t + NW - 5) * 8192;
      acc[2] = smfma(a[t % NA], wl[t % NW], acc[2], idx);
      sb();
      if (t + NW < 10) {
        const u32x2 w0 = tr8(wb), w1 = tr8(wb + 1024);
        wl[t % NW][0] = w0.x; wl[t % NW][1] = w0.y; wl[t % NW][2] = w1.x; wl[t % NW][3] = w1.y;
      }
      sb();
      acc[0] = smfma(a[t % NA], Wv[t][0], acc[0], idx);
      sb();
      if (t + NW < 10) {
        const u32x2 w2 = tr8(wb + 4096), w3 = tr8(wb + 5120);
        wl[t % NW][4] = w2.x; wl[t % NW][5] = w2.y; wl[t % NW][6] = w3.x; wl[t % NW][7] = w3.y;
      }
      sb();
      acc[1] = smfma(a[t % NA], Wv[t][1], acc[1], idx);
      sb();
      if (t + NA < 10) a[t % NA] = lds16(A + (t + NA) * 1024);
      sb();
    }
    f32x4 m[10] = {};
    *(LAS unsigned*)(TW + 2048) = pk(acc[2][0] + acc[2][2], acc[2][1] + acc[2][3]);
    *(LAS unsigned*)(TW) = pk(acc[0][0] + acc[0][2], acc[0][1] + acc[0][3]);
    *(LAS unsigned*)(TW + 1024) = pk(acc[1][0] + acc[1][2], acc[1][1] + acc[1][3]);
    sb();
#pragma unroll
    for (int i = 0; i < 4; ++i) pin_v(m[i]);
    bar();
    u32x4 a2[3];
#pragma unroll
    for (int t = 0; t < 3; ++t) a2[t] = cat(tr8(TR + t * 1024), tr8(TR + t * 1024 + 128));
    sb();
#pragma unroll
    for (int i = 4; i < 10; ++i) pin_v(m[i]);
    sb();
    float v0, v1; unsigned x[10];
    auto xpose = [&](int u) {
      const int i2 = u < 2 ? 4 * u + 2 : 8, i3 = u < 2 ? 4 * u + 3 : 9;
      auto r0 = __builtin_amdgcn_permlane32_swap(x[4 * u], x[i2], false, false);
      auto r1 = __builtin_amdgcn_permlane32_swap(x[4 * u + 1], x[i3], false, false);
      auto q0 = __builtin_amdgcn_permlane16_swap(r0[0], r1[0], false, false);
      auto q1 = __builtin_amdgcn_permlane16_swap(r0[1], r1[1], false, false);
      st16<SC1>(c.ws, mvo[u], 0, u32x4{q0[0], q0[1], q1[0], q1[1]});
    };
    sfor<0, 36>([&](auto ic) {
      constexpr int i = ic, tl = (i / 6) * 2 + (i & 1), t = (i % 6) / 2;
      if constexpr (i < 30) { m[tl] = smfma(a2[t], Wo[tl][t], m[tl], idx); sb(); }
      else if constexpr (i == 30) { asm volatile("s_nop 3" : "+v"(m[8]), "+v"(m[9])); sb(); }
      if constexpr (i >= 8 || (i >= 6 && false)) {
        constexpr int k = i - 8, pr = k / 6, w = k % 6;
        (void)pr; (void)w;
      }
      constexpr int k = i - 7;
      if constexpr (k >= 0) {
        constexpr int pr = k / 6, w = k % 6, ta = 2 * pr + (w >= 3 ? 1 : 0), ww = w % 3;
        if constexpr (pr < 5) {
          if constexpr (ww == 0) { v0 = add1(m[ta][0], m[ta][2]); sb(); }
          if constexpr (ww == 1) { v1 = add1(m[ta][1], m[ta][3]); sb(); }
          if constexpr (ww == 2) { x[ta] = pks(v0, v1); sb(); }
          if constexpr (w == 5 && (pr & 1)) { xpose(pr >> 1); sb(); }
        }
      }
    });
    x[9] = pks(v0, v1);
    xpose(2);
    stampf(c, 6 + 4 * s);
    __builtin_amdgcn_s_sleep(V_SLEEP);
  }
}

__device__ __forceinline__ void role_PZ(const Params& p, const Ctx& c, int j) {
  Sides sd;
  get_sides(c, sd);
  const unsigned mside = pz_side(sd, j);
  LAS unsigned char* R = c.lds;
  const int idx = (c.lane & 2) ? 0xEEEE : 0x4444;
  const int g = c.lane >> 4, n = c.lane & 15;
  int uready = -1;
  const unsigned l16 = (unsigned)c.lane * 16;
  const unsigned pmvo = (unsigned)(2 * g * 128 + sp_row(c.lane) * 16 + sp_half(c.lane) * 8);
  const u32x4 sv = {SENT, SENT, SENT, SENT};
  LAS float* rw = (LAS float*)(R + c.wave * 512) + 2 * g * 16 + n;
  LAS unsigned char* rp = R + (((c.lane >> 2) & 7) * 16 + (c.lane >> 5) * 8 + (c.lane & 3) * 2) * 4;
  const unsigned olo = (unsigned)(j * 256 + (c.lane >> 2) * 16);
  const bool ringer = USE_BELL_M && j % 10 == 0 && c.wave == 3;
  const unsigned bvo = bell_vo(c, 0, sd.me);
  const unsigned rvo = (ringer && c.lane < 2) ? OFF_DB + (unsigned)(4 + j / 10) * FS + (unsigned)c.lane * 8192u : 0x80000000u;
  unsigned nfail = 0; (void)nfail;
  bar();
  for (int s = 0; s < NSEG - 1; ++s) {
    if (uready < s) uready = wait_flags<2>(c, FL_U + 8 * FS * c.wave, 8, s);
    u32x4 u[8];
#pragma unroll
    for (int i = 0; i < 8; ++i) u[i] = ld16<SC1>(c.ws, l16, OFF_U + (unsigned)s * U_SZ + (unsigned)(j * 32 + 8 * c.wave + i) * 1024);
    u32x4 pa[4], mb[2]; u32x2 pm[2];
    const unsigned lp = L_P + (unsigned)s * P_SZ;
    unsigned sop = xr_phys(lp + (unsigned)c.wave * 4096, sd.me), som5 = xr_phys(lp + 16384 + (unsigned)c.wave * 1024, sd.me);
    pin_s(sop); pin_s(som5);
    unsigned som = m_phys((unsigned)s, (unsigned)j, mside) + (unsigned)c.wave * 2048;
    unsigned ovo = xr_phys(L_MEM + (unsigned)(s + 1) * MEM_SZ + olo, c.lane & 1);
    if ((c.lane & 2) || c.wave != 0) ovo = 0x80000000u;
    f32x4 acc;
    LAS float* rwb = rw + (s & 1) * 512;
    LAS unsigned char* rpb = rp + (s & 1) * 2048;
    pin_v(ovo); pin_v(rwb); pin_v(rpb);
    unsigned sp = 0;
    pin_s(som);
    wait_bells(c, bvo, s);
    if (BELL_P_GAP0) __builtin_amdgcn_s_sleep(BELL_P_GAP0);
    mb[0] = ld16<SC1>(c.ws, l16, som); mb[1] = ld16<SC1>(c.ws, l16 + 1024, som);
    sb();
    if (BELL_P_GAP) __builtin_amdgcn_s_sleep(BELL_P_GAP);
    if (BELL_M_POS == 1 && ringer) ring(c, 4 + j / 10, s + 1);
    for (;;) {
      pm[0] = ld8<SC1>(c.ws, pmvo, som5); pm[1] = ld8<SC1>(c.ws, pmvo + 128, som5);
#pragma unroll
      for (int i = 0; i < 4; ++i) pa[i] = ld16<SC1>(c.ws, l16 + i * 1024, sop);
      sb();
      acc = f32x4{0.f, 0.f, 0.f, 0.f};
      pin_v(acc);
      acc = smfma(cat(pm[0], pm[1]), cat(mb[0], mb[1]), acc, idx);
      unsigned m = umax4(0u, cat(pm[0], pm[1]));
      m = umax4(umax4(m, mb[0]), mb[1]);
      sb();
#pragma unroll
      for (int i = 0; i < 4; ++i) {
        if (BELL_M_POS == 4 + i) { st4<SC1>(c.ws, rvo, 0, (unsigned)(s + 1)); sb(); }
        acc = smfma(pa[i], cat(u[2 * i], u[2 * i + 1]), acc, idx); m = umax4(m, pa[i]); sb();
      }
      if (!__any(m == SENT)) break;
      spin_check(c, sp);
      mb[0] = ld16<SC1>(c.ws, l16, som); mb[1] = ld16<SC1>(c.ws, l16 + 1024, som);
    }
    stampf(c, 4 + 4 * s);
    rwb[0] = acc[0] + acc[2]; rwb[16] = acc[1] + acc[3];
    sb();
    if (BELL_M_POS == 2) st4<SC1>(c.ws, rvo, 0, (unsigned)(s + 1));
    bar();
    if (BELL_M_POS == 3 && ringer) ring(c, 4 + j / 10, s + 1);
    {
      const f32x2 v = (*(LAS f32x2*)(rpb) + *(LAS f32x2*)(rpb + 512)) + (*(LAS f32x2*)(rpb + 1024) + *(LAS f32x2*)(rpb + 1536));
      const unsigned d = pks(v[0], v[1]);
      st16<SC1>(c.ws, ovo, 0, u32x4{dppu<0x00>(d), dppu<0x55>(d), dppu<0xAA>(d), dppu<0xFF>(d)});
      stamp(c, 6 + 4 * s);
    }
    __builtin_amdgcn_s_sleep(PZ_SLEEP);
    if (c.wave == 0 && !(c.lane & 2) && s + 3 < NSEG) st16<SC1>(c.ws, xr_phys(L_MEM + (unsigned)(s + 3) * MEM_SZ + olo, c.lane & 1), 0, sv);
    if (s + 2 < NSEG) {
      const unsigned sa = m_phys((unsigned)(s + 2), (unsigned)j, mside) + (unsigned)c.wave * 2048;
      st16<SC1>(c.ws, l16, sa, sv); st16<SC1>(c.ws, l16 + 1024, sa, sv);
    }
  }
}

__device__ __forceinline__ void load_proj_w(const Ctx& c, const void* Wm, int h, u32x4 (&W)[3][20]) {
  rsrc_t rw = mk_rsrc(Wm, DM * NH * DH * 2);
  const unsigned src = stage_src<0>(c, NH * DH), rd = stage_rd(c.lane, c.wave);
  stage_weights<5, 0, 12, 3>(c,
    [&](auto ci, LAS unsigned char* dst) {
      constexpr int id = ci, kap = id / 3, pn = id % 3;
      dma16<0>(rw, dst, src, (unsigned)(((32 * kap) * NH * DH + h * DH + pn * 64) * 2));
    },
    [&](auto ci, LAS unsigned char* chunk) {
      constexpr int id = ci;
      W[id % 3][id / 3] = stage_frag(chunk + rd);
    },
    [&](auto ci) { constexpr int id = ci; pin_a(W[id % 3][id / 3]); }, [](auto) {});
}
__device__ __forceinline__ void dma_hb(const Ctx& c, int s, LAS unsigned char* slot) {
#pragma unroll
  for (int i = 0; i < 10; ++i)
    dma16<SC1>(c.ws, slot + (c.wave * 10 + i) * 1024, (unsigned)c.lane * 16, OFF_HB + (unsigned)s * HB_SZ + (unsigned)(c.wave * 10 + i) * 1024);
}
template <bool WA, bool WFIRST, bool ZERO>
__device__ __forceinline__ void mfma_v(f32x4& d, const u32x4& w, const u32x4& x) {
  if constexpr (ZERO) {
    if constexpr (WA && WFIRST) asm volatile("v_mfma_f32_16x16x32_bf16 %0, %1, %2, 0" : "=&v"(d) : "a"(w), "v"(x));
    else if constexpr (WA) asm volatile("v_mfma_f32_16x16x32_bf16 %0, %2, %1, 0" : "=&v"(d) : "a"(w), "v"(x));
    else if constexpr (WFIRST) asm volatile("v_mfma_f32_16x16x32_bf16 %0, %1, %2, 0" : "=&v"(d) : "v"(w), "v"(x));
    else asm volatile("v_mfma_f32_16x16x32_bf16 %0, %2, %1, 0" : "=&v"(d) : "v"(w), "v"(x));
  } else {
    if constexpr (WA && WFIRST) asm volatile("v_mfma_f32_16x16x32_bf16 %0, %1, %2, %0" : "+v"(d) : "a"(w), "v"(x));
    else if constexpr (WA) asm volatile("v_mfma_f32_16x16x32_bf16 %0, %2, %1, %0" : "+v"(d) : "a"(w), "v"(x));
    else if constexpr (WFIRST) asm volatile("v_mfma_f32_16x16x32_bf16 %0, %1, %2, %0" : "+v"(d) : "v"(w), "v"(x));
    else asm volatile("v_mfma_f32_16x16x32_bf16 %0, %2, %1, %0" : "+v"(d) : "v"(w), "v"(x));
  }
}
__device__ __forceinline__ void mfma_vn(f32x4& d, const u32x4& a, const u32x4& b) {
  asm volatile("s_nop 1\n\tv_mfma_f32_16x16x32_bf16 %0, %1, %2, %0" : "+v"(d) : "v"(a), "v"(b));
}
__device__ __forceinline__ void mfma_fence() { sb(); asm volatile("s_nop 7"); sb(); }

template <int NT, int K0, int K1, class Hook>
__device__ __forceinline__ void gemm_part(const u32x4 (&W)[NT][20], LAS unsigned char* hb, f32x4 (&acc)[NT][2], Hook&& hook) {
  u32x4 b[K1 - K0][2];
#pragma unroll
  for (int k = 0; k < 2; ++k) { b[k][0] = lds16(hb + (2 * (K0 + k)) * 1024); b[k][1] = lds16(hb + (2 * (K0 + k) + 1) * 1024); }
  sb();
  sfor<0, K1 - K0>([&](auto kc) {
    constexpr int k = kc, ka = K0 + k;
    sfor<0, 2 * NT>([&](auto ic) {
      constexpr int t = ic / NT, i = ic % NT;
      mfma_v<(ka * NT + i < 64), true, ka == 0>(acc[i][t], W[i][ka], b[k][t]);
      sb();
      if constexpr (ic < 2 && k + 2 < K1 - K0) { b[k + 2][ic] = lds16(hb + (2 * (ka + 2) + ic) * 1024); sb(); }
      if constexpr (ic == 2) { hook(kc); sb(); }
    });
  });
}
__device__ __forceinline__ void dma_hb1(const Ctx& c, int s, LAS unsigned char* slot, int i) {
  dma16<SC1>(c.ws, slot + (c.wave * 10 + i) * 1024, (unsigned)c.lane * 16, OFF_HB + (unsigned)s * HB_SZ + (unsigned)(c.wave * 10 + i) * 1024);
}
__device__ __forceinline__ u32x4 pack2(const f32x4& a, const f32x4& b) { return u32x4{pk(a[0], a[1]), pk(a[2], a[3]), pk(b[0], b[1]), pk(b[2], b[3])}; }

__device__ __forceinline__ void need_hb(const Ctx& c, unsigned long long& mask, int s) {
  if (c.wave != 0 || ((mask >> s) & 1ull)) return;
  unsigned sp = 0;
  for (;;) {
    mask = __ballot(ld4<SC1>(c.ws, FL_H + FS * (unsigned)c.lane, 0) != SENT);
    if ((mask >> s) & 1ull) return;
    __builtin_amdgcn_s_sleep(4);
    spin_check(c, sp);
  }
}

__device__ __forceinline__ void role_KV(const Params& p, const Ctx& c, int h, bool isV) {
  u32x4 W[3][20];
  load_proj_w(c, isV ? p.Wv : p.Wk, h, W);
  stamp(c, 1);
  unsigned long long hm = 0;
  need_hb(c, hm, 0);
  bar();
  stamp(c, 2);
  const unsigned obase = isV ? OFF_VX : OFF_KX, fl = (isV ? FL_V : FL_K) + FS * h;
  dma_hb(c, 0, c.lds);
  need_hb(c, hm, 1);
  bar();
  dma_hb(c, 1, c.lds + HB_SZ);
  auto kst = [&](int s, int i, const f32x4 (&a)[3][2]) {
    st16<SC1>(c.ws, (unsigned)c.lane * 16, obase + (unsigned)(s * NH + h) * KX_SZ + (unsigned)(4 * i + c.wave) * 1024, pack2(a[i][0], a[i][1]));
  };
  f32x4 accA[3][2], accB[3][2];
#pragma unroll 1
  for (int s = 0; s < 2; ++s) {
    need_hb(c, hm, s + 2);
    if (s == 0) wait_vm<10>();
    bar();
    gemm_part<3, 0, 20>(W, c.lds + s * HB_SZ + c.lane * 16, accA, [&](auto k) { if constexpr (k < 10) dma_hb1(c, s + 2, c.lds + (s == 0 ? 2 : 0) * HB_SZ, k); });
    mfma_fence();
#pragma unroll
    for (int i = 0; i < 3; ++i) kst(s, i, accA);
    publish(c, fl, s);
    stamp(c, 4 + s);
  }
  int sl = 2;
  auto seg = [&](int s, f32x4 (&cur)[3][2], const f32x4 (&prev)[3][2], bool hasprev) {
    const int s2 = min(s + 2, NSEG - 1);
    need_hb(c, hm, s2);
    wait_vm<10>();
    bar();
    if (c.tid == 0 && s >= 4) st4<SC1>(c.ws, fl, 0, (unsigned)(s - 2));
    const int sl2 = sl == 0 ? 2 : sl - 1;
    gemm_part<3, 0, 20>(W, c.lds + sl * HB_SZ + c.lane * 16, cur, [&](auto k) {
      if constexpr (k >= 2 && k < 5) { if (hasprev) kst(s - 1, k - 2, prev); }
      if constexpr (k >= 6 && k < 16) dma_hb1(c, s2, c.lds + sl2 * HB_SZ, k - 6);
    });
    stamp(c, 4 + s);
    sl = sl == 2 ? 0 : sl + 1;
  };
#pragma unroll 1
  for (int s = 2; s < NSEG; s += 2) { seg(s, accA, accB, s > 2); seg(s + 1, accB, accA, true); }
  mfma_fence();
#pragma unroll
  for (int i = 0; i < 3; ++i) kst(NSEG - 1, i, accB);
  publish(c, fl, NSEG - 1);
}

__device__ __forceinline__ void role_U(const Params& p, const Ctx& c, int h) {
  u32x4 W[10][6];
  {
    rsrc_t ro = mk_rsrc(p.Wo, DM * NH * DH * 2);
    const unsigned src = stage_src<2>(c, DM), rd = stage_rd(c.lane, c.wave);
    stage_weights<6, 0, 10, 4>(c,
      [&](auto ci, LAS unsigned char* dst) {
        constexpr int id = ci, a = id / 10, pn = id % 10;
        dma16<0>(ro, dst, src, (unsigned)(((h * DH + 32 * a) * DM + pn * 64) * 2));
      },
      [&](auto ci, LAS unsigned char* chunk) {
        constexpr int id = ci;
        W[id % 10][id / 10] = stage_frag(chunk + rd);
      },
      [&](auto ci) { constexpr int id = ci; pin_a(W[id % 10][id / 10]); }, [](auto) {});
  }
  const int g = c.lane >> 4, rp = c.lane & 15;
  const int gs = rp >> 2, i4 = rp & 3;
  const unsigned aoff = (unsigned)((g * 16 + 8 * (i4 >> 1) + 4 * (gs & 1) + (i4 & 1)) * 16 + (gs >> 1) * 8);
  const unsigned uvo = (unsigned)((h >> 1) * 2048 + (g >> 1) * 1024 + ((h & 1) * 2 + (g & 1)) * 256 + rp * 16);
  int vready = -1, issued = -1;
  stamp(c, 1);
  auto dma_v = [&](int s) {
    LAS unsigned char* slot = c.lds + (s % 3) * KX_SZ;
#pragma unroll
    for (int i = 0; i < 3; ++i)
      dma16<SC1>(c.ws, slot + (c.wave * 3 + i) * 1024, (unsigned)c.lane * 16, OFF_VX + (unsigned)(s * NH + h) * KX_SZ + (unsigned)(c.wave * 3 + i) * 1024);
  };
  for (int s = 0; s < NSEG; ++s) {
    if (issued < s) {
      unsigned sp = 0;
      while ((vready = (int)__builtin_amdgcn_readfirstlane(ld4<SC1>(c.ws, FL_V + FS * h, 0))) < s) { __builtin_amdgcn_s_sleep(2); spin_check(c, sp); }
      dma_v(s); issued = s;
      wait_vm<0>();
    } else {
      wait_vm<10>();
    }
    bar();
    if (c.tid == 0 && s >= U_EAGER + 2) st4<SC1>(c.ws, FL_U + FS * h, 0, (unsigned)(s - 2));
    if (s + 1 < NSEG && vready >= s + 1) { dma_v(s + 1); issued = s + 1; }
    const unsigned fv = ld4<SC1>(c.ws, vready < min(s + 2, NSEG - 1) ? FL_V + FS * h : 0x80000000u, 0);
    LAS unsigned char* sa0 = c.lds + (s % 3) * KX_SZ + aoff;
    LAS unsigned char* sa1 = sa0 + 32;
    pin_v(sa0); pin_v(sa1);
    u32x4 a[2][6];
#pragma unroll
    for (int k = 0; k < 6; ++k) {
      a[0][k] = cat(*(LAS u32x2*)(sa0 + (2 * k) * 1024), *(LAS u32x2*)(sa0 + (2 * k + 1) * 1024));
      a[1][k] = cat(*(LAS u32x2*)(sa1 + (2 * k) * 1024), *(LAS u32x2*)(sa1 + (2 * k + 1) * 1024));
    }
    sb();
    f32x4 d[10][2];
    u32x4 o;
    sfor<0, 10>([&](auto pc) {
      constexpr int pn = pc;
      sfor<0, 12>([&](auto ic) {
        constexpr int k = ic / 2, tp = ic % 2;
        mfma_v<true, false, k == 0>(d[pn][tp], W[pn][k], a[tp][k]);
        sb();
        if constexpr (pn > 0 && ic >= 7 && ic < 11) { constexpr int q = ic - 7; o[q] = pk(d[pn - 1][q / 2][2 * (q % 2)], d[pn - 1][q / 2][2 * (q % 2) + 1]); sb(); }
        if constexpr (pn > 0 && ic == 11) {
          st16<SC1>(c.ws, uvo, OFF_U + (unsigned)s * U_SZ + (unsigned)(4 * (pn - 1) + c.wave) * 32768, o);
          sb();
        }
      });
    });
    mfma_fence();
    st16<SC1>(c.ws, uvo, OFF_U + (unsigned)s * U_SZ + (unsigned)(36 + c.wave) * 32768, pack2(d[9][0], d[9][1]));
    {
      unsigned fvv = fv;
      asm volatile("s_waitcnt vmcnt(10)" : "+v"(fvv)::"memory");
      vready = max(vready, (int)__builtin_amdgcn_readfirstlane(fvv));
    }
    if (s < U_EAGER) {
      if (issued == s && vready >= s + 1) { dma_v(s + 1); issued = s + 1; }
      publish(c, FL_U + FS * h, s);
    }
    stamp(c, 4 + s);
  }
  publish(c, FL_U + FS * h, NSEG - 1);
}

__device__ __forceinline__ void role_Q(const Params& p, const Ctx& c, int h) {
  const int g = c.lane >> 4, n = c.lane & 15;
  const int qt = c.wave * 8 + (c.lane >> 3), tau = qt >> 4, rho = qt & 15, it0 = c.lane & 7, it = min(it0, 4);
  const int T = it >> 1;
  const bool dead = (it0 > 4) || (tau == 0 && it >= 2 && it < 4);
  const unsigned pvo = dead ? 0x80000000u : it < 4 ? (unsigned)(((h >> 1) * 3 + tau + (tau ? T : 0)) * 1024 + (((h & 1) * 2 + (it & 1)) * 16 + rho) * 16)
                                                   : (unsigned)(65536 + ((h >> 2) * 2 + tau) * 1024 + ((h & 3) * 16 + rho) * 16);
  const u32x4 sv = {SENT, SENT, SENT, SENT};
  st16<SC1>(c.ws, pvo, OFF_PX, sv); st16<SC1>(c.ws, pvo, OFF_PX + PX_SZ, sv);
  u32x4 W[3][20];
  load_proj_w(c, p.Wq, h, W);
  if (c.tid == 0) st4<SC1>(c.ws, FL_PX + FS * h, 0, 0u);
  LAS unsigned char* PART = c.lds + 2 * HB_SZ;
  constexpr int QS = 336, WS_ = 16 * QS, BS_ = 4 * WS_;
  const int tile = it == 4 ? (tau ? 4 : 1) : (tau ? 2 + T : 0);
  const int ga = it == 4 ? 0 : (it & 1), gb = it == 4 ? 1 : (it & 1) + 2;
  const int pao = rho * QS + tile * 64 + ga * 16, pbo = rho * QS + tile * 64 + gb * 16;
  LAS unsigned char* PW = PART + c.wave * WS_ + n * QS + g * 16;
  f32x4 ba, bb;
#pragma unroll
  for (int r = 0; r < 4; ++r) {
    ba[r] = (dead || (it < 4 && 16 * T + 4 * ga + r > qt)) ? -__builtin_inff() : 0.f;
    bb[r] = (dead || (it < 4 && 16 * T + 4 * gb + r > qt)) ? -__builtin_inff() : 0.f;
  }
  if (c.wave == 0) wait_flags<8>(c, FL_H, 64, 0);
  if (c.wave == 1) wait_flags<8>(c, FL_KM + FS * h, 1, 0);
  bar();
  dma_hb(c, 0, c.lds); dma_hb(c, 1, c.lds + HB_SZ);
  wait_vm<10>();
  bar();
  u32x4 q[3], kx[3];
  int kready = -1;
  auto load_kx = [&](int s, int i) {
    kx[i] = ld16<SC1>(c.ws, (unsigned)c.lane * 16, OFF_KX + (unsigned)(s * NH + h) * KX_SZ + (unsigned)(4 * i + c.wave) * 1024);
  };
  auto xscores = [&](int s) {
    f32x4 s0 = {}, s2 = {}, s3 = {};
#pragma unroll
    for (int i = 0; i < 3; ++i) {
      const u32x2 q0 = {q[i].x, q[i].y}, q1 = {q[i].z, q[i].w}, k0 = {kx[i].x, kx[i].y}, k1 = {kx[i].z, kx[i].w};
      s0 = mfma16(k0, q0, s0); s2 = mfma16(k0, q1, s2); s3 = mfma16(k1, q1, s3);
    }
    LAS unsigned char* pw = PW + (s & 1) * BS_;
    *(LAS f32x4*)(pw) = s0; *(LAS f32x4*)(pw + 128) = s2; *(LAS f32x4*)(pw + 192) = s3;
  };
  {
    f32x4 acc[3][2];
    gemm_part<3, 0, 20>(W, c.lds + c.lane * 16, acc, [](auto) {});
    mfma_fence();
#pragma unroll
    for (int i = 0; i < 3; ++i) q[i] = pack2(acc[i][0], acc[i][1]);
    kready = wait_flags<8>(c, FL_K + FS * h, 1, 0);
#pragma unroll
    for (int i = 0; i < 3; ++i) load_kx(0, i);
    xscores(0);
  }
  const unsigned kmvo = (unsigned)(g * 64 + (n & 7) * 8 + c.wave * 256);
#pragma unroll 1
  for (int s = 0; s < NSEG; ++s) {
    u32x2 km[3];
    unsigned sp = 0;
    CYCW(u0)
    if (KM_BELL) doorbell<3, KM_DELAY>(c, FL_KM + FS * h, s + 1);
    for (;;) {
#pragma unroll
      for (int i = 0; i < 3; ++i) km[i] = ld8<SC1>(c.ws, kmvo, OFF_KM + (unsigned)(s * NH + h) * 3072 + (unsigned)i * 1024);
      if (!__any(umax3(umax3(km[0].x, km[0].y, km[1].x), umax3(km[1].y, km[2].x, km[2].y), 0u) == SENT)) break;
      __builtin_amdgcn_s_sleep(3);
      spin_check(c, sp);
    }
    wait_vm<0>();
    CYCW(u1)
    LAS unsigned char* PB = PART + (s & 1) * BS_;
    {
      f32x4 s1 = {}, s4 = {};
#pragma unroll
      for (int i = 0; i < 3; ++i) { s1 = mfma16(km[i], u32x2{q[i].x, q[i].y}, s1); s4 = mfma16(km[i], u32x2{q[i].z, q[i].w}, s4); }
      LAS unsigned char* pw = PW + (s & 1) * BS_;
      *(LAS f32x4*)(pw + 64) = s1; *(LAS f32x4*)(pw + 256) = s4;
    }
    bar();
    CYCW(u2)
    {
      LAS unsigned char* pa = PB + pao;
      LAS unsigned char* pb = PB + pbo;
      f32x4 va = ((*(LAS f32x4*)pa + ba) + *(LAS f32x4*)(pa + WS_)) + (*(LAS f32x4*)(pa + 2 * WS_) + *(LAS f32x4*)(pa + 3 * WS_));
      f32x4 vb = ((*(LAS f32x4*)pb + bb) + *(LAS f32x4*)(pb + WS_)) + (*(LAS f32x4*)(pb + 2 * WS_) + *(LAS f32x4*)(pb + 3 * WS_));
      float m = fmaxf(fmaxf(fmaxf(va[0], va[1]), fmaxf(va[2], va[3])), fmaxf(fmaxf(vb[0], vb[1]), fmaxf(vb[2], vb[3])));
      asm volatile("s_nop 1\n\tv_max_f32_dpp %0, %0, %0 quad_perm:[1,0,3,2] row_mask:0xf bank_mask:0xf\n\t"
                   "s_nop 1\n\tv_max_f32_dpp %0, %0, %0 quad_perm:[2,3,0,1] row_mask:0xf bank_mask:0xf\n\t"
                   "s_nop 1\n\tv_max_f32_dpp %0, %0, %0 row_half_mirror row_mask:0xf bank_mask:0xf" : "+v"(m));
      const float cs = 0.07216878364870322f * 1.4426950408889634f;
      const float mc = -m * cs;
      float l = 0.f;
#pragma unroll
      for (int r = 0; r < 4; ++r) {
        va[r] = __builtin_amdgcn_exp2f(__builtin_fmaf(va[r], cs, mc));
        vb[r] = __builtin_amdgcn_exp2f(__builtin_fmaf(vb[r], cs, mc));
        l += va[r] + vb[r];
      }
      l += dpp<0xB1>(l); l += dpp<0x4E>(l); l += dpp<0x141>(l);
      const float inv = __builtin_amdgcn_rcpf(l);
      st16<SC1>(c.ws, pvo, OFF_PX + (unsigned)s * PX_SZ,
                u32x4{pks(va[0] * inv, va[1] * inv), pks(vb[0] * inv, vb[1] * inv), pks(va[2] * inv, va[3] * inv), pks(vb[2] * inv, vb[3] * inv)});
    }
    if (c.tid == 0) st4<SC1>(c.ws, FL_PX + FS * h, 0, (unsigned)(s + 1));
    CYCW(u3)
    stamp(c, 4 + s);
    if (s + 1 < NSEG) {
      if (s + 2 < NSEG) st16<SC1>(c.ws, pvo, OFF_PX + (unsigned)(s + 2) * PX_SZ, sv);
      if (kready < s + 1) kready = wait_flags<8>(c, FL_K + FS * h, 1, s + 1);
      const int s2 = min(s + 2, NSEG - 1);
      f32x4 acc[3][2];
      gemm_part<3, 0, 20>(W, c.lds + ((s + 1) & 1) * HB_SZ + c.lane * 16, acc, [&](auto k) {
        if constexpr (k < 3) load_kx(s + 1, k);
        if constexpr (k >= 3 && k < 13) dma_hb1(c, s2, c.lds + (s & 1) * HB_SZ, k - 3);
      });
      mfma_fence();
      CYCW(u4)
#pragma unroll
      for (int i = 0; i < 3; ++i) q[i] = pack2(acc[i][0], acc[i][1]);
      xscores(s + 1);
      CYCW(u5)
      CYCOUT(s, u0, u1, u2, u3, u4, u5, u5)
    }
  }
}

constexpr int X1_ARM = 6;
__device__ __forceinline__ void role_PX(const Params& p, const Ctx& c, int jx) {
  LAS float* ga = (LAS float*)c.lds;
  LAS unsigned char* R = c.lds + 4096;
  const int g = c.lane >> 4, n = c.lane & 15;
  const unsigned l16 = (unsigned)c.lane * 16;
  const int nt = c.wave & 1, tau = c.wave >> 1;
  const int col = 32 * jx + 16 * nt + 4 * g;
  const unsigned xo = (unsigned)(((tau * 16 + n) * DM + col) * 2);
  const unsigned x1o = (unsigned)((jx * 2 + tau) * 1024 + ((2 * nt + (g >> 1)) * 16 + n) * 16 + (g & 1) * 8);
#pragma unroll
  for (int i = 0; i < X1_ARM; ++i) st8<SC1>(c.ws, x1o, OFF_X1 + (unsigned)i * HB_SZ, u32x2{SENT, SENT});
  publish(c, FL_X1 + FS * jx, 0);
  Sides sd;
  get_sides(c, sd);
  load_mod(c, 2 * DM, DM, ga, false);
  wait_flags<8>(c, FL_PX + 8 * FS * c.wave, 8, 0);
  bar();
  const f32x4 gav = *(LAS f32x4*)(ga + col);
  rsrc_t rx = mk_rsrc(p.x, 2048 * DM * 2);
  const unsigned ms0 = pz_side(sd, 2 * jx), ms1 = pz_side(sd, 2 * jx + 1);
  int uready = -1;
  auto ld_u = [&](int s, int i) -> u32x4 {
    return ld16<SC1>(c.ws, l16, OFF_U + (unsigned)s * U_SZ + (unsigned)((2 * jx + (i & 1)) * 32 + 8 * c.wave + (i >> 1)) * 1024);
  };
  auto step = [&](int s, const u32x4 (&z)[16], u32x4 (&zn)[16]) {
    const int sn = min(s + 1, NSEG - 1);
    if (uready < sn) uready = wait_flags<8>(c, FL_U + 8 * FS * c.wave, 8, sn);
    const unsigned spx = OFF_PX + (unsigned)s * PX_SZ;
    CYCW(u0)
    doorbell<PX_SLEEP, PX_DELAY>(c, FL_PX, s + 1);
    CYCW(u1)
    const unsigned m0 = m_phys((unsigned)s, (unsigned)(2 * jx), ms0) + (unsigned)c.wave * 2048, m1 = m_phys((unsigned)s, (unsigned)(2 * jx + 1), ms1) + (unsigned)c.wave * 2048;
    u32x4 pa[12], pm[2][2], zm[2][2];
    f32x4 acc[2][2];
    const u32x2 xv = ld8<0>(rx, xo, (unsigned)s * HB_SZ);
    unsigned sp = 0;
    for (;;) {
#pragma unroll
      for (int i = 0; i < 2; ++i) {
        zm[i][0] = ld16<SC1>(c.ws, l16 + i * 1024, m0); zm[i][1] = ld16<SC1>(c.ws, l16 + i * 1024, m1);
#pragma unroll
        for (int t = 0; t < 2; ++t) pm[i][t] = ld16<SC1>(c.ws, l16, spx + 65536 + (unsigned)((2 * c.wave + i) * 2 + t) * 1024);
      }
#pragma unroll
      for (int i = 0; i < 12; ++i) pa[i] = ld16<SC1>(c.ws, l16, spx + (unsigned)(12 * c.wave + i) * 1024);
      sb();
      unsigned m = 0;
      sfor<0, 8>([&](auto ic) {
        constexpr int k = ic >> 2, t = (ic >> 1) & 1, u = ic & 1;
        mfma_v<false, true, k == 0>(acc[t][u], zm[k][t], pm[k][u]);
        sb();
        if constexpr (u == 0) m = umax4(m, zm[k][t]);
        if constexpr (t == 1) m = umax4(m, pm[k][u]);
        sb();
      });
      sfor<0, 24>([&](auto ic) {
        constexpr int f = ic >> 1, t = ic & 1, pr = f / 3, w = f % 3;
        mfma_v<false, true, false>(acc[t][w ? 1 : 0], z[2 * (2 * pr + (w == 2 ? 1 : 0)) + t], pa[f]);
        sb();
        if constexpr (t == 0) m = umax4(m, pa[f]);
        if constexpr (ic >= 8) { if (s + 1 < NSEG) zn[ic - 8] = ld_u(sn, ic - 8); }
        sb();
      });
      if (!__any(m == SENT)) break;
      wait_flags<4>(c, FL_PX + 8 * FS * c.wave, 8, s + 1);
      __builtin_amdgcn_s_sleep(2);
      spin_check(c, sp);
    }
    mfma_fence();
    CYCW(u2)
    LAS unsigned char* Rb = R + (s & 1) * 16384;
#pragma unroll
    for (int t = 0; t < 2; ++t)
#pragma unroll
      for (int u = 0; u < 2; ++u) *(LAS f32x4*)(Rb + c.wave * 4096 + (u * 2 + t) * 1024 + l16) = acc[t][u];
    bar();
    CYCW(u3)
    LAS unsigned char* rp = Rb + c.wave * 1024 + l16;
    const f32x4 v = (*(LAS f32x4*)rp + *(LAS f32x4*)(rp + 4096)) + (*(LAS f32x4*)(rp + 8192) + *(LAS f32x4*)(rp + 12288));
    const float o0 = bf_lo(xv.x) + rbf(gav[0] * rbf(v[0])), o1 = bf_hi(xv.x) + rbf(gav[1] * rbf(v[1]));
    const float o2 = bf_lo(xv.y) + rbf(gav[2] * rbf(v[2])), o3 = bf_hi(xv.y) + rbf(gav[3] * rbf(v[3]));
    st8<SC1>(c.ws, x1o, OFF_X1 + (unsigned)s * HB_SZ, u32x2{pks(o0, o1), pks(o2, o3)});
    if (c.tid == 0) st4<SC1>(c.ws, FL_X1 + FS * jx, 0, (unsigned)(s + 1));
    CYCW(u4)
    CYCOUT(s, u0, u1, u2, u3, u4, u4, u4)
    stamp(c, 4 + s);
    if (s + X1_ARM < NSEG) st8<SC1>(c.ws, x1o, OFF_X1 + (unsigned)(s + X1_ARM) * HB_SZ, u32x2{SENT, SENT});
  };
  u32x4 za[16], zb[16];
  uready = wait_flags<8>(c, FL_U + 8 * FS * c.wave, 8, 0);
#pragma unroll
  for (int i = 0; i < 16; ++i) za[i] = ld_u(0, i);
#pragma unroll 1
  for (int s = 0; s < NSEG; s += 2) { step(s, za, zb); step(s + 1, zb, za); }
}

__device__ __forceinline__ int ffn_seg(int rep, int i) { return (i >> 1) * 4 + (rep & 2) + (i & 1); }

__device__ __forceinline__ void role_F1(const Params& p, const Ctx& c, int rep, int f1) {
  const u32x4 sv = {SENT, SENT, SENT, SENT};
  const int tau = rep & 1, g = c.lane >> 4, rho = c.lane & 15;
  const unsigned avo = (unsigned)(c.lane * 16 + (f1 * 4 + c.wave) * 2048 + tau * 1024);
  st16<SC1>(c.ws, avo, OFF_ACT + (unsigned)ffn_seg(rep, 0) * ACT_SZ, sv);
  st16<SC1>(c.ws, avo, OFF_ACT + (unsigned)ffn_seg(rep, 1) * ACT_SZ, sv);
  LAS float* shiftv = (LAS float*)(c.lds + 131072);
  LAS float* scalev = (LAS float*)(c.lds + 131072 + 4096);
  bar();
  load_mod(c, 3 * DM, DM, shiftv, false);
  load_mod(c, 4 * DM, DM, scalev, true);
  u32x4 W[4][20];
  f32x4 bias[4] = {}, csum[4] = {};
  {
    rsrc_t rg = mk_rsrc(p.wg, DM * DFF * 2), ru = mk_rsrc(p.wu, DM * DFF * 2);
    const unsigned src = stage_src<0>(c, DFF), rd = stage_rd(c.lane, c.wave);
    const u32x4 ones = {0x3F803F80u, 0x3F803F80u, 0x3F803F80u, 0x3F803F80u};
    stage_weights<10, 0, 8, 4>(c,
      [&](auto ci, LAS unsigned char* dst) {
        constexpr int id = ci, kap = id / 4, pn = id % 4;
        dma16<0>(pn < 2 ? rg : ru, dst, src, (unsigned)(((32 * kap) * DFF + f1 * 128 + (pn & 1) * 64) * 2));
      },
      [&](auto ci, LAS unsigned char* chunk) {
        constexpr int id = ci;
        W[id % 4][id / 4] = stage_frag(chunk + rd);
      },
      [&](auto) {},
      [&](auto ci) {
        constexpr int id = ci, kap = id / 4, i = id % 4;
        const f32x4 s0 = *(LAS f32x4*)(scalev + 32 * kap + 8 * g), s1 = *(LAS f32x4*)(scalev + 32 * kap + 8 * g + 4);
        const f32x4 h0 = *(LAS f32x4*)(shiftv + 32 * kap + 8 * g), h1 = *(LAS f32x4*)(shiftv + 32 * kap + 8 * g + 4);
        const u32x4 w = W[i][kap];
        mfma_vn(bias[i], w, pack2(h0, h1));
        W[i][kap] = u32x4{pk(bf_lo(w.x) * s0[0], bf_hi(w.x) * s0[1]), pk(bf_lo(w.y) * s0[2], bf_hi(w.y) * s0[3]),
                          pk(bf_lo(w.z) * s1[0], bf_hi(w.z) * s1[1]), pk(bf_lo(w.w) * s1[2], bf_hi(w.w) * s1[3])};
        mfma_vn(csum[i], W[i][kap], ones);
        if constexpr (id < 64) pin_a(W[i][kap]); else pin_v(W[i][kap]);
      });
  }
  if (c.tid == 0) st4<SC1>(c.ws, FL_ACT + FS * (16 * rep + f1), 0, 0u);
  LAS unsigned char* HB = c.lds;
  LAS float* red = (LAS float*)(c.lds + 40960);
  LAS float* rw = red + rho * 20 + c.wave * 4 + g;
  LAS float* rr = red + rho * 20;
  wait_flags<8>(c, FL_X1, 20, 0);
  stamp(c, 100);
  const unsigned vo = (unsigned)((10 * c.wave + tau) * 1024 + c.lane * 16);
  unsigned ones_u = 0x3F803F80u;
  pin_v(ones_u);
  auto sum16 = [&](LAS float* q) {
    const f32x4 t = (*(LAS f32x4*)q + *(LAS f32x4*)(q + 4)) + (*(LAS f32x4*)(q + 8) + *(LAS f32x4*)(q + 12));
    return (t[0] + t[1]) + (t[2] + t[3]);
  };
#pragma unroll 1
  for (int it = 0; it < 32; ++it) {
    const int s = ffn_seg(rep, it);
    const unsigned sx = OFF_X1 + (unsigned)s * HB_SZ;
    LAS unsigned char* hb = HB + (it & 1) * 20480 + c.lane * 16;
    CYCW(u0)
    doorbell<4, F1_DELAY>(c, FL_X1, s + 1);
    CYCW(u1)
    u32x4 raw[5];
    unsigned sp = 0;
    for (;;) {
      unsigned m = 0;
      float s0 = 0.f, s1 = 0.f;
#pragma unroll
      for (int i = 0; i < 5; ++i) raw[i] = ld16<SC1>(c.ws, vo, sx + i * 2048);
      sb();
#pragma unroll
      for (int i = 0; i < 5; ++i) {
        *(LAS u32x4*)(hb + (5 * c.wave + i) * 1024) = raw[i];
        m = umax4(m, raw[i]);
        s0 = dot2(raw[i].x, ones_u, s0); s1 = dot2(raw[i].y, ones_u, s1);
        s0 = dot2(raw[i].z, ones_u, s0); s1 = dot2(raw[i].w, ones_u, s1);
      }
      rw[0] = s0 + s1;
      if (!__any(m == SENT)) break;
      wait_flags<4>(c, FL_X1 + 5 * FS * c.wave, 5, s + 1);
      __builtin_amdgcn_s_sleep(2);
      spin_check(c, sp);
    }
    bar();
    CYCW(u2)
    const float mean = sum16(rr) * (1.f / DM);
    f32x4 acc[4];
    float v0 = 0.f, v1 = 0.f;
    {
      u32x4 b[20];
#pragma unroll
      for (int k = 0; k < 3; ++k) b[k] = lds16(hb + k * 1024);
      sb();
      sfor<0, 20>([&](auto kc) {
        constexpr int k = kc;
        const unsigned d = raw[k / 4][k % 4];
        float a0, a1;
        mfma_v<(k * 4 + 0 < 64), true, k == 0>(acc[0], W[0][k], b[k]); sb();
        if constexpr (k + 3 < 20) b[k + 3] = lds16(hb + (k + 3) * 1024);
        a0 = bf_lo(d); a1 = bf_hi(d); sb();
        mfma_v<(k * 4 + 1 < 64), true, k == 0>(acc[1], W[1][k], b[k]); sb();
        a0 = add1(a0, -mean); a1 = add1(a1, -mean); sb();
        mfma_v<(k * 4 + 2 < 64), true, k == 0>(acc[2], W[2][k], b[k]); sb();
        asm("v_fmac_f32 %0, %1, %1" : "+v"(v0) : "v"(a0)); asm("v_fmac_f32 %0, %1, %1" : "+v"(v1) : "v"(a1)); sb();
        mfma_v<(k * 4 + 3 < 64), true, k == 0>(acc[3], W[3][k], b[k]); sb();
      });
    }
    rw[320] = v0 + v1;
    mfma_fence();
    bar();
    sb();
    CYCW(u3)
    const float rstd = __builtin_amdgcn_rsqf(sum16(rr + 320) * (1.f / DM) + 1e-5f);
    float a[8];
#pragma unroll
    for (int i = 0; i < 2; ++i)
#pragma unroll
      for (int r = 0; r < 4; ++r) {
        const float gv = __builtin_fmaf(__builtin_fmaf(-mean, csum[i][r], acc[i][r]), rstd, bias[i][r]);
        const float uv = __builtin_fmaf(__builtin_fmaf(-mean, csum[2 + i][r], acc[2 + i][r]), rstd, bias[2 + i][r]);
        const float z2 = gv * __builtin_fmaf(gv * gv, -2.f * 1.4426950408889634f * 0.7978845608028654f * 0.044715f, -2.f * 1.4426950408889634f * 0.7978845608028654f);
        a[4 * i + r] = gv * uv * __builtin_amdgcn_rcpf(1.f + __builtin_amdgcn_exp2f(z2));
      }
    st16<SC1>(c.ws, avo, OFF_ACT + (unsigned)s * ACT_SZ, u32x4{pks(a[0], a[1]), pks(a[2], a[3]), pks(a[4], a[5]), pks(a[6], a[7])});
    if (c.tid == 0) st4<SC1>(c.ws, FL_ACT + FS * (16 * rep + f1), 0, (unsigned)(it + 1));
    CYCW(u4)
    CYCOUT(s, u0, u1, u2, u3, u4, u4, u4)
    stamp(c, 104 + s);
    if (s + 4 < NSEG) st16<SC1>(c.ws, avo, OFF_ACT + (unsigned)(s + 4) * ACT_SZ, sv);
  }
}

__device__ __forceinline__ void role_F2(const Params& p, const Ctx& c, int rep, int f2) {
  u32x4 W[12][4];
  {
    rsrc_t rd_ = mk_rsrc(p.wd, DFF * DM * 2);
    const unsigned src = stage_src<3>(c, DM);
    unsigned rd[4];
#pragma unroll
    for (int u = 0; u < 4; ++u) rd[u] = stage_rd(c.lane, u) + (unsigned)c.wave * 4096;
    stage_weights<4, 0, 12, 3>(c,
      [&](auto ci, LAS unsigned char* dst) {
        constexpr int id = ci;
        dma16<0>(rd_, dst, src, (unsigned)(((128 * (id / 4) + 16 * (id % 4)) * DM + f2 * 64) * 2));
      },
      [&](auto ci, LAS unsigned char* chunk) {
        constexpr int id = ci;
        if constexpr (id % 4 == 0) {
#pragma unroll
          for (int u = 0; u < 4; ++u) W[id / 4][u] = stage_frag(chunk + rd[u]);
        }
      },
      [&](auto ci) { constexpr int id = ci; pin_a(W[id / 4][id % 4]); }, [](auto) {});
  }
  LAS float* gb = (LAS float*)c.lds;
  LAS unsigned char* R = c.lds + 4096;
  load_mod(c, 5 * DM, DM, gb, false);
  wait_flags<8>(c, FL_ACT + FS * 16 * rep, 12, 0);
  bar();
  const int tau = rep & 1, g = c.lane >> 4, n = c.lane & 15;
  const unsigned l16 = (unsigned)c.lane * 16;
  const int col = 64 * f2 + 16 * c.wave + 4 * g;
  const f32x4 gbv = *(LAS f32x4*)(gb + col);
  rsrc_t ro = mk_rsrc(p.out, 2048 * DM * 2);
  const unsigned xo = (unsigned)(((tau * 16 + n) * DM + col) * 2);
  const unsigned x1o = (unsigned)((col >> 5) * 2048 + tau * 1024 + (((col >> 3) & 3) * 16 + n) * 16 + (g & 1) * 8);
  stamp(c, 100);
#pragma unroll 1
  for (int it = 0; it < 32; ++it) {
    const int s = ffn_seg(rep, it);
    const unsigned sa = OFF_ACT + (unsigned)s * ACT_SZ;
    doorbell<4, F2_DELAY>(c, FL_ACT + FS * 16 * rep, it + 1);
    u32x4 b[12];
    u32x2 xv;
    f32x4 acc[4];
    unsigned sp = 0;
    for (;;) {
#pragma unroll
      for (int i = 0; i < 12; ++i) b[i] = ld16<SC1>(c.ws, l16, sa + (unsigned)((4 * i + c.wave) * 2 + tau) * 1024);
      xv = ld8<SC1>(c.ws, x1o, OFF_X1 + (unsigned)s * HB_SZ);
      sb();
      unsigned m = 0;
      sfor<0, 48>([&](auto ic) {
        constexpr int i = ic >> 2, u = ic & 3;
        mfma_v<true, true, i == 0>(acc[u], W[i][u], b[i]);
        sb();
        if constexpr (u == 0) { m = umax4(m, b[i]); sb(); }
      });
      if (!__any(m == SENT)) break;
      wait_flags<4>(c, FL_ACT + FS * 16 * rep, 12, it + 1);
      __builtin_amdgcn_s_sleep(2);
      spin_check(c, sp);
    }
    mfma_fence();
    LAS unsigned char* Rb = R + (it & 1) * 16384;
#pragma unroll
    for (int u = 0; u < 4; ++u) *(LAS f32x4*)(Rb + c.wave * 4096 + u * 1024 + l16) = acc[u];
    bar();
    LAS unsigned char* rp = Rb + c.wave * 1024 + l16;
    const f32x4 v = (*(LAS f32x4*)rp + *(LAS f32x4*)(rp + 4096)) + (*(LAS f32x4*)(rp + 8192) + *(LAS f32x4*)(rp + 12288));
    const float o0 = bf_lo(xv.x) + rbf(gbv[0] * rbf(v[0])), o1 = bf_hi(xv.x) + rbf(gbv[1] * rbf(v[1]));
    const float o2 = bf_lo(xv.y) + rbf(gbv[2] * rbf(v[2])), o3 = bf_hi(xv.y) + rbf(gbv[3] * rbf(v[3]));
    st8<0>(ro, xo, (unsigned)s * HB_SZ, u32x2{pk(o0, o1), pk(o2, o3)});
    stamp(c, 104 + s);
  }
}

enum { M_PRE = 1, M_KV = 2, M_U = 4, M_REC = 8, M_Q = 16, M_PX = 32, M_F1 = 64, M_F2 = 128, M_ALL = 255 };

__global__ void __launch_bounds__(256, 1) mega_kernel(Params p) {
  extern __shared__ __attribute__((aligned(16))) unsigned char smem[];
  Ctx c;
  c.ws = mk_rsrc(p.ws, WS_BYTES);
  c.srd = u32x4{(unsigned)(uintptr_t)p.ws, (unsigned)((uintptr_t)p.ws >> 32) & 0xFFFFu, WS_BYTES, 0x00020000u};
  c.lds = (LAS unsigned char*)smem;
  c.tid = threadIdx.x; c.lane = c.tid & 63; c.wave = __builtin_amdgcn_readfirstlane(c.tid >> 6);
  const int grp = blockIdx.x & 7, ix = blockIdx.x >> 3;
  stamp(c, 0);
  int role, h, rep, r;
  rep = grp - 4; r = ix;
  if (grp < 4) {
    if (ix < 16) { role = ix >> 3; h = grp * 8 + (ix & 7); }
    else if (ix < 26) { role = 6; h = grp * 10 + ix - 16; }
    else if (ix < 31) { role = 6; h = 40 + grp * 5 + ix - 26; }
    else { role = 2; h = 28 + grp; }
  } else {
    if (ix < 24) { role = 3 + (ix >> 3); h = rep * 8 + (ix & 7); }
    else if (ix < 31) { role = 2; h = rep * 7 + ix - 24; }
    else { role = 6; h = 60 + rep; }
  }
  if (role < 2) { for (int i = 0; i < HV_DELAY; ++i) __builtin_amdgcn_s_sleep(32); }
  if (role == 2) {
    for (int i = 0; i < Q_DELAY; ++i) __builtin_amdgcn_s_sleep(32);
  }
  if (role == 0) { if (p.mask & M_REC) role_H(p, c, h); }
  else if (role == 1) { if (p.mask & M_REC) role_V(p, c, h); }
  else if (role == 2) { if (p.mask & M_Q) role_Q(p, c, h); }
  else if (role < 6) {
    if (role == 5) { if (p.mask & M_U) role_U(p, c, h); }
    else { if (p.mask & M_KV) role_KV(p, c, h, role == 4); }
    if (r < 12) { if (p.mask & M_F1) role_F1(p, c, rep, r); }
    else if (r < 22) { if (p.mask & M_F2) role_F2(p, c, rep, r - 12); }
  } else {
    if (p.mask & M_PRE) role_pre(p, c, h, h < 40 ? h : -1);
    if (h < 40) { if (p.mask & M_REC) role_PZ(p, c, h); }
    else if (h < 60) { if (p.mask & M_PX) role_PX(p, c, h - 40); }
    else if (FFN_PREFETCH) {
      for (int i = 0; i < 24; ++i) __builtin_amdgcn_s_sleep(32);
#pragma unroll 1
      for (int m = 0; m < 3; ++m) {
        rsrc_t r = mk_rsrc(m == 0 ? p.wg : m == 1 ? p.wu : p.wd, DM * DFF * 2);
#pragma unroll 1
        for (unsigned o = (unsigned)(h - 60) * 49152u; o < DM * DFF * 2; o += 4 * 49152) {
          u32x4 t[12];
#pragma unroll
          for (int i = 0; i < 12; ++i) t[i] = ld16<0>(r, (unsigned)c.tid * 16u + i * 4096u, o);
#pragma unroll
          for (int i = 0; i < 12; ++i) asm volatile("" :: "v"(t[i]));
        }
      }
    }
  }
}

}

#define SPLIT_LAUNCH 0

extern "C" void kernel_launch(void* const* d_in, const int* in_sizes, int n_in, void* d_out, int out_size, void* d_ws,
                              size_t ws_size, hipStream_t stream) {
  Params p;
  p.x = d_in[0]; p.mem0 = d_in[1]; p.Wq = d_in[2]; p.Wk = d_in[3]; p.Wv = d_in[4]; p.Wo = d_in[5];
  p.wg = d_in[8]; p.wu = d_in[9]; p.wd = d_in[10]; p.c = d_in[11]; p.wmod = d_in[12];
  void* wsa = (void*)(((uintptr_t)d_ws + ((2u << 20) - 1)) & ~(uintptr_t)((2u << 20) - 1));
  p.out = d_out; p.ws = wsa; p.mask = M_ALL; p.pad = 0;
  (void)hipFuncSetAttribute((const void*)mega_kernel, hipFuncAttributeMaxDynamicSharedMemorySize, LDS_BYTES);
  (void)hipMemsetAsync(wsa, 0xFF, INIT_BYTES, stream);
  hipLaunchKernelGGL(mega_kernel, dim3(256), dim3(256), LDS_BYTES, stream, p);
}
```

```cpp
#include <hip/hip_runtime.h>
#include <hip/hip_bf16.h>
#include <type_traits>
#include <cstdint>

#define LAS __attribute__((address_space(3)))
typedef unsigned u32x2 __attribute__((ext_vector_type(2)));
typedef unsigned u32x4 __attribute__((ext_vector_type(4)));
typedef unsigned u32x8 __attribute__((ext_vector_type(8)));
typedef float f32x2 __attribute__((ext_vector_type(2)));
typedef float f32x4 __attribute__((ext_vector_type(4)));
typedef short s16x4 __attribute__((ext_vector_type(4)));
typedef __bf16 bf16x2 __attribute__((ext_vector_type(2)));
typedef __bf16 bf16x8 __attribute__((ext_vector_type(8)));
typedef __bf16 bf16x16 __attribute__((ext_vector_type(16)));
typedef __amdgpu_buffer_rsrc_t rsrc_t;

namespace {

constexpr int DM = 640, NH = 32, DH = 192, NSEG = 64, DFF = 1536;
constexpr unsigned SENT = 0xFFFFFFFFu;

constexpr unsigned FS = 128;
constexpr unsigned FL_ABORT = 0;
constexpr unsigned OFF_MOD = 1024;
constexpr unsigned OFF_MODP = 57344;
constexpr unsigned FL_H = 16384;
constexpr unsigned FL_KM = 24576, FL_K = 28672, FL_V = 32768, FL_U = 36864, FL_PX = 40960;
constexpr unsigned FL_X1 = 45056;
constexpr unsigned FL_ACT = 49152;
constexpr unsigned OFF_DB = 81920;
constexpr unsigned INIT_BYTES = OFF_DB + 8192 + 4096;
constexpr unsigned OFF_XR = 98304;
constexpr unsigned MEM_SZ = 10240, P_SZ = 20480;
constexpr unsigned L_MEM = 0, L_P = 64 * MEM_SZ, L_END = L_P + 64 * P_SZ;
constexpr unsigned OFF_CAL = OFF_XR + 2 * L_END;
constexpr unsigned OFF_M = OFF_CAL + 65536;
constexpr unsigned M_SZ = 40 * 16384;
constexpr unsigned OFF_HB = OFF_M + 64 * M_SZ;
constexpr unsigned HB_SZ = 40960;
constexpr unsigned OFF_KX = OFF_HB + 64 * HB_SZ;
constexpr unsigned KX_SZ = 12288;
constexpr unsigned OFF_VX = OFF_KX + 64 * 32 * KX_SZ;
constexpr unsigned OFF_U = OFF_VX + 64 * 32 * KX_SZ;
constexpr unsigned U_SZ = 40 * 32 * 1024;
constexpr unsigned OFF_KM = OFF_U + 64 * U_SZ;
constexpr unsigned OFF_PX = OFF_KM + 64 * 32 * 3072;
constexpr unsigned PX_SZ = 81920;
constexpr unsigned OFF_X1 = OFF_PX + 64 * PX_SZ;
constexpr unsigned OFF_ACT = OFF_X1 + 2048 * 640 * 2;
constexpr unsigned ACT_SZ = 48 * 2 * 1024;
constexpr unsigned OFF_DBG = OFF_ACT + 64 * ACT_SZ;
constexpr unsigned WS_BYTES = OFF_DBG + (1u << 20);
static_assert(WS_BYTES + (2u << 20) <= (256u << 20), "workspace");
static_assert(OFF_XR % 16384 == 0 && OFF_CAL % 16384 == 0 && OFF_M % 16384 == 0, "alignment");

constexpr int LDS_BYTES = 160 * 1024;
constexpr unsigned SPIN_CAP = 1u << 21;
#define PZ_SLEEP 14
#define PZ_GAPD 2
#define PZ_GAP 0
#define UST_AUX SC1
#define UST_VO ((unsigned)c.lane * 16)
#define H_SLEEP 12
#define MAP 1
#define REV 0
#define V_NW 4
#define V_NA 3
#define HV_DELAY 0
#define Q_DELAY 0
#define PZ_MODE 0
#define PZ_THR 16
#define PXT 0
#define PX_SLEEP 4
#define PX_NF 1
#define PX_DELAY 12
#define USE_BELL_P 1
#define USE_BELL_M 1
#define BELL_P_POS 5
#define BELL_P_GAP 3
#define BELL_M_POS 2
#define BELL_M_GAP 0
#define BELL_P_GAP0 0
#define H_D0 0
#define H_D1 8
#define H_D2 0
#define F1_DELAY 4
#define F2_DELAY 2
#define KM_BELL 0
#define KM_DELAY 0
#define TLBWARM 0
#define FFN_PREFETCH 1
#define Q_PREFETCH 0
#define U_EAGER 6
#define BELL_MODE 1
#define BELL_SLEEP 0
#define NBELL 4
#define EARLY_BELL 0
#define KW_DELAY 0
#define UW_DELAY 0
#define KV_AUX 16
#define U_AUX 16
#define V_SLEEP 16

struct Params {
  const void* x; const void* mem0; const void* Wq; const void* Wk; const void* Wv; const void* Wo;
  const void* wg; const void* wu; const void* wd; const void* c; const void* wmod;
  void* out; void* ws;
  unsigned mask; unsigned pad;
};

template <int I, int N, class F>
__device__ __forceinline__ void sfor(F&& f) {
  if constexpr (I < N) { f(std::integral_constant<int, I>{}); sfor<I + 1, N>(f); }
}
__device__ __forceinline__ rsrc_t mk_rsrc(const void* p, unsigned bytes) {
  return __builtin_amdgcn_make_buffer_rsrc(const_cast<void*>(p), 0, bytes, 0x00020000);
}
template <int AUX> __device__ __forceinline__ u32x4 ld16(rsrc_t r, unsigned vo, unsigned so) { return __builtin_amdgcn_raw_buffer_load_b128(r, vo, so, AUX); }
template <int AUX> __device__ __forceinline__ u32x2 ld8(rsrc_t r, unsigned vo, unsigned so) { return __builtin_amdgcn_raw_buffer_load_b64(r, vo, so, AUX); }
template <int AUX> __device__ __forceinline__ unsigned ld4(rsrc_t r, unsigned vo, unsigned so) { return __builtin_amdgcn_raw_buffer_load_b32(r, vo, so, AUX); }
template <int AUX> __device__ __forceinline__ void st16(rsrc_t r, unsigned vo, unsigned so, u32x4 v) { __builtin_amdgcn_raw_buffer_store_b128(v, r, vo, so, AUX); }
template <int AUX> __device__ __forceinline__ void st8(rsrc_t r, unsigned vo, unsigned so, u32x2 v) { __builtin_amdgcn_raw_buffer_store_b64(v, r, vo, so, AUX); }
template <int AUX> __device__ __forceinline__ void st4(rsrc_t r, unsigned vo, unsigned so, unsigned v) { __builtin_amdgcn_raw_buffer_store_b32(v, r, vo, so, AUX); }
constexpr int SC1 = 16;
template <int AUX> __device__ __forceinline__ void dma16(rsrc_t r, LAS unsigned char* dst, unsigned vo, unsigned so) {
  __builtin_amdgcn_raw_ptr_buffer_load_lds(r, (LAS void*)dst, 16, vo, so, 0, AUX);
}
template <int N> __device__ __forceinline__ void wait_vm() { asm volatile("s_waitcnt vmcnt(%0)" ::"n"(N) : "memory"); }
__device__ __forceinline__ void wait_lgkm() { asm volatile("s_waitcnt lgkmcnt(0)" ::: "memory"); }
__device__ __forceinline__ void bar() { asm volatile("s_waitcnt lgkmcnt(0)\n\ts_barrier" ::: "memory"); }

__device__ __forceinline__ unsigned pk(float a, float b) {
  f32x2 v = {a, b};
  return __builtin_bit_cast(unsigned, __builtin_convertvector(v, bf16x2));
}
__device__ __forceinline__ unsigned pks(float a, float b) { return min(pk(a, b), 0xFFFFFFFEu); }
__device__ __forceinline__ float bf_lo(unsigned u) { return __builtin_bit_cast(float, u << 16); }
__device__ __forceinline__ float bf_hi(unsigned u) { return __builtin_bit_cast(float, u & 0xFFFF0000u); }
__device__ __forceinline__ float rbf(float a) { return bf_lo(pk(a, a)); }
__device__ __forceinline__ float f_(unsigned u) { return __builtin_bit_cast(float, u); }
__device__ __forceinline__ unsigned u_(float f) { return __builtin_bit_cast(unsigned, f); }
__device__ __forceinline__ unsigned umax3(unsigned a, unsigned b, unsigned c) { return max(max(a, b), c); }
__device__ __forceinline__ unsigned umax4(unsigned m, u32x4 v) { return umax3(umax3(m, v.x, v.y), v.z, v.w); }

__device__ __forceinline__ float dot2(unsigned a, unsigned b, float c) {
  return __builtin_amdgcn_fdot2_f32_bf16(__builtin_bit_cast(bf16x2, a), __builtin_bit_cast(bf16x2, b), c, false);
}
__device__ __forceinline__ f32x4 mfma32(u32x4 a, u32x4 b, f32x4 c) {
  return __builtin_amdgcn_mfma_f32_16x16x32_bf16(__builtin_bit_cast(bf16x8, a), __builtin_bit_cast(bf16x8, b), c, 0, 0, 0);
}
__device__ __forceinline__ f32x4 mfma16(u32x2 a, u32x2 b, f32x4 c) {
  return __builtin_amdgcn_mfma_f32_16x16x16bf16_1k(__builtin_bit_cast(s16x4, a), __builtin_bit_cast(s16x4, b), c, 0, 0, 0);
}
__device__ __forceinline__ f32x4 smfma(u32x4 a, u32x8 b, f32x4 c, int idx) {
  return __builtin_amdgcn_smfmac_f32_16x16x64_bf16(__builtin_bit_cast(bf16x8, a), __builtin_bit_cast(bf16x16, b), c, idx, 0, 0);
}
__device__ __forceinline__ u32x2 tr8(LAS unsigned char* p) {
  return __builtin_bit_cast(u32x2, __builtin_amdgcn_ds_read_tr16_b64_v4i16((LAS s16x4*)p));
}
__device__ __forceinline__ u32x4 lds16(LAS unsigned char* p) { return *(LAS u32x4*)p; }
__device__ __forceinline__ u32x4 cat(u32x2 a, u32x2 b) { return u32x4{a.x, a.y, b.x, b.y}; }
__device__ __forceinline__ u32x8 cat(u32x4 a, u32x4 b) { return u32x8{a.x, a.y, a.z, a.w, b.x, b.y, b.z, b.w}; }
__device__ __forceinline__ float halfsum(float x, float y) {
  auto r = __builtin_amdgcn_permlane32_swap(u_(x), u_(y), false, false);
  return f_(r[0]) + f_(r[1]);
}
__device__ __forceinline__ float halfsum1(float x, float y) {
  auto r = __builtin_amdgcn_permlane32_swap(u_(x), u_(y), false, false);
  float o;
  asm("v_add_f32 %0, %1, %2" : "=v"(o) : "v"(r[0]), "v"(r[1]));
  return o;
}
template <int CTRL> __device__ __forceinline__ float dpp(float x) {
  return f_((unsigned)__builtin_amdgcn_update_dpp(0, (int)u_(x), CTRL, 0xf, 0xf, false));
}

template <class T> __device__ __forceinline__ void pin_a(T& v) { asm volatile("" : "+a"(v)); }
template <class T> __device__ __forceinline__ void pin_v(T& v) { asm volatile("" : "+v"(v)); }

__device__ __forceinline__ void sb() { __builtin_amdgcn_sched_barrier(0); }

struct Ctx {
  rsrc_t ws;
  u32x4 srd;
  LAS unsigned char* lds;
  int tid, lane, wave;
};

__device__ __forceinline__ void stamp(const Ctx&, int, int = 0) {}
__device__ __forceinline__ void stampc(const Ctx&, int, int = 0) {}
__device__ __forceinline__ void stampl(const Ctx&, int) {}
__device__ __forceinline__ void stampf(const Ctx&, int, int = 0) {}
#define CYC(x)
#define CYCW(x)
#define CYCOUT(...)
__device__ __forceinline__ void spin_check(const Ctx& c, unsigned& n) {
  unsigned ts, tv;
  ++n;
  asm volatile(
      "s_and_b32 %0, %2, 0x3ff\n\t"
      "s_cbranch_scc1 1f\n\t"
      "buffer_load_dword %1, off, %3, 0 sc1\n\t"
      "s_waitcnt vmcnt(0)\n\t"
      "v_readfirstlane_b32 %0, %1\n\t"
      "s_nop 0\n\t"
      "s_cmp_lg_u32 %0, -1\n\t"
      "s_cbranch_scc1 2f\n\t"
      "s_cmp_lt_u32 %2, %4\n\t"
      "s_cbranch_scc1 1f\n\t"
      "v_mov_b32 %1, 1\n\t"
      "buffer_store_dword %1, off, %3, 0 sc1\n\t"
      "s_waitcnt vmcnt(0)\n"
      "2:\n\t"
      "s_endpgm\n"
      "1:"
      : "=&s"(ts), "=&v"(tv)
      : "s"(n), "s"(c.srd), "s"(SPIN_CAP)
      : "memory", "scc");
}
template <class T> __device__ __forceinline__ void pin_s(T& v) { asm volatile("" : "+s"(v)); }
template <int SLEEP>
__device__ __forceinline__ int wait_flags(const Ctx& c, unsigned fl, int n, int need) {
  unsigned sp = 0;
  const unsigned vo = fl + FS * (unsigned)min(c.lane, n - 1);
  for (;;) {
    int v = (int)ld4<SC1>(c.ws, vo, 0);
    if (!__any(v < need)) {
      int got = need;
      if (!__any(v < need + 2)) got = need + 2;
      if (!__any(v < need + 6)) got = need + 6;
      if (!__any(v < need + 16)) got = need + 16;
      return got;
    }
    __builtin_amdgcn_s_sleep(SLEEP);
    spin_check(c, sp);
  }
}
template <int SLEEP>
__device__ __forceinline__ int wait_flags_blk(const Ctx& c, unsigned fl, int n, int need, LAS int* w) {
  if (c.wave == 0) { const int r = wait_flags<SLEEP>(c, fl, n, need); if (c.lane == 0) *w = r; }
  bar();
  const int r = __builtin_amdgcn_readfirstlane(*w);
  bar();
  return r;
}
__device__ __forceinline__ void publish(const Ctx& c, unsigned fl, int val) {
  wait_vm<0>();
  bar();
  if (c.tid == 0) st4<SC1>(c.ws, fl, 0, (unsigned)val);
}

template <int SLEEP, int DELAY>
__device__ __forceinline__ void doorbell(const Ctx& c, unsigned fl, int need) {
  unsigned sp = 0;
  while ((int)__builtin_amdgcn_readfirstlane(ld4<SC1>(c.ws, fl, 0)) < need) { __builtin_amdgcn_s_sleep(SLEEP); spin_check(c, sp); }
  if (__builtin_amdgcn_readfirstlane(sp)) __builtin_amdgcn_s_sleep(DELAY);
}
__device__ __forceinline__ u32x4 nosent(u32x4 v) { return u32x4{min(v.x, 0xFFFFFFFEu), min(v.y, 0xFFFFFFFEu), min(v.z, 0xFFFFFFFEu), min(v.w, 0xFFFFFFFEu)}; }
__device__ __forceinline__ u32x2 nosent(u32x2 v) { return u32x2{min(v.x, 0xFFFFFFFEu), min(v.y, 0xFFFFFFFEu)}; }

__device__ __forceinline__ unsigned side_bit(unsigned b, unsigned side) { return b | (((side ^ (b >> 21)) & 1u) << 13); }
__device__ __forceinline__ unsigned xr_phys(unsigned o, unsigned side) { return side_bit(OFF_XR + ((o >> 13) << 14) + (o & 8191u), side); }
__device__ __forceinline__ unsigned m_phys(unsigned s, unsigned j, unsigned side) { return side_bit(OFF_M + (s * 40u + j) * 16384u, side); }
__device__ __forceinline__ unsigned my_iod() { return (((unsigned)__builtin_amdgcn_s_getreg(20 | (3 << 11))) >> 2) & 1u; }
struct Sides { unsigned me; unsigned long long light; };
__device__ __forceinline__ unsigned ld_table(const Ctx& c) { return ld4<SC1>(c.ws, FL_H + FS * (unsigned)c.lane, 0); }
__device__ __forceinline__ void get_sides(const Ctx& c, Sides& o, unsigned v = SENT) {
  unsigned sp = 0;
  if (__any(v == SENT)) {
    for (;;) {
      v = ld_table(c);
      if (!__any(v == SENT)) break;
      __builtin_amdgcn_s_sleep(8);
      spin_check(c, sp);
    }
  }
  const unsigned cc = __popcll(__ballot(v & 1u) >> 32) > 16 ? 1u : 0u;
  const unsigned long long iod = __ballot(v & 2u);
  o.me = cc ^ my_iod();
  o.light = cc ? ~iod : iod;
}
__device__ __forceinline__ unsigned pz_side(const Sides& sd, int j) { return (unsigned)(sd.light >> j) & 1u; }
__device__ __forceinline__ void ring(const Ctx& c, int bell, int val) {
  if (c.lane < 2) st4<SC1>(c.ws, OFF_DB + (unsigned)bell * FS + (unsigned)c.lane * 8192u, 0, (unsigned)val);
}
__device__ __forceinline__ void wait_bells(const Ctx& c, unsigned vo, int need, u32x2& keep) {
  unsigned sp = 0;
  if (c.wave == 0) {
    while (__any((int)ld4<SC1>(c.ws, vo, 0) < need)) { if (BELL_SLEEP) __builtin_amdgcn_s_sleep(BELL_SLEEP); spin_check(c, sp); }
  } else {
    wait_vm<0>();
  }
  __builtin_amdgcn_s_barrier();
}
__device__ __forceinline__ unsigned bell_vo(const Ctx& c, int bell0, unsigned side) { return side_bit(OFF_DB + (unsigned)(bell0 + (c.lane & (NBELL - 1))) * FS, side); }
__device__ __forceinline__ unsigned calibrate(const Ctx& c, int slot) {
  const unsigned base = OFF_CAL + (unsigned)slot * 256u;
  st16<SC1>(c.ws, base + (unsigned)(c.lane >> 3) * 8192u + (unsigned)(c.lane & 7) * 16u, 0, u32x4{0u, 0u, 0u, 0u});
  wait_vm<0>();
  unsigned best[2] = {~0u, ~0u};
#pragma unroll
  for (int i = 0; i < 8; ++i) {
    const unsigned long long t0 = __builtin_amdgcn_s_memtime();
    wait_lgkm();
    unsigned v = ld4<SC1>(c.ws, base, (unsigned)i * 8192u);
    asm volatile("s_waitcnt vmcnt(0)" : "+v"(v)::"memory");
    const unsigned long long t1 = __builtin_amdgcn_s_memtime();
    wait_lgkm();
    const unsigned par = (((OFF_CAL + i * 8192u) >> 13) ^ ((OFF_CAL + i * 8192u) >> 21)) & 1u;
    best[par] = min(best[par], (unsigned)(t1 - t0));
  }
  return best[1] < best[0] ? 1u : 0u;
}

template <int MODE> __device__ __forceinline__ int rowfn(int g, int j) {
  if (MODE == 0) return 8 * g + j;
  if (MODE == 1) return 16 * (g >> 1) + 8 * ((j >> 1) & 1) + 4 * (g & 1) + 2 * ((j >> 2) & 1) + (j & 1);
  if (MODE == 2) return 16 * (j >> 2) + 4 * g + (j & 3);
  return 64 * (j >> 2) + 4 * g + (j & 3);
}
template <int MODE> __device__ __forceinline__ unsigned stage_src(const Ctx& c, int ldw) {
  const int pos = c.lane >> 3, cp = c.lane & 7;
  const int u = (cp >> 1) ^ ((pos >> 1) & 3);
  const int ch = 2 * u + (cp & 1);
  const int g = 2 * (c.wave >> 1) + (pos >> 2), j = 4 * (c.wave & 1) + (pos & 3);
  return (unsigned)((rowfn<MODE>(g, j) * ldw + ch * 8) * 2);
}
__device__ __forceinline__ unsigned stage_rd(int lane, int u) {
  const int g = lane >> 4, q = (lane & 15) >> 2, p = lane & 3, pos = 4 * (g & 1) + q;
  return (unsigned)((g >> 1) * 2048 + pos * 128 + ((u ^ ((pos >> 1) & 3)) * 32) + p * 8);
}
__device__ __forceinline__ u32x4 stage_frag(LAS unsigned char* chunk_plus_rd) { return cat(tr8(chunk_plus_rd), tr8(chunk_plus_rd + 1024)); }

template <int NB, int NRES, int BATCH, int NBUF, class Issue, class Consume, class Pin, class Post>
__device__ __forceinline__ void stage_weights(const Ctx& c, Issue&& issue, Consume&& consume, Pin&& pin, Post&& post) {
  constexpr int SLOT = BATCH * 4096;
  bar();
  sfor<0, (NBUF < NB ? NBUF : NB)>([&](auto b) {
    sfor<0, BATCH>([&](auto i) { issue(std::integral_constant<int, b * BATCH + i>{}, c.lds + b * SLOT + i * 4096 + c.wave * 1024); });
  });
  stampl(c, 300);
  sfor<0, NB - NRES>([&](auto b) {
    constexpr int inflight = ((b + NBUF < NB ? b + NBUF : NB) - (b + 1)) * BATCH;
    wait_vm<inflight>();
    bar();
    stampl(c, 301 + b);
    sfor<0, BATCH>([&](auto i) { consume(std::integral_constant<int, b * BATCH + i>{}, c.lds + (b % NBUF) * SLOT + i * 4096); });
    sb();
    sfor<0, BATCH>([&](auto i) { pin(std::integral_constant<int, b * BATCH + i>{}); });
    if constexpr (b + NBUF < NB) {
      bar();
      sfor<0, BATCH>([&](auto i) {
        issue(std::integral_constant<int, (b + NBUF) * BATCH + i>{}, c.lds + (b % NBUF) * SLOT + i * 4096 + c.wave * 1024);
      });
    }
    sb();
    sfor<0, BATCH>([&](auto i) { post(std::integral_constant<int, b * BATCH + i>{}); });
  });
  wait_vm<0>();
  bar();
}

__device__ __forceinline__ float ln_stats(const Ctx& c, const u32x4 (&raw)[10], f32x2 (&cv)[40], LAS float* red) {
  const int tau = c.wave & 1, kh = c.wave >> 1, g = c.lane >> 4, rho = c.lane & 15, row = tau * 16 + rho;
  float s0 = 0.f, s1 = 0.f;
  unsigned ones_u = 0x3F803F80u;
  pin_v(ones_u);
#pragma unroll
  for (int i = 0; i < 10; ++i) {
    s0 = dot2(raw[i].x, ones_u, s0); s1 = dot2(raw[i].y, ones_u, s1);
    s0 = dot2(raw[i].z, ones_u, s0); s1 = dot2(raw[i].w, ones_u, s1);
  }
  float s = s0 + s1;
  s += __shfl_xor(s, 16); s += __shfl_xor(s, 32);
  if (g == 0) red[kh * 32 + row] = s;
  bar();
  const float mean = (red[row] + red[32 + row]) * (1.f / DM);
  f32x2 v2 = {0.f, 0.f};
#pragma unroll
  for (int i = 0; i < 10; ++i)
#pragma unroll
    for (int d = 0; d < 4; ++d) {
      const f32x2 t = f32x2{bf_lo(raw[i][d]), bf_hi(raw[i][d])} - mean;
      cv[4 * i + d] = t;
      v2 += t * t;
    }
  float v = v2[0] + v2[1];
  v += __shfl_xor(v, 16); v += __shfl_xor(v, 32);
  if (g == 0) red[64 + kh * 32 + row] = v;
  bar();
  return 1.f / sqrtf((red[64 + row] + red[96 + row]) * (1.f / DM) + 1e-5f);
}
template <class Store>
__device__ __forceinline__ void ln_apply(const Ctx& c, const f32x2 (&cv)[40], float rstd, LAS float* shiftv, LAS float* scalev, Store&& store) {
  const int tau = c.wave & 1, kh = c.wave >> 1, g = c.lane >> 4;
#pragma unroll
  for (int i = 0; i < 10; ++i) {
    const int kap = kh * 10 + i, d0 = (4 * kap + g) * 8;
    const f32x4 sc0 = *(LAS f32x4*)(scalev + d0), sc1 = *(LAS f32x4*)(scalev + d0 + 4);
    const f32x4 sh0 = *(LAS f32x4*)(shiftv + d0), sh1 = *(LAS f32x4*)(shiftv + d0 + 4);
    const f32x2 r0 = (cv[4 * i + 0] * rstd) * f32x2{sc0[0], sc0[1]} + f32x2{sh0[0], sh0[1]};
    const f32x2 r1 = (cv[4 * i + 1] * rstd) * f32x2{sc0[2], sc0[3]} + f32x2{sh0[2], sh0[3]};
    const f32x2 r2 = (cv[4 * i + 2] * rstd) * f32x2{sc1[0], sc1[1]} + f32x2{sh1[0], sh1[1]};
    const f32x2 r3 = (cv[4 * i + 3] * rstd) * f32x2{sc1[2], sc1[3]} + f32x2{sh1[2], sh1[3]};
    store(kap * 2 + tau, u32x4{pk(r0[0], r0[1]), pk(r1[0], r1[1]), pk(r2[0], r2[1]), pk(r3[0], r3[1])});
  }
}
__device__ __forceinline__ void load_mod(const Ctx& c, int f0, int nfl, LAS float* dst, bool add1) {
  for (int ch = c.tid; ch < ((nfl / 4 + 255) & ~255); ch += 256) {
    const int cc = min(ch, nfl / 4 - 1);
    unsigned sp = 0;
    u32x4 v;
    for (;;) {
      v = ld16<SC1>(c.ws, OFF_MOD + (unsigned)(f0 * 4 + cc * 16), 0);
      if (!__any(umax4(0u, v) == SENT)) break;
      __builtin_amdgcn_s_sleep(1);
      spin_check(c, sp);
    }
    f32x4 f = __builtin_bit_cast(f32x4, v);
    if (add1) { f[0] = rbf(1.f + f[0]); f[1] = rbf(1.f + f[1]); f[2] = rbf(1.f + f[2]); f[3] = rbf(1.f + f[3]); }
    *(LAS f32x4*)(dst + cc * 4) = f;
  }
}

__device__ __forceinline__ void role_pre(const Params& p, const Ctx& c, int li, int jpz) {
  const unsigned iod = my_iod();
  unsigned tabw = iod << 1;
  if (jpz >= 0) {
    const u32x4 sv = {SENT, SENT, SENT, SENT};
#pragma unroll
    for (int i = 0; i < 8; ++i) st16<SC1>(c.ws, (unsigned)c.tid * 16u, OFF_M + (unsigned)((i >> 2) * 40 + jpz) * 16384u + (unsigned)(i & 3) * 4096u, sv);
    if (c.tid < 128) st16<SC1>(c.ws, xr_phys(L_P + (unsigned)jpz * 1024u + (unsigned)(c.tid & 63) * 16u, c.tid >> 6), 0, sv);
    else if (c.tid < 192) st16<SC1>(c.ws, xr_phys(L_MEM + (unsigned)(1 + ((c.tid >> 4) & 1)) * MEM_SZ + (unsigned)jpz * 256u + (unsigned)(c.tid & 15) * 16u, (c.tid >> 5) & 1), 0, sv);
    else if (c.tid < 224) {
      const u32x4 m0 = ld16<0>(mk_rsrc(p.mem0, 8 * DM * 2), (unsigned)((c.tid & 7) * DM * 2 + jpz * 32 + ((c.tid >> 3) & 1) * 16), 0);
      st16<SC1>(c.ws, xr_phys(L_MEM + (unsigned)jpz * 256u + (unsigned)(c.tid & 15) * 16u, (c.tid >> 4) & 1), 0, nosent(m0));
    }
  }
  LAS float* sc = (LAS float*)c.lds;
  LAS float* red = (LAS float*)(c.lds + 4096);
  rsrc_t rw = mk_rsrc(p.wmod, DM * 3840 * 2);
  const int cg = c.tid & 7, rg = c.tid >> 3;
  u32x4 w[20];
  const unsigned cv3 = ld4<0>(mk_rsrc(p.c, DM * 2), (unsigned)c.tid * 4u, 0), cv4 = ld4<0>(mk_rsrc(p.c, DM * 2), (unsigned)c.tid * 4u + 1024u, 0);
  const int r0 = (li / 20) * 224 + rg * 7;
  if (li < 60) {
#pragma unroll
    for (int i = 0; i < 7; ++i) w[i] = ld16<0>(rw, (unsigned)(((r0 + i) * 3840 + (li % 20) * 64 + cg * 8) * 2), 0);
  }
  rsrc_t rx = mk_rsrc(p.x, 2048 * DM * 2);
  u32x4 raw[10];
  {
    const int tau = c.wave & 1, kh = c.wave >> 1, g = c.lane >> 4, rho = c.lane & 15;
    const unsigned vo = (unsigned)(((li * 32 + tau * 16 + rho) * DM + (40 * kh + g) * 8) * 2);
#pragma unroll
    for (int i = 0; i < 10; ++i) raw[i] = ld16<0>(rx, vo + i * 64, 0);
  }
  sb();
  stampl(c, 300);
  auto silu = [](float v) { return rbf(v / (1.f + __expf(-v))); };
  *(LAS f32x2*)(sc + 2 * c.tid) = f32x2{silu(bf_lo(cv3)), silu(bf_hi(cv3))};
  if (c.tid < 80) *(LAS f32x2*)(sc + 512 + 2 * c.tid) = f32x2{silu(bf_lo(cv4)), silu(bf_hi(cv4))};
  bar();
  stampl(c, 301);
  auto gemv = [&](auto nrow, int row0, unsigned dst, bool round) {
    float a[8] = {};
#pragma unroll
    for (int i = 0; i < nrow; ++i) {
      const float sv = sc[row0 + i];
#pragma unroll
      for (int d = 0; d < 4; ++d) { a[2 * d] += sv * bf_lo(w[i][d]); a[2 * d + 1] += sv * bf_hi(w[i][d]); }
    }
    *(LAS f32x4*)(red + rg * 64 + cg * 8) = f32x4{a[0], a[1], a[2], a[3]};
    *(LAS f32x4*)(red + rg * 64 + cg * 8 + 4) = f32x4{a[4], a[5], a[6], a[7]};
    bar();
    float t = 0.f;
#pragma unroll
    for (int r = 0; r < 8; ++r) t += red[(c.wave * 8 + r) * 64 + c.lane];
    red[2048 + c.tid] = t;
    bar();
    if (c.tid < 64) {
      t = (red[2048 + c.tid] + red[2112 + c.tid]) + (red[2176 + c.tid] + red[2240 + c.tid]);
      st4<SC1>(c.ws, dst + (unsigned)c.tid * 4, 0, min(u_(round ? rbf(t) : t), 0xFFFFFFFEu));
    }
  };
  if (li < 60) gemv(std::integral_constant<int, 7>{}, r0, OFF_MODP + (unsigned)((li / 20) * 1280 + (li % 20) * 64) * 4, false);
  stampl(c, 302);
  LAS float* shiftv = (LAS float*)(c.lds + 16384);
  LAS float* red2 = (LAS float*)(c.lds + 24576);
  f32x2 cv[40];
  const float rstd = ln_stats(c, raw, cv, red2);
  stampl(c, 303);
  for (int ch = c.tid; ch < 512; ch += 256) {
    const int cc = min(ch, 319);
    unsigned sp = 0;
    u32x4 v0, v1, v2;
    for (;;) {
      v0 = ld16<SC1>(c.ws, (unsigned)cc * 16, OFF_MODP); v1 = ld16<SC1>(c.ws, (unsigned)cc * 16, OFF_MODP + 5120); v2 = ld16<SC1>(c.ws, (unsigned)cc * 16, OFF_MODP + 10240);
      if (!__any(umax4(umax4(umax4(0u, v0), v1), v2) == SENT)) break;
      __builtin_amdgcn_s_sleep(2);
      spin_check(c, sp);
    }
    f32x4 f = __builtin_bit_cast(f32x4, v0) + __builtin_bit_cast(f32x4, v1) + __builtin_bit_cast(f32x4, v2);
    const float one = cc >= 160 ? 1.f : 0.f;
#pragma unroll
    for (int d = 0; d < 4; ++d) f[d] = rbf(one + rbf(f[d]));
    *(LAS f32x4*)(shiftv + cc * 4) = f;
  }
  bar();
  stampl(c, 304);
  ln_apply(c, cv, rstd, shiftv, shiftv + DM, [&](int fr, u32x4 o) { st16<SC1>(c.ws, (unsigned)c.lane * 16 + (unsigned)fr * 1024, OFF_HB + (unsigned)li * HB_SZ, o); });
  stampl(c, 305);
  if (li >= 32 && c.wave == 0) tabw |= calibrate(c, li - 32) ^ iod;
  publish(c, FL_H + FS * li, (int)tabw);
  stamp(c, 1);
  if (li >= 24) {
#pragma unroll
    for (int i = 0; i < 20; ++i) w[i] = ld16<0>(rw, (unsigned)(((rg * 20 + i) * 3840 + 1280 + (li - 24) * 64 + cg * 8) * 2), 0);
    sb();
    gemv(std::integral_constant<int, 20>{}, rg * 20, OFF_MOD + (unsigned)(1280 + (li - 24) * 64) * 4, true);
  }
}

__device__ __forceinline__ int sp_row(int lane) { return ((lane >> 1) & 6) | (lane & 1); }
__device__ __forceinline__ int sp_half(int lane) { return (lane >> 1) & 1; }
__device__ __forceinline__ int sp_pos(int row, int half) { return 4 * (row >> 1) + 2 * half + (row & 1); }
__device__ __forceinline__ float add1(float a, float b) { float o; asm("v_add_f32 %0, %1, %2" : "=v"(o) : "v"(a), "v"(b)); return o; }
struct Gath { unsigned vo2, bvo; LAS unsigned char* w0; LAS unsigned char* w2; };
__device__ __forceinline__ Gath gather_setup(const Ctx& c, bool perm, unsigned side) {
  int r = c.lane & 7;
  if (perm) r = (r & 1) | ((r & 2) << 1) | ((r & 4) >> 1);
  const int wpos = ((c.lane & 48) + sp_pos(r, (c.lane >> 3) & 1)) * 16;
  Gath g;
  g.vo2 = c.wave < 2 ? (unsigned)c.lane * 16 : 0x80000000u;
  g.bvo = bell_vo(c, 16, side);
  g.w0 = c.lds + 2 * c.wave * 1024 + wpos;
  g.w2 = c.lds + (c.wave < 2 ? (8 + c.wave) * 1024 : 79872 + (c.wave - 2) * 1024) + wpos;
  return g;
}
__device__ __forceinline__ void gather_mem(const Ctx& c, const Gath& ga, int s, unsigned side, unsigned& nfail) {
  const unsigned lo = L_MEM + (unsigned)s * MEM_SZ;
  unsigned s0 = xr_phys(lo + c.wave * 2048, side), s2 = xr_phys(lo + 8192 + (c.wave & 1) * 1024, side);
  pin_s(s0); pin_s(s2);
  unsigned sp = 0;
  u32x2 keep = {0u, 0u};
  if (s > 0) wait_bells(c, ga.bvo, s, keep);
  if (BELL_M_GAP) __builtin_amdgcn_s_sleep(BELL_M_GAP);
  for (;;) {
    const u32x4 v0 = ld16<SC1>(c.ws, (unsigned)c.lane * 16, s0), v1 = ld16<SC1>(c.ws, (unsigned)c.lane * 16 + 1024, s0), v2 = ld16<SC1>(c.ws, ga.vo2, s2);
    sb();
    *(LAS u32x4*)(ga.w0) = v0;
    unsigned m = umax4(0u, v0);
    sb();
    *(LAS u32x4*)(ga.w0 + 1024) = v1;
    m = umax4(m, v1);
    sb();
    *(LAS u32x4*)(ga.w2) = v2;
    m = umax4(m, v2);
    if (!__any(m == SENT)) break;
    spin_check(c, sp);
  }
  asm volatile("" :: "v"(keep.x), "v"(keep.y));
  bar();
}
struct ResW { LAS unsigned char* lo; LAS unsigned char* hi; };
__device__ __forceinline__ ResW res_base(const Ctx& c) {
  ResW r; r.lo = c.lds + 81920 + stage_rd(c.lane, c.wave); r.hi = r.lo + 40960;
  pin_v(r.lo); pin_v(r.hi);
  return r;
}
__device__ __forceinline__ u32x8 res_frag(const ResW& w, int t) {
  LAS unsigned char* b = t < 5 ? w.lo + t * 8192 : w.hi + (t - 5) * 8192;
  return cat(stage_frag(b), stage_frag(b + 4096));
}
template <int CTRL> __device__ __forceinline__ unsigned dppu(unsigned x) { return (unsigned)__builtin_amdgcn_update_dpp(0, (int)x, CTRL, 0xf, 0xf, true); }
__device__ __forceinline__ float row16_max(float m) {
  asm volatile("s_nop 1\n\tv_max_f32_dpp %0, %0, %0 row_ror:8 row_mask:0xf bank_mask:0xf\n\t"
               "s_nop 1\n\tv_max_f32_dpp %0, %0, %0 row_ror:4 row_mask:0xf bank_mask:0xf\n\t"
               "s_nop 1\n\tv_max_f32_dpp %0, %0, %0 row_ror:2 row_mask:0xf bank_mask:0xf\n\t"
               "s_nop 1\n\tv_max_f32_dpp %0, %0, %0 row_ror:1 row_mask:0xf bank_mask:0xf" : "+v"(m));
  return m;
}
__device__ __forceinline__ float row16_sum(float m) {
  asm volatile("s_nop 1\n\tv_add_f32_dpp %0, %0, %0 row_ror:8 row_mask:0xf bank_mask:0xf\n\t"
               "s_nop 1\n\tv_add_f32_dpp %0, %0, %0 row_ror:4 row_mask:0xf bank_mask:0xf\n\t"
               "s_nop 1\n\tv_add_f32_dpp %0, %0, %0 row_ror:2 row_mask:0xf bank_mask:0xf\n\t"
               "s_nop 1\n\tv_add_f32_dpp %0, %0, %0 row_ror:1 row_mask:0xf bank_mask:0xf" : "+v"(m));
  return m;
}

constexpr int hmap(int id) { return (REV && id < 100) ? (9 - id / 10) * 10 + id % 10 : id; }
constexpr int vmap(int id) {
  if (!REV || id >= 100) return id;
  const int f = id / 2, a = id % 2;
  const int blk = f / 5, k = f % 5;
  return (k < 2 ? (19 - (2 * blk + k)) * 2 : 40 + (29 - (3 * blk + k - 2)) * 2) + a;
}
__device__ __forceinline__ void role_H(const Params& p, const Ctx& c, int h) {
  u32x8 W[10][5];
  unsigned tabv, kfl;
  const unsigned kmvo = (c.lane & 8) ? (unsigned)((c.lane >> 4) * 64 + (c.lane & 7) * 8 + c.wave * 256) : 0x80000000u;
#pragma unroll
  for (int i = 0; i < 6; ++i) st8<SC1>(c.ws, kmvo, OFF_KM + (unsigned)((i / 3) * NH + h) * 3072 + (unsigned)(i % 3) * 1024, u32x2{SENT, SENT});
  {
    rsrc_t rq = mk_rsrc(p.Wq, DM * NH * DH * 2), rk = mk_rsrc(p.Wk, DM * NH * DH * 2);
    const unsigned src = stage_src<1>(c, NH * DH), rd = stage_rd(c.lane, c.wave);
    stage_weights<12, 2, 10, 4>(c,
      [&](auto ci, LAS unsigned char* dst) {
        constexpr int id = hmap(ci);
        if constexpr (id < 100) {
          constexpr int t = id / 10, pn = (id % 10) / 2, a = id % 2;
          constexpr int col = (pn < 3 ? pn : pn - 3) * 64;
          dma16<0>(pn < 3 ? rq : rk, dst, src, (unsigned)(((64 * t + 32 * a) * NH * DH + h * DH + col) * 2));
        } else {
          constexpr int sp = id - 100;
          dma16<0>(rk, dst, src, (unsigned)(((32 * sp) * NH * DH + h * DH + 128) * 2));
        }
      },
      [&](auto ci, LAS unsigned char* chunk) {
        constexpr int id = hmap(ci), t = id / 10, pn = (id % 10) / 2, a = id % 2;
        if constexpr (a == 1) {
          W[t][pn] = cat(stage_frag(chunk - 4096 + rd), stage_frag(chunk + rd));
        }
      },
      [&](auto ci) {
        constexpr int id = hmap(ci), t = id / 10, pn = (id % 10) / 2, a = id % 2;
        if constexpr (a == 1) { if constexpr (t * 5 + pn < 32) pin_a(W[t][pn]); else pin_v(W[t][pn]); }
      },
      [&](auto ci) { if constexpr (ci == 80) { tabv = ld_table(c); kfl = ld4<SC1>(c.ws, FL_K + FS * h, 0); } });
  }
  if (c.tid == 0) st4<SC1>(c.ws, FL_KM + FS * h, 0, 0u);
  Sides sd;
  get_sides(c, sd, tabv);
  LAS unsigned char* A = c.lds + c.lane * 16;
  const int g = c.lane >> 4, n = c.lane & 15;
  LAS unsigned char* TW = c.lds + 10240 + c.wave * 1920 + n * 40 + g * 4;
  LAS unsigned char* TR = c.lds + 10240 + c.wave * 1920 + (c.lane >> 2) * 40 + (c.lane & 3) * 8;
  LAS unsigned char* PART = c.lds + 49152;
  LAS unsigned char* PW = PART + c.wave * 3328 + n * 208 + g * 16;
  const ResW WR = res_base(c);
  const int idx = (c.lane & 2) ? 0xEEEE : 0x4444;
  const int q = (c.wave & 1) * 4 + g, it = min(n, 9);
  LAS unsigned char* PR = PART + q * 208 + (it < 8 ? it : it + 2) * 16;
  const float lmask = n < 10 ? 1.f : 0.f;
  const unsigned plo = it < 8 ? (unsigned)((h >> 1) * 1024 + (it >> 2) * 512 + (h & 1) * 256 + sp_pos(q, (it >> 1) & 1) * 16) : (unsigned)(16384 + h * 128 + q * 16);
  const bool ringer = USE_BELL_P && h % (32 / NBELL) == 0 && c.wave == 0;
  const Gath ga = gather_setup(c, false, sd.me);
  const unsigned rvo = (ringer && c.lane < 2) ? OFF_DB + (unsigned)(h / (32 / NBELL)) * FS + (unsigned)c.lane * 8192u : 0x80000000u;
  unsigned nfail = 0;
  LAS unsigned char* KX = c.lds + 24576;
  int kready = (int)__builtin_amdgcn_readfirstlane(kfl);
  auto dma_kx = [&](int s) {
    if (kready < s) kready = wait_flags<2>(c, FL_K + FS * h, 1, s);
#pragma unroll
    for (int i = 0; i < 6; ++i)
      dma16<SC1>(c.ws, KX + (s & 1) * KX_SZ + (c.wave * 6 + i) * 1024, (unsigned)c.lane * 16, OFF_KX + (unsigned)(s * NH + h) * KX_SZ + (unsigned)(c.wave * 6 + i) * 1024);
  };
  stamp(c, 1);
  if (c.wave < 2) dma_kx(0);
  for (int s = 0; s < NSEG; ++s) {
    unsigned pvo = xr_phys(L_P + (unsigned)s * P_SZ + plo, n & 1);
    if (n >= 10) pvo = 0x80000000u;
    f32x4 acc[6] = {};
    u32x8 wl[10];
    wl[0] = res_frag(WR, 0); wl[1] = res_frag(WR, 1);
#pragma unroll
    for (int i = 0; i < 6; ++i) pin_v(acc[i]);
    pin_v(wl[0]); pin_v(wl[1]); pin_v(pvo);
    gather_mem(c, ga, s, sd.me, nfail);
    stampf(c, 4 + 4 * s, 128);
    CYC(t0)
    u32x4 a[10], kx[3];
    a[0] = lds16(A); a[1] = lds16(A + 1024); a[2] = lds16(A + 2048);
    sb();
#pragma unroll
    for (int t = 0; t < 6; ++t) {
      LAS unsigned char* wb = t + 2 < 5 ? WR.lo + (t + 2) * 8192 : WR.hi + (t + 2 - 5) * 8192;
      acc[5] = smfma(a[t], wl[t], acc[5], idx); sb();
      a[t + 3] = lds16(A + (t + 3) * 1024); sb();
      acc[0] = smfma(a[t], W[t][0], acc[0], idx); sb();
      const u32x2 w0 = tr8(wb); sb();
      acc[1] = smfma(a[t], W[t][1], acc[1], idx); sb();
      const u32x2 w1 = tr8(wb + 1024); sb();
      acc[2] = smfma(a[t], W[t][2], acc[2], idx); sb();
      const u32x2 w2 = tr8(wb + 4096); sb();
      acc[3] = smfma(a[t], W[t][3], acc[3], idx); sb();
      const u32x2 w3 = tr8(wb + 5120); sb();
      acc[4] = smfma(a[t], W[t][4], acc[4], idx); sb();
      wl[t + 2] = cat(cat(w0, w1), cat(w2, w3));
    }
    CYC(t1)
    f32x4 sx0, sx1, sm;
    u32x2 F[3];
    float v[4];
#define H_S(i, t) acc[i] = smfma(a[t], W[t][i], acc[i], idx); sb();
#define H_L(t) acc[5] = smfma(a[t], wl[t], acc[5], idx); sb();
#define H_A(i, r) v[r] = add1(acc[(r >> 1) * 3 + i][r & 1], acc[(r >> 1) * 3 + i][2 + (r & 1)]); sb();
#define H_K(i) kx[i] = lds16(KX + (s & 1) * KX_SZ + (4 * i + c.wave) * 1024 + c.lane * 16); sb();
#define H_MZ(d, x, i) asm volatile("v_mfma_f32_16x16x16_bf16 %0, %1, %2, 0" : "=&v"(d) : "v"(x), "v"(F[i])); sb();
#define H_MA(d, x, i) asm volatile("v_mfma_f32_16x16x16_bf16 %0, %1, %2, %0" : "+v"(d) : "v"(x), "v"(F[i])); sb();
#define KLO(i) (u32x2{kx[i].x, kx[i].y})
#define KHI(i) (u32x2{kx[i].z, kx[i].w})
#define H_C(r) pw[r] = pk(v[2 * r], v[2 * r + 1]); sb();
#define H_D(i) *(LAS unsigned*)(TW + i * 640) = pw[0]; *(LAS unsigned*)(TW + i * 640 + 16) = pw[1]; F[i] = tr8(TR + i * 640); sb();
    unsigned pw[2];
    {
      LAS unsigned char* wb = WR.hi + 3 * 8192;
      H_S(1, 6) a[9] = lds16(A + 9 * 1024); const u32x2 w0 = tr8(wb); sb(); H_S(4, 6) const u32x2 w1 = tr8(wb + 1024); sb();
      H_S(1, 7) const u32x2 w2 = tr8(wb + 4096); sb(); H_S(4, 7) const u32x2 w3 = tr8(wb + 5120); sb();
      wl[8] = cat(cat(w0, w1), cat(w2, w3));
      H_S(1, 8) H_S(4, 8) H_S(1, 9) H_S(4, 9)
      H_L(6) const u32x2 w4 = tr8(wb + 8192), w5 = tr8(wb + 9216); sb(); H_S(2, 6) const u32x2 w6 = tr8(wb + 12288), w7 = tr8(wb + 13312); sb();
      wl[9] = cat(cat(w4, w5), cat(w6, w7));
    }
    H_L(7) H_A(1, 0) H_K(1) H_S(2, 7) H_A(1, 1) H_L(8) H_A(1, 2) H_S(2, 8) H_A(1, 3) H_S(2, 9) H_C(0) H_K(2) H_L(9) H_C(1)
    H_S(0, 6) H_D(1) H_S(3, 6) H_K(0) H_S(0, 7) H_A(2, 0) H_S(3, 7) H_A(2, 1) H_S(0, 8) H_A(2, 2) H_S(3, 8) H_A(2, 3) H_S(0, 9) H_C(0) H_S(3, 9) H_C(1) H_D(2)
    H_MZ(sx0, KLO(1), 1)
    stampf(c, 5 + 4 * s, 128);
    CYC(t2)
    asm volatile("s_nop 1" : "+v"(acc[0]));
    H_A(0, 0) H_A(0, 1) H_A(0, 2) H_A(0, 3) H_C(0) H_C(1) H_D(0) H_MZ(sx1, KHI(1), 1)
    if (BELL_P_POS == 5) { st4<SC1>(c.ws, rvo, 0, (unsigned)s); sb(); }
    H_MZ(sm, F[1], 1)
    H_MA(sx0, KLO(2), 2) H_MA(sx1, KHI(2), 2) H_MA(sm, F[2], 2)
    H_MA(sx0, KLO(0), 0) H_MA(sx1, KHI(0), 0) H_MA(sm, F[0], 0)
    asm volatile("s_nop 5" : "+v"(sx0), "+v"(sx1));
    CYC(t3)
    if (s < NSEG - 1) {
      *(LAS f32x4*)(PW) = sx0; sb(); *(LAS f32x4*)(PW + 64) = sx1; sb(); *(LAS f32x4*)(PW + 128) = sm;
      bar();
      CYC(t4)
      if (__builtin_expect(c.wave >= 2, 1)) {
        const f32x4 p0 = *(LAS f32x4*)PR, p1 = *(LAS f32x4*)(PR + 3328), p2 = *(LAS f32x4*)(PR + 6656), p3 = *(LAS f32x4*)(PR + 9984);
        const f32x4 xa = p0 + p1, xb = p2 + p3;
        CYC(t4a)
        u32x4 o;
        float x0, x1, x2, x3, ml, mm, t, d;
        asm volatile(
            "v_add_f32 %4, %12, %16\n\tv_add_f32 %5, %13, %17\n\tv_add_f32 %6, %14, %18\n\tv_add_f32 %7, %15, %19\n\t"
            "v_max3_f32 %8, %4, %5, %6\n\tv_max_f32 %8, %8, %7\n\t"
            "v_mul_f32 %10, %20, %8\n\tv_fma_f32 %4, %4, %20, -%10\n\t"
            "v_max_f32_dpp %9, %8, %8 row_ror:8 row_mask:0xf bank_mask:0xf\n\t"
            "v_fma_f32 %5, %5, %20, -%10\n\tv_fma_f32 %6, %6, %20, -%10\n\t"
            "v_max_f32_dpp %9, %9, %9 row_ror:4 row_mask:0xf bank_mask:0xf\n\t"
            "v_fma_f32 %7, %7, %20, -%10\n\tv_exp_f32 %4, %4\n\t"
            "v_max_f32_dpp %9, %9, %9 row_ror:2 row_mask:0xf bank_mask:0xf\n\t"
            "v_exp_f32 %5, %5\n\tv_exp_f32 %6, %6\n\t"
            "v_max_f32_dpp %9, %9, %9 row_ror:1 row_mask:0xf bank_mask:0xf\n\t"
            "v_exp_f32 %7, %7\n\t"
            "v_fma_f32 %11, %9, -%20, %10\n\t"
            "v_add_f32 %8, %4, %5\n\t"
            "v_exp_f32 %11, %11\n\t"
            "v_add_f32 %9, %6, %7\n\t"
            "v_add_f32 %8, %8, %9\n\t"
            "v_mul_f32 %10, %11, %21\n\t"
            "v_mul_f32 %8, %8, %10\n\t"
            "v_mul_f32 %4, %4, %11\n\tv_mul_f32 %5, %5, %11\n\t"
            "v_add_f32_dpp %8, %8, %8 row_ror:8 row_mask:0xf bank_mask:0xf\n\t"
            "v_mul_f32 %6, %6, %11\n\tv_mul_f32 %7, %7, %11\n\t"
            "v_add_f32_dpp %8, %8, %8 row_ror:4 row_mask:0xf bank_mask:0xf\n\t"
            "s_nop 1\n\t"
            "v_add_f32_dpp %8, %8, %8 row_ror:2 row_mask:0xf bank_mask:0xf\n\t"
            "s_nop 1\n\t"
            "v_add_f32_dpp %8, %8, %8 row_ror:1 row_mask:0xf bank_mask:0xf\n\t"
            "v_rcp_f32 %8, %8\n\t"
            "s_nop 0\n\t"
            "v_mul_f32 %4, %4, %8\n\tv_mul_f32 %5, %5, %8\n\tv_mul_f32 %6, %6, %8\n\tv_mul_f32 %7, %7, %8\n\t"
            "v_cvt_pk_bf16_f32 %9, %4, %5\n\tv_cvt_pk_bf16_f32 %10, %6, %7\n\t"
            "s_nop 0\n\t"
            "v_min_u32_dpp %0, %9, %22 quad_perm:[0,0,2,2] row_mask:0xf bank_mask:0xf\n\t"
            "v_min_u32_dpp %2, %9, %22 quad_perm:[1,1,3,3] row_mask:0xf bank_mask:0xf\n\t"
            "v_min_u32_dpp %1, %10, %22 quad_perm:[0,0,2,2] row_mask:0xf bank_mask:0xf\n\t"
            "v_min_u32_dpp %3, %10, %22 quad_perm:[1,1,3,3] row_mask:0xf bank_mask:0xf"
            : "=&v"(o.x), "=&v"(o.y), "=&v"(o.z), "=&v"(o.w), "=&v"(x0), "=&v"(x1), "=&v"(x2), "=&v"(x3), "=&v"(ml), "=&v"(mm), "=&v"(t), "=&v"(d)
            : "v"(xa[0]), "v"(xa[1]), "v"(xa[2]), "v"(xa[3]), "v"(xb[0]), "v"(xb[1]), "v"(xb[2]), "v"(xb[3]),
              "s"(0.07216878364870322f * 1.4426950408889634f), "v"(lmask), "v"(0xFFFFFFFEu));
        CYC(t4b)
        st16<SC1>(c.ws, pvo, 0, o);
        stampf(c, 6 + 4 * s, 128);
        CYC(t5)
        if (H_D2) __builtin_amdgcn_s_sleep(H_D2);
        if (n < 10 && s + 2 < NSEG - 1) st16<SC1>(c.ws, xr_phys(L_P + (unsigned)(s + 2) * P_SZ + plo, n & 1), 0, u32x4{SENT, SENT, SENT, SENT});
      } else {
            if (H_D0) __builtin_amdgcn_s_sleep(H_D0);
        dma_kx(s + 1);
        if (H_D1) __builtin_amdgcn_s_sleep(H_D1);
      }
    }
#pragma unroll
    for (int i = 0; i < 3; ++i) st8<SC1>(c.ws, kmvo, OFF_KM + (unsigned)(s * NH + h) * 3072 + (unsigned)i * 1024, nosent(F[i]));
    if (KM_BELL && c.tid == 0) st4<SC1>(c.ws, FL_KM + FS * h, 0, (unsigned)(s + 1));
    if (s + 2 < NSEG) {
#pragma unroll
      for (int i = 0; i < 3; ++i) st8<SC1>(c.ws, kmvo, OFF_KM + (unsigned)((s + 2) * NH + h) * 3072 + (unsigned)i * 1024, u32x2{SENT, SENT});
    }
    __builtin_amdgcn_s_sleep(H_SLEEP);
  }
}

__device__ __forceinline__ void role_V(const Params& p, const Ctx& c, int h) {
  u32x8 Wv[10][2];
  u32x8 Wo[10][3];
  unsigned tabv;
  {
    rsrc_t rv = mk_rsrc(p.Wv, DM * NH * DH * 2), ro = mk_rsrc(p.Wo, DM * NH * DH * 2);
    const unsigned srcv = stage_src<1>(c, NH * DH), srco = stage_src<1>(c, DM), rd = stage_rd(c.lane, c.wave);
    stage_weights<12, 2, 10, 4>(c,
      [&](auto ci, LAS unsigned char* dst) {
        constexpr int id = vmap(ci);
        if constexpr (id < 40) {
          constexpr int t = id / 4, pn = (id % 4) / 2, a = id % 2;
          dma16<0>(rv, dst, srcv, (unsigned)(((64 * t + 32 * a) * NH * DH + h * DH + pn * 64) * 2));
        } else if constexpr (id < 100) {
          constexpr int f = (id - 40) / 2, a = id % 2, t = f / 10, pn = f % 10;
          dma16<0>(ro, dst, srco, (unsigned)(((h * DH + 64 * t + 32 * a) * DM + pn * 64) * 2));
        } else {
          constexpr int sp = id - 100;
          dma16<0>(rv, dst, srcv, (unsigned)(((32 * sp) * NH * DH + h * DH + 128) * 2));
        }
      },
      [&](auto ci, LAS unsigned char* chunk) {
        constexpr int id = vmap(ci);
        if constexpr (id % 2 == 1) {
          u32x8 f = cat(stage_frag(chunk - 4096 + rd), stage_frag(chunk + rd));
          if constexpr (id < 40) Wv[id / 4][(id % 4) / 2] = f;
          else Wo[((id - 40) / 2) % 10][((id - 40) / 2) / 10] = f;
        }
      },
      [&](auto ci) {
        constexpr int id = vmap(ci);
        if constexpr (id % 2 == 1) {
          if constexpr (id < 36) pin_v(Wv[id / 4][(id % 4) / 2]);
          else if constexpr (id < 40) pin_a(Wv[id / 4][(id % 4) / 2]);
          else pin_a(Wo[((id - 40) / 2) % 10][((id - 40) / 2) / 10]);
        }
      },
      [&](auto ci) { if constexpr (ci == 80) tabv = ld_table(c); });
  }
  Sides sd;
  get_sides(c, sd, tabv);
  LAS unsigned char* A = c.lds + c.lane * 16;
  const ResW WR = res_base(c);
  const int idx = (c.lane & 2) ? 0xEEEE : 0x4444;
  const int g = c.lane >> 4, n = c.lane & 15;
  LAS unsigned char* TW = c.lds + 10240 + c.wave * 256 + ((n & 7) * 4 + g) * 8 + (n >> 3) * 4;
  LAS unsigned char* TR = c.lds + 10240 + g * 256 + n * 8;
  unsigned nfail = 0;
  const Gath ga = gather_setup(c, true, sd.me);
  unsigned mj[3], ms[3];
#pragma unroll
  for (int u = 0; u < 3; ++u) {
    const int pn = u < 2 ? 4 * u + g : 8 + (g & 1);
    mj[u] = (unsigned)(4 * pn + c.wave);
    ms[u] = pz_side(sd, (int)mj[u]);
  }
  stamp(c, 1);
  for (int s = 0; s < NSEG; ++s) {
    unsigned mvo[3];
#pragma unroll
    for (int u = 0; u < 3; ++u) {
      mvo[u] = m_phys((unsigned)s, mj[u], ms[u]) + (unsigned)(h * 256 + n * 16);
      if (u == 2 && g >= 2) mvo[u] = 0x80000000u;
      pin_v(mvo[u]);
    }
    f32x4 acc[3] = {};
    constexpr int NW = V_NW, NA = V_NA;
    u32x8 wl[NW];
#pragma unroll
    for (int i = 0; i < NW; ++i) wl[i] = res_frag(WR, i);
#pragma unroll
    for (int i = 0; i < NW; ++i) pin_v(wl[i]);
#pragma unroll
    for (int i = 0; i < 3; ++i) pin_v(acc[i]);
    gather_mem(c, ga, s, sd.me, nfail);
    stampf(c, 4 + 4 * s);
    CYCW(u0)
    u32x4 a[NA];
#pragma unroll
    for (int t = 0; t < NA; ++t) a[t] = lds16(A + t * 1024);
    sb();
#pragma unroll
    for (int t = 0; t < 10; ++t) {
      LAS unsigned char* wb = t + NW < 5 ? WR.lo + (t + NW) * 8192 : WR.hi + (t + NW - 5) * 8192;
      acc[2] = smfma(a[t % NA], wl[t % NW], acc[2], idx);
      sb();
      if (t + NW < 10) {
        const u32x2 w0 = tr8(wb), w1 = tr8(wb + 1024);
        wl[t % NW][0] = w0.x; wl[t % NW][1] = w0.y; wl[t % NW][2] = w1.x; wl[t % NW][3] = w1.y;
      }
      sb();
      acc[0] = smfma(a[t % NA], Wv[t][0], acc[0], idx);
      sb();
      if (t + NW < 10) {
        const u32x2 w2 = tr8(wb + 4096), w3 = tr8(wb + 5120);
        wl[t % NW][4] = w2.x; wl[t % NW][5] = w2.y; wl[t % NW][6] = w3.x; wl[t % NW][7] = w3.y;
      }
      sb();
      acc[1] = smfma(a[t % NA], Wv[t][1], acc[1], idx);
      sb();
      if (t + NA < 10) a[t % NA] = lds16(A + (t + NA) * 1024);
      sb();
    }
    CYCW(u1)
    f32x4 m[10] = {};
    *(LAS unsigned*)(TW + 2048) = pk(acc[2][0] + acc[2][2], acc[2][1] + acc[2][3]);
    *(LAS unsigned*)(TW) = pk(acc[0][0] + acc[0][2], acc[0][1] + acc[0][3]);
    *(LAS unsigned*)(TW + 1024) = pk(acc[1][0] + acc[1][2], acc[1][1] + acc[1][3]);
    sb();
#pragma unroll
    for (int i = 0; i < 4; ++i) pin_v(m[i]);
    bar();
    CYCW(u2)
    u32x4 a2[3];
#pragma unroll
    for (int t = 0; t < 3; ++t) a2[t] = cat(tr8(TR + t * 1024), tr8(TR + t * 1024 + 128));
    sb();
#pragma unroll
    for (int i = 4; i < 10; ++i) pin_v(m[i]);
    sb();
    float v0, v1; unsigned x[10];
    auto xpose = [&](int u) {
      const int i2 = u < 2 ? 4 * u + 2 : 8, i3 = u < 2 ? 4 * u + 3 : 9;
      auto r0 = __builtin_amdgcn_permlane32_swap(x[4 * u], x[i2], false, false);
      auto r1 = __builtin_amdgcn_permlane32_swap(x[4 * u + 1], x[i3], false, false);
      auto q0 = __builtin_amdgcn_permlane16_swap(r0[0], r1[0], false, false);
      auto q1 = __builtin_amdgcn_permlane16_swap(r0[1], r1[1], false, false);
      st16<SC1>(c.ws, mvo[u], 0, u32x4{q0[0], q0[1], q1[0], q1[1]});
    };
    sfor<0, 36>([&](auto ic) {
      constexpr int i = ic, tl = (i / 6) * 2 + (i & 1), t = (i % 6) / 2;
      if constexpr (i < 30) { m[tl] = smfma(a2[t], Wo[tl][t], m[tl], idx); sb(); }
      else if constexpr (i == 30) { asm volatile("s_nop 3" : "+v"(m[8]), "+v"(m[9])); sb(); }
      if constexpr (i >= 8 || (i >= 6 && false)) {
        constexpr int k = i - 8, pr = k / 6, w = k % 6;
        (void)pr; (void)w;
      }
      constexpr int k = i - 7;
      if constexpr (k >= 0) {
        constexpr int pr = k / 6, w = k % 6, ta = 2 * pr + (w >= 3 ? 1 : 0), ww = w % 3;
        if constexpr (pr < 5) {
          if constexpr (ww == 0) { v0 = add1(m[ta][0], m[ta][2]); sb(); }
          if constexpr (ww == 1) { v1 = add1(m[ta][1], m[ta][3]); sb(); }
          if constexpr (ww == 2) { x[ta] = pks(v0, v1); sb(); }
          if constexpr (w == 5 && (pr & 1)) { xpose(pr >> 1); sb(); }
        }
      }
    });
    x[9] = pks(v0, v1);
    xpose(2);
    CYCW(u3)
    CYCOUT(s, u0, u1, u2, u3, u3, u3, u3)
    stampf(c, 6 + 4 * s);
    __builtin_amdgcn_s_sleep(V_SLEEP);
  }
}

__device__ __forceinline__ void role_PZ(const Params& p, const Ctx& c, int j) {
  Sides sd;
  get_sides(c, sd);
  const unsigned mside = pz_side(sd, j);
  LAS unsigned char* R = c.lds;
  const int idx = (c.lane & 2) ? 0xEEEE : 0x4444;
  const int g = c.lane >> 4, n = c.lane & 15;
  int uready = -1;
  const unsigned l16 = (unsigned)c.lane * 16;
  const unsigned pmvo = (unsigned)(2 * g * 128 + sp_row(c.lane) * 16 + sp_half(c.lane) * 8);
  const u32x4 sv = {SENT, SENT, SENT, SENT};
  LAS float* rw = (LAS float*)(R + c.wave * 512) + 2 * g * 16 + n;
  LAS unsigned char* rp = R + (((c.lane >> 2) & 7) * 16 + (c.lane >> 5) * 8 + (c.lane & 3) * 2) * 4;
  const unsigned olo = (unsigned)(j * 256 + (c.lane >> 2) * 16);
  const bool ringer = USE_BELL_M && j % (40 / NBELL) == 0 && j / (40 / NBELL) < NBELL && c.wave == 3;
  const unsigned bvo = bell_vo(c, 0, sd.me);
  const unsigned rvo = (ringer && c.lane < 2) ? OFF_DB + (unsigned)(16 + j / (40 / NBELL)) * FS + (unsigned)c.lane * 8192u : 0x80000000u;
  unsigned nfail = 0; (void)nfail;
  bar();
  for (int s = 0; s < NSEG - 1; ++s) {
    if (uready < s) uready = wait_flags<2>(c, FL_U + 8 * FS * c.wave, 8, s);
    u32x4 u[8];
#pragma unroll
    for (int i = 0; i < 8; ++i) u[i] = ld16<SC1>(c.ws, l16, OFF_U + (unsigned)s * U_SZ + (unsigned)(j * 32 + 8 * c.wave + i) * 1024);
    u32x4 pa[4], mb[2]; u32x2 pm[2];
    const unsigned lp = L_P + (unsigned)s * P_SZ;
    unsigned sop = xr_phys(lp + (unsigned)c.wave * 4096, sd.me), som5 = xr_phys(lp + 16384 + (unsigned)c.wave * 1024, sd.me);
    pin_s(sop); pin_s(som5);
    unsigned som = m_phys((unsigned)s, (unsigned)j, mside) + (unsigned)c.wave * 2048;
    unsigned ovo = xr_phys(L_MEM + (unsigned)(s + 1) * MEM_SZ + olo, c.lane & 1);
    if ((c.lane & 2) || c.wave != 0) ovo = 0x80000000u;
    f32x4 acc;
    LAS float* rwb = rw + (s & 1) * 512;
    LAS unsigned char* rpb = rp + (s & 1) * 2048;
    pin_v(ovo); pin_v(rwb); pin_v(rpb);
    unsigned sp = 0;
    pin_s(som);
    u32x2 keep = {0u, 0u};
    wait_bells(c, bvo, s, keep);
    if (BELL_P_GAP0) __builtin_amdgcn_s_sleep(BELL_P_GAP0);
    mb[0] = ld16<SC1>(c.ws, l16, som); mb[1] = ld16<SC1>(c.ws, l16 + 1024, som);
    sb();
    if (BELL_P_GAP) __builtin_amdgcn_s_sleep(BELL_P_GAP);
    for (;;) {
      pm[0] = ld8<SC1>(c.ws, pmvo, som5); pm[1] = ld8<SC1>(c.ws, pmvo + 128, som5);
#pragma unroll
      for (int i = 0; i < 4; ++i) pa[i] = ld16<SC1>(c.ws, l16 + i * 1024, sop);
      sb();
      acc = f32x4{0.f, 0.f, 0.f, 0.f};
      pin_v(acc);
      acc = smfma(cat(pm[0], pm[1]), cat(mb[0], mb[1]), acc, idx);
      unsigned m = umax4(0u, cat(pm[0], pm[1]));
      m = umax4(umax4(m, mb[0]), mb[1]);
      sb();
#pragma unroll
      for (int i = 0; i < 4; ++i) {
        if (BELL_M_POS == 4 + i) { st4<SC1>(c.ws, rvo, 0, (unsigned)(s + 1)); sb(); }
        acc = smfma(pa[i], cat(u[2 * i], u[2 * i + 1]), acc, idx); m = umax4(m, pa[i]); sb();
      }
      if (!__any(m == SENT)) break;
      spin_check(c, sp);
      mb[0] = ld16<SC1>(c.ws, l16, som); mb[1] = ld16<SC1>(c.ws, l16 + 1024, som);
    }
    asm volatile("" :: "v"(keep.x), "v"(keep.y));
    stampf(c, 4 + 4 * s);
    rwb[0] = acc[0] + acc[2]; rwb[16] = acc[1] + acc[3];
    sb();
    if (BELL_M_POS == 2) st4<SC1>(c.ws, rvo, 0, (unsigned)(s + 1));
    bar();
    {
      const f32x2 v = (*(LAS f32x2*)(rpb) + *(LAS f32x2*)(rpb + 512)) + (*(LAS f32x2*)(rpb + 1024) + *(LAS f32x2*)(rpb + 1536));
      const unsigned d = pks(v[0], v[1]);
      st16<SC1>(c.ws, ovo, 0, u32x4{dppu<0x00>(d), dppu<0x55>(d), dppu<0xAA>(d), dppu<0xFF>(d)});
      stamp(c, 6 + 4 * s);
    }
    __builtin_amdgcn_s_sleep(PZ_SLEEP);
    if (c.wave == 0 && !(c.lane & 2) && s + 3 < NSEG) st16<SC1>(c.ws, xr_phys(L_MEM + (unsigned)(s + 3) * MEM_SZ + olo, c.lane & 1), 0, sv);
    if (s + 2 < NSEG) {
      const unsigned sa = m_phys((unsigned)(s + 2), (unsigned)j, mside) + (unsigned)c.wave * 2048;
      st16<SC1>(c.ws, l16, sa, sv); st16<SC1>(c.ws, l16 + 1024, sa, sv);
    }
  }
}

__device__ __forceinline__ void load_proj_w(const Ctx& c, const void* Wm, int h, u32x4 (&W)[3][20]) {
  rsrc_t rw = mk_rsrc(Wm, DM * NH * DH * 2);
  const unsigned src = stage_src<0>(c, NH * DH), rd = stage_rd(c.lane, c.wave);
  stage_weights<5, 0, 12, 3>(c,
    [&](auto ci, LAS unsigned char* dst) {
      constexpr int id = ci, kap = id / 3, pn = id % 3;
      dma16<0>(rw, dst, src, (unsigned)(((32 * kap) * NH * DH + h * DH + pn * 64) * 2));
    },
    [&](auto ci, LAS unsigned char* chunk) {
      constexpr int id = ci;
      W[id % 3][id / 3] = stage_frag(chunk + rd);
    },
    [&](auto ci) { constexpr int id = ci; pin_a(W[id % 3][id / 3]); }, [](auto) {});
}
__device__ __forceinline__ void dma_hb(const Ctx& c, int s, LAS unsigned char* slot) {
#pragma unroll
  for (int i = 0; i < 10; ++i)
    dma16<SC1>(c.ws, slot + (c.wave * 10 + i) * 1024, (unsigned)c.lane * 16, OFF_HB + (unsigned)s * HB_SZ + (unsigned)(c.wave * 10 + i) * 1024);
}
template <bool WA, bool WFIRST, bool ZERO>
__device__ __forceinline__ void mfma_v(f32x4& d, const u32x4& w, const u32x4& x) {
  if constexpr (ZERO) {
    if constexpr (WA && WFIRST) asm volatile("v_mfma_f32_16x16x32_bf16 %0, %1, %2, 0" : "=&v"(d) : "a"(w), "v"(x));
    else if constexpr (WA) asm volatile("v_mfma_f32_16x16x32_bf16 %0, %2, %1, 0" : "=&v"(d) : "a"(w), "v"(x));
    else if constexpr (WFIRST) asm volatile("v_mfma_f32_16x16x32_bf16 %0, %1, %2, 0" : "=&v"(d) : "v"(w), "v"(x));
    else asm volatile("v_mfma_f32_16x16x32_bf16 %0, %2, %1, 0" : "=&v"(d) : "v"(w), "v"(x));
  } else {
    if constexpr (WA && WFIRST) asm volatile("v_mfma_f32_16x16x32_bf16 %0, %1, %2, %0" : "+v"(d) : "a"(w), "v"(x));
    else if constexpr (WA) asm volatile("v_mfma_f32_16x16x32_bf16 %0, %2, %1, %0" : "+v"(d) : "a"(w), "v"(x));
    else if constexpr (WFIRST) asm volatile("v_mfma_f32_16x16x32_bf16 %0, %1, %2, %0" : "+v"(d) : "v"(w), "v"(x));
    else asm volatile("v_mfma_f32_16x16x32_bf16 %0, %2, %1, %0" : "+v"(d) : "v"(w), "v"(x));
  }
}
__device__ __forceinline__ void mfma_vn(f32x4& d, const u32x4& a, const u32x4& b) {
  asm volatile("s_nop 1\n\tv_mfma_f32_16x16x32_bf16 %0, %1, %2, %0" : "+v"(d) : "v"(a), "v"(b));
}
__device__ __forceinline__ void mfma_fence() { sb(); asm volatile("s_nop 7"); sb(); }

template <int NT, int K0, int K1, class Hook>
__device__ __forceinline__ void gemm_part(const u32x4 (&W)[NT][20], LAS unsigned char* hb, f32x4 (&acc)[NT][2], Hook&& hook) {
  u32x4 b[K1 - K0][2];
#pragma unroll
  for (int k = 0; k < 2; ++k) { b[k][0] = lds16(hb + (2 * (K0 + k)) * 1024); b[k][1] = lds16(hb + (2 * (K0 + k) + 1) * 1024); }
  sb();
  sfor<0, K1 - K0>([&](auto kc) {
    constexpr int k = kc, ka = K0 + k;
    sfor<0, 2 * NT>([&](auto ic) {
      constexpr int t = ic / NT, i = ic % NT;
      mfma_v<(ka * NT + i < 64), true, ka == 0>(acc[i][t], W[i][ka], b[k][t]);
      sb();
      if constexpr (ic < 2 && k + 2 < K1 - K0) { b[k + 2][ic] = lds16(hb + (2 * (ka + 2) + ic) * 1024); sb(); }
      if constexpr (ic == 2) { hook(kc); sb(); }
    });
  });
}
__device__ __forceinline__ void dma_hb1(const Ctx& c, int s, LAS unsigned char* slot, int i) {
  dma16<SC1>(c.ws, slot + (c.wave * 10 + i) * 1024, (unsigned)c.lane * 16, OFF_HB + (unsigned)s * HB_SZ + (unsigned)(c.wave * 10 + i) * 1024);
}
__device__ __forceinline__ u32x4 pack2(const f32x4& a, const f32x4& b) { return u32x4{pk(a[0], a[1]), pk(a[2], a[3]), pk(b[0], b[1]), pk(b[2], b[3])}; }

__device__ __forceinline__ void need_hb(const Ctx& c, unsigned long long& mask, int s) {
  if (c.wave != 0 || ((mask >> s) & 1ull)) return;
  unsigned sp = 0;
  for (;;) {
    mask = __ballot(ld4<SC1>(c.ws, FL_H + FS * (unsigned)c.lane, 0) != SENT);
    if ((mask >> s) & 1ull) return;
    __builtin_amdgcn_s_sleep(4);
    spin_check(c, sp);
  }
}

__device__ __forceinline__ void role_KV(const Params& p, const Ctx& c, int h, bool isV) {
  u32x4 W[3][20];
  load_proj_w(c, isV ? p.Wv : p.Wk, h, W);
  stamp(c, 1);
  unsigned long long hm = 0;
  need_hb(c, hm, 0);
  bar();
  stamp(c, 2);
  const unsigned obase = isV ? OFF_VX : OFF_KX, fl = (isV ? FL_V : FL_K) + FS * h;
  dma_hb(c, 0, c.lds);
  need_hb(c, hm, 1);
  bar();
  dma_hb(c, 1, c.lds + HB_SZ);
  auto kst = [&](int s, int i, const f32x4 (&a)[3][2]) {
    st16<KV_AUX>(c.ws, (unsigned)c.lane * 16, obase + (unsigned)(s * NH + h) * KX_SZ + (unsigned)(4 * i + c.wave) * 1024, pack2(a[i][0], a[i][1]));
  };
  f32x4 accA[3][2], accB[3][2];
#pragma unroll 1
  for (int s = 0; s < 2; ++s) {
    need_hb(c, hm, s + 2);
    if (s == 0) wait_vm<10>();
    bar();
    gemm_part<3, 0, 20>(W, c.lds + s * HB_SZ + c.lane * 16, accA, [&](auto k) { if constexpr (k < 10) dma_hb1(c, s + 2, c.lds + (s == 0 ? 2 : 0) * HB_SZ, k); });
    mfma_fence();
#pragma unroll
    for (int i = 0; i < 3; ++i) kst(s, i, accA);
    publish(c, fl, s);
    stamp(c, 4 + s);
  }
  int sl = 2;
  auto seg = [&](int s, f32x4 (&cur)[3][2], const f32x4 (&prev)[3][2], bool hasprev) {
    const int s2 = min(s + 2, NSEG - 1);
    need_hb(c, hm, s2);
    wait_vm<10>();
    bar();
    if (c.tid == 0 && s >= 4) st4<SC1>(c.ws, fl, 0, (unsigned)(s - 2));
    const int sl2 = sl == 0 ? 2 : sl - 1;
    gemm_part<3, 0, 20>(W, c.lds + sl * HB_SZ + c.lane * 16, cur, [&](auto k) {
      if constexpr (k >= 2 && k < 5) { if (hasprev) kst(s - 1, k - 2, prev); }
      if constexpr (k >= 6 && k < 16) dma_hb1(c, s2, c.lds + sl2 * HB_SZ, k - 6);
    });
    stamp(c, 4 + s);
    sl = sl == 2 ? 0 : sl + 1;
  };
#pragma unroll 1
  for (int s = 2; s < NSEG; s += 2) { seg(s, accA, accB, s > 2); seg(s + 1, accB, accA, true); }
  mfma_fence();
#pragma unroll
  for (int i = 0; i < 3; ++i) kst(NSEG - 1, i, accB);
  publish(c, fl, NSEG - 1);
}

__device__ __forceinline__ void role_U(const Params& p, const Ctx& c, int h) {
  u32x4 W[10][6];
  {
    rsrc_t ro = mk_rsrc(p.Wo, DM * NH * DH * 2);
    const unsigned src = stage_src<2>(c, DM), rd = stage_rd(c.lane, c.wave);
    stage_weights<6, 0, 10, 4>(c,
      [&](auto ci, LAS unsigned char* dst) {
        constexpr int id = ci, a = id / 10, pn = id % 10;
        dma16<0>(ro, dst, src, (unsigned)(((h * DH + 32 * a) * DM + pn * 64) * 2));
      },
      [&](auto ci, LAS unsigned char* chunk) {
        constexpr int id = ci;
        W[id % 10][id / 10] = stage_frag(chunk + rd);
      },
      [&](auto ci) { constexpr int id = ci; pin_a(W[id % 10][id / 10]); }, [](auto) {});
  }
  const int g = c.lane >> 4, rp = c.lane & 15;
  const int gs = rp >> 2, i4 = rp & 3;
  const unsigned aoff = (unsigned)((g * 16 + 8 * (i4 >> 1) + 4 * (gs & 1) + (i4 & 1)) * 16 + (gs >> 1) * 8);
  const unsigned uvo = (unsigned)((h >> 1) * 2048 + (g >> 1) * 1024 + ((h & 1) * 2 + (g & 1)) * 256 + rp * 16);
  int vready = -1, issued = -1;
  stamp(c, 1);
  auto dma_v = [&](int s) {
    LAS unsigned char* slot = c.lds + (s % 3) * KX_SZ;
#pragma unroll
    for (int i = 0; i < 3; ++i)
      dma16<SC1>(c.ws, slot + (c.wave * 3 + i) * 1024, (unsigned)c.lane * 16, OFF_VX + (unsigned)(s * NH + h) * KX_SZ + (unsigned)(c.wave * 3 + i) * 1024);
  };
  for (int s = 0; s < NSEG; ++s) {
    if (issued < s) {
      unsigned sp = 0;
      while ((vready = (int)__builtin_amdgcn_readfirstlane(ld4<SC1>(c.ws, FL_V + FS * h, 0))) < s) { __builtin_amdgcn_s_sleep(2); spin_check(c, sp); }
      dma_v(s); issued = s;
      wait_vm<0>();
    } else {
      wait_vm<10>();
    }
    bar();
    if (c.tid == 0 && s >= U_EAGER + 2) st4<SC1>(c.ws, FL_U + FS * h, 0, (unsigned)(s - 2));
    if (s + 1 < NSEG && vready >= s + 1) { dma_v(s + 1); issued = s + 1; }
    const unsigned fv = ld4<SC1>(c.ws, vready < min(s + 2, NSEG - 1) ? FL_V + FS * h : 0x80000000u, 0);
    LAS unsigned char* sa0 = c.lds + (s % 3) * KX_SZ + aoff;
    LAS unsigned char* sa1 = sa0 + 32;
    pin_v(sa0); pin_v(sa1);
    u32x4 a[2][6];
#pragma unroll
    for (int k = 0; k < 6; ++k) {
      a[0][k] = cat(*(LAS u32x2*)(sa0 + (2 * k) * 1024), *(LAS u32x2*)(sa0 + (2 * k + 1) * 1024));
      a[1][k] = cat(*(LAS u32x2*)(sa1 + (2 * k) * 1024), *(LAS u32x2*)(sa1 + (2 * k + 1) * 1024));
    }
    sb();
    f32x4 d[10][2];
    u32x4 o;
    sfor<0, 10>([&](auto pc) {
      constexpr int pn = pc;
      sfor<0, 12>([&](auto ic) {
        constexpr int k = ic / 2, tp = ic % 2;
        mfma_v<true, false, k == 0>(d[pn][tp], W[pn][k], a[tp][k]);
        sb();
        if constexpr (pn > 0 && ic >= 7 && ic < 11) { constexpr int q = ic - 7; o[q] = pk(d[pn - 1][q / 2][2 * (q % 2)], d[pn - 1][q / 2][2 * (q % 2) + 1]); sb(); }
        if constexpr (pn > 0 && ic == 11) {
          st16<U_AUX>(c.ws, uvo, OFF_U + (unsigned)s * U_SZ + (unsigned)(4 * (pn - 1) + c.wave) * 32768, o);
          sb();
        }
      });
    });
    mfma_fence();
    st16<U_AUX>(c.ws, uvo, OFF_U + (unsigned)s * U_SZ + (unsigned)(36 + c.wave) * 32768, pack2(d[9][0], d[9][1]));
    {
      unsigned fvv = fv;
      asm volatile("s_waitcnt vmcnt(10)" : "+v"(fvv)::"memory");
      vready = max(vready, (int)__builtin_amdgcn_readfirstlane(fvv));
    }
    if (s < U_EAGER) {
      if (issued == s && vready >= s + 1) { dma_v(s + 1); issued = s + 1; }
      publish(c, FL_U + FS * h, s);
    }
    stamp(c, 4 + s);
  }
  publish(c, FL_U + FS * h, NSEG - 1);
}

__device__ __forceinline__ void role_Q(const Params& p, const Ctx& c, int h) {
  const int g = c.lane >> 4, n = c.lane & 15;
  const int qt = c.wave * 8 + (c.lane >> 3), tau = qt >> 4, rho = qt & 15, it0 = c.lane & 7, it = min(it0, 4);
  const int T = it >> 1;
  const bool dead = (it0 > 4) || (tau == 0 && it >= 2 && it < 4);
  const unsigned pvo = dead ? 0x80000000u : it < 4 ? (unsigned)(((h >> 1) * 3 + tau + (tau ? T : 0)) * 1024 + (((h & 1) * 2 + (it & 1)) * 16 + rho) * 16)
                                                   : (unsigned)(65536 + ((h >> 2) * 2 + tau) * 1024 + ((h & 3) * 16 + rho) * 16);
  const u32x4 sv = {SENT, SENT, SENT, SENT};
  st16<SC1>(c.ws, pvo, OFF_PX, sv); st16<SC1>(c.ws, pvo, OFF_PX + PX_SZ, sv);
  u32x4 W[3][20];
  load_proj_w(c, p.Wq, h, W);
  if (c.tid == 0) st4<SC1>(c.ws, FL_PX + FS * h, 0, 0u);
  LAS unsigned char* PART = c.lds + 2 * HB_SZ;
  constexpr int QS = 336, WS_ = 16 * QS, BS_ = 4 * WS_;
  const int tile = it == 4 ? (tau ? 4 : 1) : (tau ? 2 + T : 0);
  const int ga = it == 4 ? 0 : (it & 1), gb = it == 4 ? 1 : (it & 1) + 2;
  const int pao = rho * QS + tile * 64 + ga * 16, pbo = rho * QS + tile * 64 + gb * 16;
  LAS unsigned char* PW = PART + c.wave * WS_ + n * QS + g * 16;
  f32x4 ba, bb;
#pragma unroll
  for (int r = 0; r < 4; ++r) {
    ba[r] = (dead || (it < 4 && 16 * T + 4 * ga + r > qt)) ? -__builtin_inff() : 0.f;
    bb[r] = (dead || (it < 4 && 16 * T + 4 * gb + r > qt)) ? -__builtin_inff() : 0.f;
  }
  if (c.wave == 0) wait_flags<8>(c, FL_H, 64, 0);
  if (c.wave == 1) wait_flags<8>(c, FL_KM + FS * h, 1, 0);
  bar();
  dma_hb(c, 0, c.lds); dma_hb(c, 1, c.lds + HB_SZ);
  wait_vm<10>();
  bar();
  u32x4 q[3], kx[3];
  int kready = -1;
  auto load_kx = [&](int s, int i) {
    kx[i] = ld16<SC1>(c.ws, (unsigned)c.lane * 16, OFF_KX + (unsigned)(s * NH + h) * KX_SZ + (unsigned)(4 * i + c.wave) * 1024);
  };
  auto xscores = [&](int s) {
    f32x4 s0 = {}, s2 = {}, s3 = {};
#pragma unroll
    for (int i = 0; i < 3; ++i) {
      const u32x2 q0 = {q[i].x, q[i].y}, q1 = {q[i].z, q[i].w}, k0 = {kx[i].x, kx[i].y}, k1 = {kx[i].z, kx[i].w};
      s0 = mfma16(k0, q0, s0); s2 = mfma16(k0, q1, s2); s3 = mfma16(k1, q1, s3);
    }
    LAS unsigned char* pw = PW + (s & 1) * BS_;
    *(LAS f32x4*)(pw) = s0; *(LAS f32x4*)(pw + 128) = s2; *(LAS f32x4*)(pw + 192) = s3;
  };
  {
    f32x4 acc[3][2];
    gemm_part<3, 0, 20>(W, c.lds + c.lane * 16, acc, [](auto) {});
    mfma_fence();
#pragma unroll
    for (int i = 0; i < 3; ++i) q[i] = pack2(acc[i][0], acc[i][1]);
    kready = wait_flags<8>(c, FL_K + FS * h, 1, 0);
#pragma unroll
    for (int i = 0; i < 3; ++i) load_kx(0, i);
    xscores(0);
  }
  const unsigned kmvo = (unsigned)(g * 64 + (n & 7) * 8 + c.wave * 256);
#pragma unroll 1
  for (int s = 0; s < NSEG; ++s) {
    u32x2 km[3];
    unsigned sp = 0;
    CYCW(u0)
    if (KM_BELL) doorbell<3, KM_DELAY>(c, FL_KM + FS * h, s + 1);
    for (;;) {
#pragma unroll
      for (int i = 0; i < 3; ++i) km[i] = ld8<SC1>(c.ws, kmvo, OFF_KM + (unsigned)(s * NH + h) * 3072 + (unsigned)i * 1024);
      if (!__any(umax3(umax3(km[0].x, km[0].y, km[1].x), umax3(km[1].y, km[2].x, km[2].y), 0u) == SENT)) break;
      __builtin_amdgcn_s_sleep(3);
      spin_check(c, sp);
    }
    wait_vm<0>();
    CYCW(u1)
    LAS unsigned char* PB = PART + (s & 1) * BS_;
    {
      f32x4 s1 = {}, s4 = {};
#pragma unroll
      for (int i = 0; i < 3; ++i) { s1 = mfma16(km[i], u32x2{q[i].x, q[i].y}, s1); s4 = mfma16(km[i], u32x2{q[i].z, q[i].w}, s4); }
      LAS unsigned char* pw = PW + (s & 1) * BS_;
      *(LAS f32x4*)(pw + 64) = s1; *(LAS f32x4*)(pw + 256) = s4;
    }
    if (EARLY_BELL && c.tid == 0) st4<SC1>(c.ws, FL_PX + FS * h, 0, (unsigned)(s + 1));
    bar();
    CYCW(u2)
    {
      LAS unsigned char* pa = PB + pao;
      LAS unsigned char* pb = PB + pbo;
      f32x4 va = ((*(LAS f32x4*)pa + ba) + *(LAS f32x4*)(pa + WS_)) + (*(LAS f32x4*)(pa + 2 * WS_) + *(LAS f32x4*)(pa + 3 * WS_));
      f32x4 vb = ((*(LAS f32x4*)pb + bb) + *(LAS f32x4*)(pb + WS_)) + (*(LAS f32x4*)(pb + 2 * WS_) + *(LAS f32x4*)(pb + 3 * WS_));
      float m = fmaxf(fmaxf(fmaxf(va[0], va[1]), fmaxf(va[2], va[3])), fmaxf(fmaxf(vb[0], vb[1]), fmaxf(vb[2], vb[3])));
      asm volatile("s_nop 1\n\tv_max_f32_dpp %0, %0, %0 quad_perm:[1,0,3,2] row_mask:0xf bank_mask:0xf\n\t"
                   "s_nop 1\n\tv_max_f32_dpp %0, %0, %0 quad_perm:[2,3,0,1] row_mask:0xf bank_mask:0xf\n\t"
                   "s_nop 1\n\tv_max_f32_dpp %0, %0, %0 row_half_mirror row_mask:0xf bank_mask:0xf" : "+v"(m));
      const float cs = 0.07216878364870322f * 1.4426950408889634f;
      const float mc = -m * cs;
      float l = 0.f;
#pragma unroll
      for (int r = 0; r < 4; ++r) {
        va[r] = __builtin_amdgcn_exp2f(__builtin_fmaf(va[r], cs, mc));
        vb[r] = __builtin_amdgcn_exp2f(__builtin_fmaf(vb[r], cs, mc));
        l += va[r] + vb[r];
      }
      l += dpp<0xB1>(l); l += dpp<0x4E>(l); l += dpp<0x141>(l);
      const float inv = __builtin_amdgcn_rcpf(l);
      st16<SC1>(c.ws, pvo, OFF_PX + (unsigned)s * PX_SZ,
                u32x4{pks(va[0] * inv, va[1] * inv), pks(vb[0] * inv, vb[1] * inv), pks(va[2] * inv, va[3] * inv), pks(vb[2] * inv, vb[3] * inv)});
    }
    if (!EARLY_BELL && c.tid == 0) st4<SC1>(c.ws, FL_PX + FS * h, 0, (unsigned)(s + 1));
    CYCW(u3)
    stamp(c, 4 + s);
    if (s + 1 < NSEG) {
      if (s + 2 < NSEG) st16<SC1>(c.ws, pvo, OFF_PX + (unsigned)(s + 2) * PX_SZ, sv);
      if (kready < s + 1) kready = wait_flags<8>(c, FL_K + FS * h, 1, s + 1);
      const int s2 = min(s + 2, NSEG - 1);
      f32x4 acc[3][2];
      gemm_part<3, 0, 20>(W, c.lds + ((s + 1) & 1) * HB_SZ + c.lane * 16, acc, [&](auto k) {
        if constexpr (k < 3) load_kx(s + 1, k);
        if constexpr (k >= 3 && k < 13) dma_hb1(c, s2, c.lds + (s & 1) * HB_SZ, k - 3);
      });
      mfma_fence();
      CYCW(u4)
#pragma unroll
      for (int i = 0; i < 3; ++i) q[i] = pack2(acc[i][0], acc[i][1]);
      xscores(s + 1);
      CYCW(u5)
      CYCOUT(s, u0, u1, u2, u3, u4, u5, u5)
    }
  }
}

constexpr int X1_ARM = 6;
__device__ __forceinline__ void role_PX(const Params& p, const Ctx& c, int jx) {
  LAS float* ga = (LAS float*)c.lds;
  LAS unsigned char* R = c.lds + 4096;
  const int g = c.lane >> 4, n = c.lane & 15;
  const unsigned l16 = (unsigned)c.lane * 16;
  const int nt = c.wave & 1, tau = c.wave >> 1;
  const int col = 32 * jx + 16 * nt + 4 * g;
  const unsigned xo = (unsigned)(((tau * 16 + n) * DM + col) * 2);
  const unsigned x1o = (unsigned)((jx * 2 + tau) * 1024 + ((2 * nt + (g >> 1)) * 16 + n) * 16 + (g & 1) * 8);
#pragma unroll
  for (int i = 0; i < X1_ARM; ++i) st8<SC1>(c.ws, x1o, OFF_X1 + (unsigned)i * HB_SZ, u32x2{SENT, SENT});
  publish(c, FL_X1 + FS * jx, 0);
  Sides sd;
  get_sides(c, sd);
  load_mod(c, 2 * DM, DM, ga, false);
  wait_flags<8>(c, FL_PX + 8 * FS * c.wave, 8, 0);
  bar();
  const f32x4 gav = *(LAS f32x4*)(ga + col);
  rsrc_t rx = mk_rsrc(p.x, 2048 * DM * 2);
  const unsigned ms0 = pz_side(sd, 2 * jx), ms1 = pz_side(sd, 2 * jx + 1);
  int uready = -1;
  auto ld_u = [&](int s, int i) -> u32x4 {
    return ld16<SC1>(c.ws, l16, OFF_U + (unsigned)s * U_SZ + (unsigned)((2 * jx + (i & 1)) * 32 + 8 * c.wave + (i >> 1)) * 1024);
  };
  auto step = [&](int s, const u32x4 (&z)[16], u32x4 (&zn)[16]) {
    const int sn = min(s + 1, NSEG - 1);
    if (uready < sn) uready = wait_flags<8>(c, FL_U + 8 * FS * c.wave, 8, sn);
    const unsigned spx = OFF_PX + (unsigned)s * PX_SZ;
    CYCW(u0)
    doorbell<PX_SLEEP, PX_DELAY>(c, FL_PX, s + 1);
    CYCW(u1)
    const unsigned m0 = m_phys((unsigned)s, (unsigned)(2 * jx), ms0) + (unsigned)c.wave * 2048, m1 = m_phys((unsigned)s, (unsigned)(2 * jx + 1), ms1) + (unsigned)c.wave * 2048;
    u32x4 pa[12], pm[2][2], zm[2][2];
    f32x4 acc[2][2];
    const u32x2 xv = ld8<0>(rx, xo, (unsigned)s * HB_SZ);
    unsigned sp = 0;
    for (;;) {
#pragma unroll
      for (int i = 0; i < 2; ++i) {
        zm[i][0] = ld16<SC1>(c.ws, l16 + i * 1024, m0); zm[i][1] = ld16<SC1>(c.ws, l16 + i * 1024, m1);
#pragma unroll
        for (int t = 0; t < 2; ++t) pm[i][t] = ld16<SC1>(c.ws, l16, spx + 65536 + (unsigned)((2 * c.wave + i) * 2 + t) * 1024);
      }
#pragma unroll
      for (int i = 0; i < 12; ++i) pa[i] = ld16<SC1>(c.ws, l16, spx + (unsigned)(12 * c.wave + i) * 1024);
      sb();
      unsigned m = 0;
      sfor<0, 8>([&](auto ic) {
        constexpr int k = ic >> 2, t = (ic >> 1) & 1, u = ic & 1;
        mfma_v<false, true, k == 0>(acc[t][u], zm[k][t], pm[k][u]);
        sb();
        if constexpr (u == 0) m = umax4(m, zm[k][t]);
        if constexpr (t == 1) m = umax4(m, pm[k][u]);
        sb();
      });
      sfor<0, 24>([&](auto ic) {
        constexpr int f = ic >> 1, t = ic & 1, pr = f / 3, w = f % 3;
        mfma_v<false, true, false>(acc[t][w ? 1 : 0], z[2 * (2 * pr + (w == 2 ? 1 : 0)) + t], pa[f]);
        sb();
        if constexpr (t == 0) m = umax4(m, pa[f]);
        if constexpr (ic >= 8) { if (s + 1 < NSEG) zn[ic - 8] = ld_u(sn, ic - 8); }
        sb();
      });
      if (!__any(m == SENT)) break;
      wait_flags<4>(c, FL_PX + 8 * FS * c.wave, 8, s + 1);
      __builtin_amdgcn_s_sleep(2);
      spin_check(c, sp);
    }
    if (EARLY_BELL && c.tid == 0) st4<SC1>(c.ws, FL_X1 + FS * jx, 0, (unsigned)(s + 1));
    mfma_fence();
    CYCW(u2)
    LAS unsigned char* Rb = R + (s & 1) * 16384;
#pragma unroll
    for (int t = 0; t < 2; ++t)
#pragma unroll
      for (int u = 0; u < 2; ++u) *(LAS f32x4*)(Rb + c.wave * 4096 + (u * 2 + t) * 1024 + l16) = acc[t][u];
    bar();
    CYCW(u3)
    LAS unsigned char* rp = Rb + c.wave * 1024 + l16;
    const f32x4 v = (*(LAS f32x4*)rp + *(LAS f32x4*)(rp + 4096)) + (*(LAS f32x4*)(rp + 8192) + *(LAS f32x4*)(rp + 12288));
    const float o0 = bf_lo(xv.x) + rbf(gav[0] * rbf(v[0])), o1 = bf_hi(xv.x) + rbf(gav[1] * rbf(v[1]));
    const float o2 = bf_lo(xv.y) + rbf(gav[2] * rbf(v[2])), o3 = bf_hi(xv.y) + rbf(gav[3] * rbf(v[3]));
    st8<SC1>(c.ws, x1o, OFF_X1 + (unsigned)s * HB_SZ, u32x2{pks(o0, o1), pks(o2, o3)});
    if (!EARLY_BELL && c.tid == 0) st4<SC1>(c.ws, FL_X1 + FS * jx, 0, (unsigned)(s + 1));
    CYCW(u4)
    CYCOUT(s, u0, u1, u2, u3, u4, u4, u4)
    stamp(c, 4 + s);
    if (s + X1_ARM < NSEG) st8<SC1>(c.ws, x1o, OFF_X1 + (unsigned)(s + X1_ARM) * HB_SZ, u32x2{SENT, SENT});
  };
  u32x4 za[16], zb[16];
  uready = wait_flags<8>(c, FL_U + 8 * FS * c.wave, 8, 0);
#pragma unroll
  for (int i = 0; i < 16; ++i) za[i] = ld_u(0, i);
#pragma unroll 1
  for (int s = 0; s < NSEG; s += 2) { step(s, za, zb); step(s + 1, zb, za); }
}

__device__ __forceinline__ int ffn_seg(int rep, int i) { return (i >> 1) * 4 + (rep & 2) + (i & 1); }

__device__ __forceinline__ void role_F1(const Params& p, const Ctx& c, int rep, int f1) {
  const u32x4 sv = {SENT, SENT, SENT, SENT};
  const int tau = rep & 1, g = c.lane >> 4, rho = c.lane & 15;
  const unsigned avo = (unsigned)(c.lane * 16 + (f1 * 4 + c.wave) * 2048 + tau * 1024);
  st16<SC1>(c.ws, avo, OFF_ACT + (unsigned)ffn_seg(rep, 0) * ACT_SZ, sv);
  st16<SC1>(c.ws, avo, OFF_ACT + (unsigned)ffn_seg(rep, 1) * ACT_SZ, sv);
  LAS float* shiftv = (LAS float*)(c.lds + 131072);
  LAS float* scalev = (LAS float*)(c.lds + 131072 + 4096);
  bar();
  load_mod(c, 3 * DM, DM, shiftv, false);
  load_mod(c, 4 * DM, DM, scalev, true);
  u32x4 W[4][20];
  f32x4 bias[4] = {}, csum[4] = {};
  {
    rsrc_t rg = mk_rsrc(p.wg, DM * DFF * 2), ru = mk_rsrc(p.wu, DM * DFF * 2);
    const unsigned src = stage_src<0>(c, DFF), rd = stage_rd(c.lane, c.wave);
    const u32x4 ones = {0x3F803F80u, 0x3F803F80u, 0x3F803F80u, 0x3F803F80u};
    stage_weights<10, 0, 8, 4>(c,
      [&](auto ci, LAS unsigned char* dst) {
        constexpr int id = ci, kap = id / 4, pn = id % 4;
        dma16<0>(pn < 2 ? rg : ru, dst, src, (unsigned)(((32 * kap) * DFF + f1 * 128 + (pn & 1) * 64) * 2));
      },
      [&](auto ci, LAS unsigned char* chunk) {
        constexpr int id = ci;
        W[id % 4][id / 4] = stage_frag(chunk + rd);
      },
      [&](auto) {},
      [&](auto ci) {
        constexpr int id = ci, kap = id / 4, i = id % 4;
        const f32x4 s0 = *(LAS f32x4*)(scalev + 32 * kap + 8 * g), s1 = *(LAS f32x4*)(scalev + 32 * kap + 8 * g + 4);
        const f32x4 h0 = *(LAS f32x4*)(shiftv + 32 * kap + 8 * g), h1 = *(LAS f32x4*)(shiftv + 32 * kap + 8 * g + 4);
        const u32x4 w = W[i][kap];
        mfma_vn(bias[i], w, pack2(h0, h1));
        W[i][kap] = u32x4{pk(bf_lo(w.x) * s0[0], bf_hi(w.x) * s0[1]), pk(bf_lo(w.y) * s0[2], bf_hi(w.y) * s0[3]),
                          pk(bf_lo(w.z) * s1[0], bf_hi(w.z) * s1[1]), pk(bf_lo(w.w) * s1[2], bf_hi(w.w) * s1[3])};
        mfma_vn(csum[i], W[i][kap], ones);
        if constexpr (id < 64) pin_a(W[i][kap]); else pin_v(W[i][kap]);
      });
  }
  if (c.tid == 0) st4<SC1>(c.ws, FL_ACT + FS * (16 * rep + f1), 0, 0u);
  LAS unsigned char* HB = c.lds;
  LAS float* red = (LAS float*)(c.lds + 40960);
  LAS float* rw = red + rho * 20 + c.wave * 4 + g;
  LAS float* rr = red + rho * 20;
  wait_flags<8>(c, FL_X1, 20, 0);
  stamp(c, 100);
  const unsigned vo = (unsigned)((10 * c.wave + tau) * 1024 + c.lane * 16);
  unsigned ones_u = 0x3F803F80u;
  pin_v(ones_u);
  auto sum16 = [&](LAS float* q) {
    const f32x4 t = (*(LAS f32x4*)q + *(LAS f32x4*)(q + 4)) + (*(LAS f32x4*)(q + 8) + *(LAS f32x4*)(q + 12));
    return (t[0] + t[1]) + (t[2] + t[3]);
  };
#pragma unroll 1
  for (int it = 0; it < 32; ++it) {
    const int s = ffn_seg(rep, it);
    const unsigned sx = OFF_X1 + (unsigned)s * HB_SZ;
    LAS unsigned char* hb = HB + (it & 1) * 20480 + c.lane * 16;
    CYCW(u0)
    doorbell<4, F1_DELAY>(c, FL_X1, s + 1);
    CYCW(u1)
    u32x4 raw[5];
    unsigned sp = 0;
    for (;;) {
      unsigned m = 0;
      float s0 = 0.f, s1 = 0.f;
#pragma unroll
      for (int i = 0; i < 5; ++i) raw[i] = ld16<SC1>(c.ws, vo, sx + i * 2048);
      sb();
#pragma unroll
      for (int i = 0; i < 5; ++i) {
        *(LAS u32x4*)(hb + (5 * c.wave + i) * 1024) = raw[i];
        m = umax4(m, raw[i]);
        s0 = dot2(raw[i].x, ones_u, s0); s1 = dot2(raw[i].y, ones_u, s1);
        s0 = dot2(raw[i].z, ones_u, s0); s1 = dot2(raw[i].w, ones_u, s1);
      }
      rw[0] = s0 + s1;
      if (!__any(m == SENT)) break;
      wait_flags<4>(c, FL_X1 + 5 * FS * c.wave, 5, s + 1);
      __builtin_amdgcn_s_sleep(2);
      spin_check(c, sp);
    }
    bar();
    CYCW(u2)
    const float mean = sum16(rr) * (1.f / DM);
    f32x4 acc[4];
    float v0 = 0.f, v1 = 0.f;
    {
      u32x4 b[20];
#pragma unroll
      for (int k = 0; k < 3; ++k) b[k] = lds16(hb + k * 1024);
      sb();
      sfor<0, 20>([&](auto kc) {
        constexpr int k = kc;
        const unsigned d = raw[k / 4][k % 4];
        float a0, a1;
        mfma_v<(k * 4 + 0 < 64), true, k == 0>(acc[0], W[0][k], b[k]); sb();
        if constexpr (k + 3 < 20) b[k + 3] = lds16(hb + (k + 3) * 1024);
        a0 = bf_lo(d); a1 = bf_hi(d); sb();
        mfma_v<(k * 4 + 1 < 64), true, k == 0>(acc[1], W[1][k], b[k]); sb();
        a0 = add1(a0, -mean); a1 = add1(a1, -mean); sb();
        mfma_v<(k * 4 + 2 < 64), true, k == 0>(acc[2], W[2][k], b[k]); sb();
        asm("v_fmac_f32 %0, %1, %1" : "+v"(v0) : "v"(a0)); asm("v_fmac_f32 %0, %1, %1" : "+v"(v1) : "v"(a1)); sb();
        mfma_v<(k * 4 + 3 < 64), true, k == 0>(acc[3], W[3][k], b[k]); sb();
      });
    }
    rw[320] = v0 + v1;
    if (EARLY_BELL && c.tid == 0) st4<SC1>(c.ws, FL_ACT + FS * (16 * rep + f1), 0, (unsigned)(it + 1));
    mfma_fence();
    bar();
    sb();
    CYCW(u3)
    const float rstd = __builtin_amdgcn_rsqf(sum16(rr + 320) * (1.f / DM) + 1e-5f);
    float a[8];
#pragma unroll
    for (int i = 0; i < 2; ++i)
#pragma unroll
      for (int r = 0; r < 4; ++r) {
        const float gv = __builtin_fmaf(__builtin_fmaf(-mean, csum[i][r], acc[i][r]), rstd, bias[i][r]);
        const float uv = __builtin_fmaf(__builtin_fmaf(-mean, csum[2 + i][r], acc[2 + i][r]), rstd, bias[2 + i][r]);
        const float z2 = gv * __builtin_fmaf(gv * gv, -2.f * 1.4426950408889634f * 0.7978845608028654f * 0.044715f, -2.f * 1.4426950408889634f * 0.7978845608028654f);
        a[4 * i + r] = gv * uv * __builtin_amdgcn_rcpf(1.f + __builtin_amdgcn_exp2f(z2));
      }
    st16<SC1>(c.ws, avo, OFF_ACT + (unsigned)s * ACT_SZ, u32x4{pks(a[0], a[1]), pks(a[2], a[3]), pks(a[4], a[5]), pks(a[6], a[7])});
    if (!EARLY_BELL && c.tid == 0) st4<SC1>(c.ws, FL_ACT + FS * (16 * rep + f1), 0, (unsigned)(it + 1));
    CYCW(u4)
    CYCOUT(s, u0, u1, u2, u3, u4, u4, u4)
    stamp(c, 104 + s);
    if (s + 4 < NSEG) st16<SC1>(c.ws, avo, OFF_ACT + (unsigned)(s + 4) * ACT_SZ, sv);
  }
}

__device__ __forceinline__ void role_F2(const Params& p, const Ctx& c, int rep, int f2) {
  u32x4 W[12][4];
  {
    rsrc_t rd_ = mk_rsrc(p.wd, DFF * DM * 2);
    const unsigned src = stage_src<3>(c, DM);
    unsigned rd[4];
#pragma unroll
    for (int u = 0; u < 4; ++u) rd[u] = stage_rd(c.lane, u) + (unsigned)c.wave * 4096;
    stage_weights<4, 0, 12, 3>(c,
      [&](auto ci, LAS unsigned char* dst) {
        constexpr int id = ci;
        dma16<0>(rd_, dst, src, (unsigned)(((128 * (id / 4) + 16 * (id % 4)) * DM + f2 * 64) * 2));
      },
      [&](auto ci, LAS unsigned char* chunk) {
        constexpr int id = ci;
        if constexpr (id % 4 == 0) {
#pragma unroll
          for (int u = 0; u < 4; ++u) W[id / 4][u] = stage_frag(chunk + rd[u]);
        }
      },
      [&](auto ci) { constexpr int id = ci; pin_a(W[id / 4][id % 4]); }, [](auto) {});
  }
  LAS float* gb = (LAS float*)c.lds;
  LAS unsigned char* R = c.lds + 4096;
  load_mod(c, 5 * DM, DM, gb, false);
  wait_flags<8>(c, FL_ACT + FS * 16 * rep, 12, 0);
  bar();
  const int tau = rep & 1, g = c.lane >> 4, n = c.lane & 15;
  const unsigned l16 = (unsigned)c.lane * 16;
  const int col = 64 * f2 + 16 * c.wave + 4 * g;
  const f32x4 gbv = *(LAS f32x4*)(gb + col);
  rsrc_t ro = mk_rsrc(p.out, 2048 * DM * 2);
  const unsigned xo = (unsigned)(((tau * 16 + n) * DM + col) * 2);
  const unsigned x1o = (unsigned)((col >> 5) * 2048 + tau * 1024 + (((col >> 3) & 3) * 16 + n) * 16 + (g & 1) * 8);
  stamp(c, 100);
#pragma unroll 1
  for (int it = 0; it < 32; ++it) {
    const int s = ffn_seg(rep, it);
    const unsigned sa = OFF_ACT + (unsigned)s * ACT_SZ;
    doorbell<4, F2_DELAY>(c, FL_ACT + FS * 16 * rep, it + 1);
    u32x4 b[12];
    u32x2 xv;
    f32x4 acc[4];
    unsigned sp = 0;
    for (;;) {
#pragma unroll
      for (int i = 0; i < 12; ++i) b[i] = ld16<SC1>(c.ws, l16, sa + (unsigned)((4 * i + c.wave) * 2 + tau) * 1024);
      xv = ld8<SC1>(c.ws, x1o, OFF_X1 + (unsigned)s * HB_SZ);
      sb();
      unsigned m = 0;
      sfor<0, 48>([&](auto ic) {
        constexpr int i = ic >> 2, u = ic & 3;
        mfma_v<true, true, i == 0>(acc[u], W[i][u], b[i]);
        sb();
        if constexpr (u == 0) { m = umax4(m, b[i]); sb(); }
      });
      if (!__any(m == SENT)) break;
      wait_flags<4>(c, FL_ACT + FS * 16 * rep, 12, it + 1);
      __builtin_amdgcn_s_sleep(2);
      spin_check(c, sp);
    }
    mfma_fence();
    LAS unsigned char* Rb = R + (it & 1) * 16384;
#pragma unroll
    for (int u = 0; u < 4; ++u) *(LAS f32x4*)(Rb + c.wave * 4096 + u * 1024 + l16) = acc[u];
    bar();
    LAS unsigned char* rp = Rb + c.wave * 1024 + l16;
    const f32x4 v = (*(LAS f32x4*)rp + *(LAS f32x4*)(rp + 4096)) + (*(LAS f32x4*)(rp + 8192) + *(LAS f32x4*)(rp + 12288));
    const float o0 = bf_lo(xv.x) + rbf(gbv[0] * rbf(v[0])), o1 = bf_hi(xv.x) + rbf(gbv[1] * rbf(v[1]));
    const float o2 = bf_lo(xv.y) + rbf(gbv[2] * rbf(v[2])), o3 = bf_hi(xv.y) + rbf(gbv[3] * rbf(v[3]));
    st8<0>(ro, xo, (unsigned)s * HB_SZ, u32x2{pk(o0, o1), pk(o2, o3)});
    stamp(c, 104 + s);
  }
}

enum { M_PRE = 1, M_KV = 2, M_U = 4, M_REC = 8, M_Q = 16, M_PX = 32, M_F1 = 64, M_F2 = 128, M_ALL = 255 };

__global__ void __launch_bounds__(256, 1) mega_kernel(Params p) {
  extern __shared__ __attribute__((aligned(16))) unsigned char smem[];
  Ctx c;
  c.ws = mk_rsrc(p.ws, WS_BYTES);
  c.srd = u32x4{(unsigned)(uintptr_t)p.ws, (unsigned)((uintptr_t)p.ws >> 32) & 0xFFFFu, WS_BYTES, 0x00020000u};
  c.lds = (LAS unsigned char*)smem;
  c.tid = threadIdx.x; c.lane = c.tid & 63; c.wave = __builtin_amdgcn_readfirstlane(c.tid >> 6);
  const int grp = blockIdx.x & 7, ix = blockIdx.x >> 3;
  stamp(c, 0);
  int role, h, rep, r;
  rep = grp - 4; r = ix;
  if (grp < 4) {
    if (ix < 16) { role = ix >> 3; h = grp * 8 + (ix & 7); }
    else if (ix < 26) { role = 6; h = grp * 10 + ix - 16; }
    else if (ix < 31) { role = 6; h = 40 + grp * 5 + ix - 26; }
    else { role = 2; h = 28 + grp; }
  } else {
    if (ix < 24) { role = 3 + (ix >> 3); h = rep * 8 + (ix & 7); }
    else if (ix < 31) { role = 2; h = rep * 7 + ix - 24; }
    else { role = 6; h = 60 + rep; }
  }
  if (role < 2) { for (int i = 0; i < HV_DELAY; ++i) __builtin_amdgcn_s_sleep(32); }
  if (role == 3) { for (int i = 0; i < KW_DELAY; ++i) __builtin_amdgcn_s_sleep(32); }
  if (role == 5) { for (int i = 0; i < UW_DELAY; ++i) __builtin_amdgcn_s_sleep(32); }
  if (role == 2) {
    for (int i = 0; i < Q_DELAY; ++i) __builtin_amdgcn_s_sleep(32);
  }
  if (role == 0) { if (p.mask & M_REC) role_H(p, c, h); }
  else if (role == 1) { if (p.mask & M_REC) role_V(p, c, h); }
  else if (role == 2) { if (p.mask & M_Q) role_Q(p, c, h); }
  else if (role < 6) {
    if (role == 5) { if (p.mask & M_U) role_U(p, c, h); }
    else { if (p.mask & M_KV) role_KV(p, c, h, role == 4); }
    if (r < 12) { if (p.mask & M_F1) role_F1(p, c, rep, r); }
    else if (r < 22) { if (p.mask & M_F2) role_F2(p, c, rep, r - 12); }
  } else {
    if (p.mask & M_PRE) role_pre(p, c, h, h < 40 ? h : -1);
    if (h < 40) { if (p.mask & M_REC) role_PZ(p, c, h); }
    else if (h < 60) { if (p.mask & M_PX) role_PX(p, c, h - 40); }
    else if (FFN_PREFETCH) {
      for (int i = 0; i < 24; ++i) __builtin_amdgcn_s_sleep(32);
#pragma unroll 1
      for (int m = 0; m < 3; ++m) {
        rsrc_t r = mk_rsrc(m == 0 ? p.wg : m == 1 ? p.wu : p.wd, DM * DFF * 2);
#pragma unroll 1
        for (unsigned o = (unsigned)(h - 60) * 49152u; o < DM * DFF * 2; o += 4 * 49152) {
          u32x4 t[12];
#pragma unroll
          for (int i = 0; i < 12; ++i) t[i] = ld16<0>(r, (unsigned)c.tid * 16u + i * 4096u, o);
#pragma unroll
          for (int i = 0; i < 12; ++i) asm volatile("" :: "v"(t[i]));
        }
      }
    }
  }
}

}

#define SPLIT_LAUNCH 0

extern "C" void kernel_launch(void* const* d_in, const int* in_sizes, int n_in, void* d_out, int out_size, void* d_ws,
                              size_t ws_size, hipStream_t stream) {
  Params p;
  p.x = d_in[0]; p.mem0 = d_in[1]; p.Wq = d_in[2]; p.Wk = d_in[3]; p.Wv = d_in[4]; p.Wo = d_in[5];
  p.wg = d_in[8]; p.wu = d_in[9]; p.wd = d_in[10]; p.c = d_in[11]; p.wmod = d_in[12];
  void* wsa = (void*)(((uintptr_t)d_ws + ((2u << 20) - 1)) & ~(uintptr_t)((2u << 20) - 1));
  p.out = d_out; p.ws = wsa; p.mask = M_ALL; p.pad = 0;
  (void)hipFuncSetAttribute((const void*)mega_kernel, hipFuncAttributeMaxDynamicSharedMemorySize, LDS_BYTES);
  (void)hipMemsetAsync(wsa, 0xFF, INIT_BYTES, stream);
  hipLaunchKernelGGL(mega_kernel, dim3(256), dim3(256), LDS_BYTES, stream, p);
}
```

```cpp
#include <hip/hip_runtime.h>
#include <hip/hip_bf16.h>
#include <type_traits>
#include <cstdint>

#define LAS __attribute__((address_space(3)))
typedef unsigned u32x2 __attribute__((ext_vector_type(2)));
typedef unsigned u32x4 __attribute__((ext_vector_type(4)));
typedef unsigned u32x8 __attribute__((ext_vector_type(8)));
typedef float f32x2 __attribute__((ext_vector_type(2)));
typedef float f32x4 __attribute__((ext_vector_type(4)));
typedef short s16x4 __attribute__((ext_vector_type(4)));
typedef __bf16 bf16x2 __attribute__((ext_vector_type(2)));
typedef __bf16 bf16x8 __attribute__((ext_vector_type(8)));
typedef __bf16 bf16x16 __attribute__((ext_vector_type(16)));
typedef __amdgpu_buffer_rsrc_t rsrc_t;

namespace {

constexpr int DM = 640, NH = 32, DH = 192, NSEG = 64, DFF = 1536;
constexpr unsigned SENT = 0xFFFFFFFFu;

constexpr unsigned FS = 128;
constexpr unsigned FL_ABORT = 0;
constexpr unsigned FL_EPOCH = 128;
constexpr unsigned OFF_MOD = 1024;
constexpr unsigned OFF_MODP = 57344;
constexpr unsigned FL_H = 16384;
constexpr unsigned FL_KM = 24576, FL_K = 28672, FL_V = 32768, FL_U = 36864, FL_PX = 40960;
constexpr unsigned FL_X1 = 45056;
constexpr unsigned FL_ACT = 49152;
constexpr unsigned FL_MP = OFF_MODP + 15360 + 1024;
constexpr unsigned OFF_DB = 81920;
constexpr unsigned FL_MD = OFF_DB + 4096;
constexpr unsigned FL_MD2 = OFF_DB + 8192 + 4096;
constexpr unsigned OFF_XR = 98304;
constexpr unsigned MEM_SZ = 10240, P_SZ = 20480;
constexpr unsigned L_MEM = 0, L_P = 64 * MEM_SZ, L_END = L_P + 64 * P_SZ;
constexpr unsigned OFF_CAL = OFF_XR + 2 * L_END;
constexpr unsigned OFF_M = OFF_CAL + 65536;
constexpr unsigned M_SZ = 40 * 16384;
constexpr unsigned OFF_HB = OFF_M + 64 * M_SZ;
constexpr unsigned HB_SZ = 40960;
constexpr unsigned OFF_KX = OFF_HB + 64 * HB_SZ;
constexpr unsigned KX_SZ = 12288;
constexpr unsigned OFF_VX = OFF_KX + 64 * 32 * KX_SZ;
constexpr unsigned OFF_U = OFF_VX + 64 * 32 * KX_SZ;
constexpr unsigned U_SZ = 40 * 32 * 1024;
constexpr unsigned OFF_KM = OFF_U + 64 * U_SZ;
constexpr unsigned OFF_PX = OFF_KM + 64 * 32 * 3072;
constexpr unsigned PX_SZ = 81920;
constexpr unsigned OFF_X1 = OFF_PX + 64 * PX_SZ;
constexpr unsigned OFF_ACT = OFF_X1 + 2048 * 640 * 2;
constexpr unsigned ACT_SZ = 48 * 2 * 1024;
constexpr unsigned OFF_DBG = OFF_ACT + 64 * ACT_SZ;
constexpr unsigned WS_BYTES = OFF_DBG + (1u << 20);
static_assert(WS_BYTES + (2u << 20) <= (256u << 20), "workspace");
static_assert(OFF_XR % 16384 == 0 && OFF_CAL % 16384 == 0 && OFF_M % 16384 == 0, "alignment");

constexpr int LDS_BYTES = 160 * 1024;
constexpr unsigned SPIN_CAP = 1u << 21;
#define PZ_SLEEP 14
#define PZ_GAPD 2
#define PZ_GAP 0
#define UST_AUX SC1
#define UST_VO ((unsigned)c.lane * 16)
#define H_SLEEP 12
#define MAP 1
#define REV 0
#define V_NW 4
#define V_NA 3
#define HV_DELAY 0
#define Q_DELAY 0
#define PZ_MODE 0
#define PZ_THR 16
#define PXT 0
#define PX_SLEEP 4
#define PX_NF 1
#define PX_DELAY 12
#define USE_BELL_P 1
#define USE_BELL_M 1
#define BELL_P_POS 5
#define BELL_P_GAP 3
#define BELL_M_POS 2
#define BELL_M_GAP 0
#define BELL_P_GAP0 0
#define H_D0 0
#define H_D1 8
#define H_D2 0
#define F1_DELAY 4
#define F2_DELAY 2
#define KM_BELL 0
#define KM_DELAY 0
#define TLBWARM 0
#define FFN_PREFETCH 1
#define Q_PREFETCH 0
#define U_EAGER 6
#define BELL_MODE 1
#define BELL_SLEEP 0
#define NBELL 4
#define EARLY_BELL 0
#define KW_DELAY 0
#define UW_DELAY 0
#define KV_AUX 16
#define U_AUX 16
#define WS_WARM 0
#define SPLIT_LAUNCH 0
#define BUMP (!SPLIT_LAUNCH)
#define V_SLEEP 16

struct Params {
  const void* x; const void* mem0; const void* Wq; const void* Wk; const void* Wv; const void* Wo;
  const void* wg; const void* wu; const void* wd; const void* c; const void* wmod;
  void* out; void* ws;
  unsigned mask; unsigned pad;
};

template <int I, int N, class F>
__device__ __forceinline__ void sfor(F&& f) {
  if constexpr (I < N) { f(std::integral_constant<int, I>{}); sfor<I + 1, N>(f); }
}
__device__ __forceinline__ rsrc_t mk_rsrc(const void* p, unsigned bytes) {
  return __builtin_amdgcn_make_buffer_rsrc(const_cast<void*>(p), 0, bytes, 0x00020000);
}
template <int AUX> __device__ __forceinline__ u32x4 ld16(rsrc_t r, unsigned vo, unsigned so) { return __builtin_amdgcn_raw_buffer_load_b128(r, vo, so, AUX); }
template <int AUX> __device__ __forceinline__ u32x2 ld8(rsrc_t r, unsigned vo, unsigned so) { return __builtin_amdgcn_raw_buffer_load_b64(r, vo, so, AUX); }
template <int AUX> __device__ __forceinline__ unsigned ld4(rsrc_t r, unsigned vo, unsigned so) { return __builtin_amdgcn_raw_buffer_load_b32(r, vo, so, AUX); }
template <int AUX> __device__ __forceinline__ void st16(rsrc_t r, unsigned vo, unsigned so, u32x4 v) { __builtin_amdgcn_raw_buffer_store_b128(v, r, vo, so, AUX); }
template <int AUX> __device__ __forceinline__ void st8(rsrc_t r, unsigned vo, unsigned so, u32x2 v) { __builtin_amdgcn_raw_buffer_store_b64(v, r, vo, so, AUX); }
template <int AUX> __device__ __forceinline__ void st4(rsrc_t r, unsigned vo, unsigned so, unsigned v) { __builtin_amdgcn_raw_buffer_store_b32(v, r, vo, so, AUX); }
constexpr int SC1 = 16;
template <int AUX> __device__ __forceinline__ void dma16(rsrc_t r, LAS unsigned char* dst, unsigned vo, unsigned so) {
  __builtin_amdgcn_raw_ptr_buffer_load_lds(r, (LAS void*)dst, 16, vo, so, 0, AUX);
}
template <int N> __device__ __forceinline__ void wait_vm() { asm volatile("s_waitcnt vmcnt(%0)" ::"n"(N) : "memory"); }
__device__ __forceinline__ void wait_lgkm() { asm volatile("s_waitcnt lgkmcnt(0)" ::: "memory"); }
__device__ __forceinline__ void bar() { asm volatile("s_waitcnt lgkmcnt(0)\n\ts_barrier" ::: "memory"); }

__device__ __forceinline__ unsigned pk(float a, float b) {
  f32x2 v = {a, b};
  return __builtin_bit_cast(unsigned, __builtin_convertvector(v, bf16x2));
}
__device__ __forceinline__ unsigned pks(float a, float b) { return min(pk(a, b), 0xFFFFFFFEu); }
__device__ __forceinline__ float bf_lo(unsigned u) { return __builtin_bit_cast(float, u << 16); }
__device__ __forceinline__ float bf_hi(unsigned u) { return __builtin_bit_cast(float, u & 0xFFFF0000u); }
__device__ __forceinline__ float rbf(float a) { return bf_lo(pk(a, a)); }
__device__ __forceinline__ float f_(unsigned u) { return __builtin_bit_cast(float, u); }
__device__ __forceinline__ unsigned u_(float f) { return __builtin_bit_cast(unsigned, f); }
__device__ __forceinline__ unsigned umax3(unsigned a, unsigned b, unsigned c) { return max(max(a, b), c); }
__device__ __forceinline__ unsigned umax4(unsigned m, u32x4 v) { return umax3(umax3(m, v.x, v.y), v.z, v.w); }

__device__ __forceinline__ float dot2(unsigned a, unsigned b, float c) {
  return __builtin_amdgcn_fdot2_f32_bf16(__builtin_bit_cast(bf16x2, a), __builtin_bit_cast(bf16x2, b), c, false);
}
__device__ __forceinline__ f32x4 mfma32(u32x4 a, u32x4 b, f32x4 c) {
  return __builtin_amdgcn_mfma_f32_16x16x32_bf16(__builtin_bit_cast(bf16x8, a), __builtin_bit_cast(bf16x8, b), c, 0, 0, 0);
}
__device__ __forceinline__ f32x4 mfma16(u32x2 a, u32x2 b, f32x4 c) {
  return __builtin_amdgcn_mfma_f32_16x16x16bf16_1k(__builtin_bit_cast(s16x4, a), __builtin_bit_cast(s16x4, b), c, 0, 0, 0);
}
__device__ __forceinline__ f32x4 smfma(u32x4 a, u32x8 b, f32x4 c, int idx) {
  return __builtin_amdgcn_smfmac_f32_16x16x64_bf16(__builtin_bit_cast(bf16x8, a), __builtin_bit_cast(bf16x16, b), c, idx, 0, 0);
}
__device__ __forceinline__ u32x2 tr8(LAS unsigned char* p) {
  return __builtin_bit_cast(u32x2, __builtin_amdgcn_ds_read_tr16_b64_v4i16((LAS s16x4*)p));
}
__device__ __forceinline__ u32x4 lds16(LAS unsigned char* p) { return *(LAS u32x4*)p; }
__device__ __forceinline__ u32x4 cat(u32x2 a, u32x2 b) { return u32x4{a.x, a.y, b.x, b.y}; }
__device__ __forceinline__ u32x8 cat(u32x4 a, u32x4 b) { return u32x8{a.x, a.y, a.z, a.w, b.x, b.y, b.z, b.w}; }
__device__ __forceinline__ float halfsum(float x, float y) {
  auto r = __builtin_amdgcn_permlane32_swap(u_(x), u_(y), false, false);
  return f_(r[0]) + f_(r[1]);
}
__device__ __forceinline__ float halfsum1(float x, float y) {
  auto r = __builtin_amdgcn_permlane32_swap(u_(x), u_(y), false, false);
  float o;
  asm("v_add_f32 %0, %1, %2" : "=v"(o) : "v"(r[0]), "v"(r[1]));
  return o;
}
template <int CTRL> __device__ __forceinline__ float dpp(float x) {
  return f_((unsigned)__builtin_amdgcn_update_dpp(0, (int)u_(x), CTRL, 0xf, 0xf, false));
}

template <class T> __device__ __forceinline__ void pin_a(T& v) { asm volatile("" : "+a"(v)); }
template <class T> __device__ __forceinline__ void pin_v(T& v) { asm volatile("" : "+v"(v)); }

__device__ __forceinline__ void sb() { __builtin_amdgcn_sched_barrier(0); }

struct Ctx {
  rsrc_t ws;
  u32x4 srd;
  unsigned tag;
  const void* wsp;
  LAS unsigned char* lds;
  int tid, lane, wave;
};

__device__ __forceinline__ void stamp(const Ctx&, int, int = 0) {}
__device__ __forceinline__ void stampc(const Ctx&, int, int = 0) {}
__device__ __forceinline__ void stampl(const Ctx&, int) {}
__device__ __forceinline__ void stampf(const Ctx&, int, int = 0) {}
#define CYC(x)
#define CYCW(x)
#define CYCOUT(...)
__device__ __forceinline__ void spin_check(const Ctx& c, unsigned& n) {
  unsigned ts, tv;
  ++n;
  asm volatile(
      "s_and_b32 %0, %2, 0x3ff\n\t"
      "s_cbranch_scc1 1f\n\t"
      "buffer_load_dword %1, off, %3, 0 sc1\n\t"
      "s_waitcnt vmcnt(0)\n\t"
      "v_cmp_eq_u32 vcc, %1, %5\n\t"
      "s_nop 4\n\t"
      "s_cbranch_vccnz 2f\n\t"
      "s_cmp_lt_u32 %2, %4\n\t"
      "s_cbranch_scc1 1f\n\t"
      "buffer_store_dword %5, off, %3, 0 sc1\n\t"
      "s_waitcnt vmcnt(0)\n"
      "2:\n\t"
      "s_endpgm\n"
      "1:"
      : "=&s"(ts), "=&v"(tv)
      : "s"(n), "s"(c.srd), "s"(SPIN_CAP), "v"(c.tag)
      : "memory", "scc", "vcc");
}
template <class T> __device__ __forceinline__ void pin_s(T& v) { asm volatile("" : "+s"(v)); }
__device__ __forceinline__ void load_tag(Ctx& c) {
  unsigned e;
  asm volatile("s_load_dword %0, %1, %2\n\ts_waitcnt lgkmcnt(0)" : "=s"(e) : "s"(c.wsp), "i"(FL_EPOCH) : "memory");
  c.tag = e ^ 0x9E3779B9u;
}
__device__ __forceinline__ u32x2 ld_flag_raw(const Ctx& c, unsigned vo) { return ld8<SC1>(c.ws, vo, 0); }
__device__ __forceinline__ int flag_val(const Ctx& c, u32x2 r) { return r.y == c.tag ? (int)r.x : -1; }
__device__ __forceinline__ int ld_flag(const Ctx& c, unsigned vo) { return flag_val(c, ld_flag_raw(c, vo)); }
__device__ __forceinline__ void st_flag(const Ctx& c, unsigned fl, int v) { st8<SC1>(c.ws, fl, 0, u32x2{(unsigned)v, c.tag}); }
template <int SLEEP>
__device__ __forceinline__ int wait_flags(const Ctx& c, unsigned fl, int n, int need) {
  unsigned sp = 0;
  const unsigned vo = fl + FS * (unsigned)min(c.lane, n - 1);
  for (;;) {
    const int v = ld_flag(c, vo);
    if (!__any(v < need)) {
      int got = need;
      if (!__any(v < need + 2)) got = need + 2;
      if (!__any(v < need + 6)) got = need + 6;
      if (!__any(v < need + 16)) got = need + 16;
      return got;
    }
    __builtin_amdgcn_s_sleep(SLEEP);
    spin_check(c, sp);
  }
}
template <int SLEEP>
__device__ __forceinline__ int wait_flags_blk(const Ctx& c, unsigned fl, int n, int need, LAS int* w) {
  if (c.wave == 0) { const int r = wait_flags<SLEEP>(c, fl, n, need); if (c.lane == 0) *w = r; }
  bar();
  const int r = __builtin_amdgcn_readfirstlane(*w);
  bar();
  return r;
}
__device__ __forceinline__ void publish(const Ctx& c, unsigned fl, int val) {
  wait_vm<0>();
  bar();
  if (c.tid == 0) st_flag(c, fl, val);
}

template <int SLEEP, int DELAY>
__device__ __forceinline__ void doorbell(const Ctx& c, unsigned fl, int need) {
  unsigned sp = 0;
  while (__builtin_amdgcn_readfirstlane(ld_flag(c, fl)) < need) { __builtin_amdgcn_s_sleep(SLEEP); spin_check(c, sp); }
  if (__builtin_amdgcn_readfirstlane(sp)) __builtin_amdgcn_s_sleep(DELAY);
}
__device__ __forceinline__ u32x4 nosent(u32x4 v) { return u32x4{min(v.x, 0xFFFFFFFEu), min(v.y, 0xFFFFFFFEu), min(v.z, 0xFFFFFFFEu), min(v.w, 0xFFFFFFFEu)}; }
__device__ __forceinline__ u32x2 nosent(u32x2 v) { return u32x2{min(v.x, 0xFFFFFFFEu), min(v.y, 0xFFFFFFFEu)}; }

__device__ __forceinline__ unsigned side_bit(unsigned b, unsigned side) { return b | (((side ^ (b >> 21)) & 1u) << 13); }
__device__ __forceinline__ unsigned xr_phys(unsigned o, unsigned side) { return side_bit(OFF_XR + ((o >> 13) << 14) + (o & 8191u), side); }
__device__ __forceinline__ unsigned m_phys(unsigned s, unsigned j, unsigned side) { return side_bit(OFF_M + (s * 40u + j) * 16384u, side); }
__device__ __forceinline__ unsigned my_iod() { return (((unsigned)__builtin_amdgcn_s_getreg(20 | (3 << 11))) >> 2) & 1u; }
struct Sides { unsigned me; unsigned long long light; };
__device__ __forceinline__ u32x2 ld_table(const Ctx& c) { return ld_flag_raw(c, FL_H + FS * (unsigned)c.lane); }
__device__ __forceinline__ void get_sides(const Ctx& c, Sides& o, unsigned v = SENT) {
  unsigned sp = 0;
  if (__any(v == SENT)) {
    for (;;) {
      v = (unsigned)flag_val(c, ld_table(c));
      if (!__any(v == SENT)) break;
      __builtin_amdgcn_s_sleep(8);
      spin_check(c, sp);
    }
  }
  const unsigned cc = __popcll(__ballot(v & 1u) >> 32) > 16 ? 1u : 0u;
  const unsigned long long iod = __ballot(v & 2u);
  o.me = cc ^ my_iod();
  o.light = cc ? ~iod : iod;
}
__device__ __forceinline__ unsigned pz_side(const Sides& sd, int j) { return (unsigned)(sd.light >> j) & 1u; }
__device__ __forceinline__ void ring(const Ctx& c, int bell, int val) {
  if (c.lane < 2) st4<SC1>(c.ws, OFF_DB + (unsigned)bell * FS + (unsigned)c.lane * 8192u, 0, (unsigned)val);
}
__device__ __forceinline__ void wait_bells(const Ctx& c, unsigned vo, int need, u32x2& keep) {
  unsigned sp = 0;
  if (c.wave == 0) {
    while (__any((int)ld4<SC1>(c.ws, vo, 0) < need)) { if (BELL_SLEEP) __builtin_amdgcn_s_sleep(BELL_SLEEP); spin_check(c, sp); }
  } else {
    wait_vm<0>();
  }
  __builtin_amdgcn_s_barrier();
}
__device__ __forceinline__ unsigned bell_vo(const Ctx& c, int bell0, unsigned side) { return side_bit(OFF_DB + (unsigned)(bell0 + (c.lane & (NBELL - 1))) * FS, side); }
__device__ __forceinline__ unsigned calibrate(const Ctx& c, int slot) {
  const unsigned base = OFF_CAL + (unsigned)slot * 256u;
  st16<SC1>(c.ws, base + (unsigned)(c.lane >> 3) * 8192u + (unsigned)(c.lane & 7) * 16u, 0, u32x4{0u, 0u, 0u, 0u});
  wait_vm<0>();
  unsigned best[2] = {~0u, ~0u};
#pragma unroll
  for (int i = 0; i < 8; ++i) {
    const unsigned long long t0 = __builtin_amdgcn_s_memtime();
    wait_lgkm();
    unsigned v = ld4<SC1>(c.ws, base, (unsigned)i * 8192u);
    asm volatile("s_waitcnt vmcnt(0)" : "+v"(v)::"memory");
    const unsigned long long t1 = __builtin_amdgcn_s_memtime();
    wait_lgkm();
    const unsigned par = (((OFF_CAL + i * 8192u) >> 13) ^ ((OFF_CAL + i * 8192u) >> 21)) & 1u;
    best[par] = min(best[par], (unsigned)(t1 - t0));
  }
  return best[1] < best[0] ? 1u : 0u;
}

template <int MODE> __device__ __forceinline__ int rowfn(int g, int j) {
  if (MODE == 0) return 8 * g + j;
  if (MODE == 1) return 16 * (g >> 1) + 8 * ((j >> 1) & 1) + 4 * (g & 1) + 2 * ((j >> 2) & 1) + (j & 1);
  if (MODE == 2) return 16 * (j >> 2) + 4 * g + (j & 3);
  return 64 * (j >> 2) + 4 * g + (j & 3);
}
template <int MODE> __device__ __forceinline__ unsigned stage_src(const Ctx& c, int ldw) {
  const int pos = c.lane >> 3, cp = c.lane & 7;
  const int u = (cp >> 1) ^ ((pos >> 1) & 3);
  const int ch = 2 * u + (cp & 1);
  const int g = 2 * (c.wave >> 1) + (pos >> 2), j = 4 * (c.wave & 1) + (pos & 3);
  return (unsigned)((rowfn<MODE>(g, j) * ldw + ch * 8) * 2);
}
__device__ __forceinline__ unsigned stage_rd(int lane, int u) {
  const int g = lane >> 4, q = (lane & 15) >> 2, p = lane & 3, pos = 4 * (g & 1) + q;
  return (unsigned)((g >> 1) * 2048 + pos * 128 + ((u ^ ((pos >> 1) & 3)) * 32) + p * 8);
}
__device__ __forceinline__ u32x4 stage_frag(LAS unsigned char* chunk_plus_rd) { return cat(tr8(chunk_plus_rd), tr8(chunk_plus_rd + 1024)); }

template <bool FIRST, int NB, int NRES, int BATCH, int NBUF, class Issue, class Consume, class Pin, class Post>
__device__ __forceinline__ void stage_weights(Ctx& c, Issue&& issue, Consume&& consume, Pin&& pin, Post&& post) {
  constexpr int SLOT = BATCH * 4096;
  sfor<0, (NBUF < NB ? NBUF : NB)>([&](auto b) {
    sfor<0, BATCH>([&](auto i) { issue(std::integral_constant<int, b * BATCH + i>{}, c.lds + b * SLOT + i * 4096 + c.wave * 1024); });
  });
  if (FIRST) load_tag(c);
  stampl(c, 300);
  sfor<0, NB - NRES>([&](auto b) {
    constexpr int inflight = ((b + NBUF < NB ? b + NBUF : NB) - (b + 1)) * BATCH;
    wait_vm<inflight>();
    bar();
    stampl(c, 301 + b);
    sfor<0, BATCH>([&](auto i) { consume(std::integral_constant<int, b * BATCH + i>{}, c.lds + (b % NBUF) * SLOT + i * 4096); });
    sb();
    sfor<0, BATCH>([&](auto i) { pin(std::integral_constant<int, b * BATCH + i>{}); });
    if constexpr (b + NBUF < NB) {
      bar();
      sfor<0, BATCH>([&](auto i) {
        issue(std::integral_constant<int, (b + NBUF) * BATCH + i>{}, c.lds + (b % NBUF) * SLOT + i * 4096 + c.wave * 1024);
      });
    }
    sb();
    sfor<0, BATCH>([&](auto i) { post(std::integral_constant<int, b * BATCH + i>{}); });
  });
  wait_vm<0>();
  bar();
}

__device__ __forceinline__ float ln_stats(const Ctx& c, const u32x4 (&raw)[10], f32x2 (&cv)[40], LAS float* red) {
  const int tau = c.wave & 1, kh = c.wave >> 1, g = c.lane >> 4, rho = c.lane & 15, row = tau * 16 + rho;
  float s0 = 0.f, s1 = 0.f;
  unsigned ones_u = 0x3F803F80u;
  pin_v(ones_u);
#pragma unroll
  for (int i = 0; i < 10; ++i) {
    s0 = dot2(raw[i].x, ones_u, s0); s1 = dot2(raw[i].y, ones_u, s1);
    s0 = dot2(raw[i].z, ones_u, s0); s1 = dot2(raw[i].w, ones_u, s1);
  }
  float s = s0 + s1;
  s += __shfl_xor(s, 16); s += __shfl_xor(s, 32);
  if (g == 0) red[kh * 32 + row] = s;
  bar();
  const float mean = (red[row] + red[32 + row]) * (1.f / DM);
  f32x2 v2 = {0.f, 0.f};
#pragma unroll
  for (int i = 0; i < 10; ++i)
#pragma unroll
    for (int d = 0; d < 4; ++d) {
      const f32x2 t = f32x2{bf_lo(raw[i][d]), bf_hi(raw[i][d])} - mean;
      cv[4 * i + d] = t;
      v2 += t * t;
    }
  float v = v2[0] + v2[1];
  v += __shfl_xor(v, 16); v += __shfl_xor(v, 32);
  if (g == 0) red[64 + kh * 32 + row] = v;
  bar();
  return 1.f / sqrtf((red[64 + row] + red[96 + row]) * (1.f / DM) + 1e-5f);
}
template <class Store>
__device__ __forceinline__ void ln_apply(const Ctx& c, const f32x2 (&cv)[40], float rstd, LAS float* shiftv, LAS float* scalev, Store&& store) {
  const int tau = c.wave & 1, kh = c.wave >> 1, g = c.lane >> 4;
#pragma unroll
  for (int i = 0; i < 10; ++i) {
    const int kap = kh * 10 + i, d0 = (4 * kap + g) * 8;
    const f32x4 sc0 = *(LAS f32x4*)(scalev + d0), sc1 = *(LAS f32x4*)(scalev + d0 + 4);
    const f32x4 sh0 = *(LAS f32x4*)(shiftv + d0), sh1 = *(LAS f32x4*)(shiftv + d0 + 4);
    const f32x2 r0 = (cv[4 * i + 0] * rstd) * f32x2{sc0[0], sc0[1]} + f32x2{sh0[0], sh0[1]};
    const f32x2 r1 = (cv[4 * i + 1] * rstd) * f32x2{sc0[2], sc0[3]} + f32x2{sh0[2], sh0[3]};
    const f32x2 r2 = (cv[4 * i + 2] * rstd) * f32x2{sc1[0], sc1[1]} + f32x2{sh1[0], sh1[1]};
    const f32x2 r3 = (cv[4 * i + 3] * rstd) * f32x2{sc1[2], sc1[3]} + f32x2{sh1[2], sh1[3]};
    store(kap * 2 + tau, u32x4{pk(r0[0], r0[1]), pk(r1[0], r1[1]), pk(r2[0], r2[1]), pk(r3[0], r3[1])});
  }
}
__device__ __forceinline__ unsigned md_flag(int pr) { return pr < 32 ? FL_MD + FS * (unsigned)pr : FL_MD2 + FS * (unsigned)(pr - 32); }
__device__ __forceinline__ void load_mod(const Ctx& c, int f0, LAS float* dst, bool add1) {
  unsigned sp = 0;
  const unsigned fo = md_flag((f0 - 1280) / 64 + min(c.lane, 9));
  while (__any(ld_flag(c, fo) < 0)) { __builtin_amdgcn_s_sleep(8); spin_check(c, sp); }
  const int cc = min(c.tid, 159);
  f32x4 f = __builtin_bit_cast(f32x4, ld16<SC1>(c.ws, OFF_MOD + (unsigned)(f0 * 4 + cc * 16), 0));
  if (add1) { f[0] = rbf(1.f + f[0]); f[1] = rbf(1.f + f[1]); f[2] = rbf(1.f + f[2]); f[3] = rbf(1.f + f[3]); }
  *(LAS f32x4*)(dst + cc * 4) = f;
}

__device__ __forceinline__ void role_pre(const Params& p, Ctx& c, int li, int jpz) {
  const unsigned iod = my_iod();
  unsigned tabw = iod << 1;
  if (jpz >= 0) {
    const u32x4 sv = {SENT, SENT, SENT, SENT};
#pragma unroll
    for (int i = 0; i < 8; ++i) st16<SC1>(c.ws, (unsigned)c.tid * 16u, OFF_M + (unsigned)((i >> 2) * 40 + jpz) * 16384u + (unsigned)(i & 3) * 4096u, sv);
    if (c.tid < 128) st16<SC1>(c.ws, xr_phys(L_P + (unsigned)jpz * 1024u + (unsigned)(c.tid & 63) * 16u, c.tid >> 6), 0, sv);
    else if (c.tid < 192) st16<SC1>(c.ws, xr_phys(L_MEM + (unsigned)(1 + ((c.tid >> 4) & 1)) * MEM_SZ + (unsigned)jpz * 256u + (unsigned)(c.tid & 15) * 16u, (c.tid >> 5) & 1), 0, sv);
    else if (c.tid >= 252) { if (jpz < NBELL) st4<SC1>(c.ws, OFF_DB + (unsigned)(jpz + 16 * (c.tid & 1)) * FS + (unsigned)((c.tid >> 1) & 1) * 8192u, 0, SENT); }
    else if (c.tid < 224) {
      const u32x4 m0 = ld16<0>(mk_rsrc(p.mem0, 8 * DM * 2), (unsigned)((c.tid & 7) * DM * 2 + jpz * 32 + ((c.tid >> 3) & 1) * 16), 0);
      st16<SC1>(c.ws, xr_phys(L_MEM + (unsigned)jpz * 256u + (unsigned)(c.tid & 15) * 16u, (c.tid >> 4) & 1), 0, nosent(m0));
    }
  }
  LAS float* sc = (LAS float*)c.lds;
  LAS float* red = (LAS float*)(c.lds + 4096);
  rsrc_t rw = mk_rsrc(p.wmod, DM * 3840 * 2);
  const int cg = c.tid & 7, rg = c.tid >> 3;
  u32x4 w[20];
  const unsigned cv3 = ld4<0>(mk_rsrc(p.c, DM * 2), (unsigned)c.tid * 4u, 0), cv4 = ld4<0>(mk_rsrc(p.c, DM * 2), (unsigned)c.tid * 4u + 1024u, 0);
  const int r0 = (li / 20) * 224 + rg * 7;
  if (li < 60) {
#pragma unroll
    for (int i = 0; i < 7; ++i) w[i] = ld16<0>(rw, (unsigned)(((r0 + i) * 3840 + (li % 20) * 64 + cg * 8) * 2), 0);
  }
  rsrc_t rx = mk_rsrc(p.x, 2048 * DM * 2);
  u32x4 raw[10];
  {
    const int tau = c.wave & 1, kh = c.wave >> 1, g = c.lane >> 4, rho = c.lane & 15;
    const unsigned vo = (unsigned)(((li * 32 + tau * 16 + rho) * DM + (40 * kh + g) * 8) * 2);
#pragma unroll
    for (int i = 0; i < 10; ++i) raw[i] = ld16<0>(rx, vo + i * 64, 0);
  }
  sb();
  load_tag(c);
  stampl(c, 300);
  auto silu = [](float v) { return rbf(v / (1.f + __expf(-v))); };
  *(LAS f32x2*)(sc + 2 * c.tid) = f32x2{silu(bf_lo(cv3)), silu(bf_hi(cv3))};
  if (c.tid < 80) *(LAS f32x2*)(sc + 512 + 2 * c.tid) = f32x2{silu(bf_lo(cv4)), silu(bf_hi(cv4))};
  bar();
  stampl(c, 301);
  auto gemv = [&](auto nrow, int row0, unsigned dst, bool round, unsigned fl) {
    float a[8] = {};
#pragma unroll
    for (int i = 0; i < nrow; ++i) {
      const float sv = sc[row0 + i];
#pragma unroll
      for (int d = 0; d < 4; ++d) { a[2 * d] += sv * bf_lo(w[i][d]); a[2 * d + 1] += sv * bf_hi(w[i][d]); }
    }
    *(LAS f32x4*)(red + rg * 64 + cg * 8) = f32x4{a[0], a[1], a[2], a[3]};
    *(LAS f32x4*)(red + rg * 64 + cg * 8 + 4) = f32x4{a[4], a[5], a[6], a[7]};
    bar();
    float t = 0.f;
#pragma unroll
    for (int r = 0; r < 8; ++r) t += red[(c.wave * 8 + r) * 64 + c.lane];
    red[2048 + c.tid] = t;
    bar();
    if (c.tid < 64) {
      t = (red[2048 + c.tid] + red[2112 + c.tid]) + (red[2176 + c.tid] + red[2240 + c.tid]);
      st4<SC1>(c.ws, dst + (unsigned)c.tid * 4, 0, u_(round ? rbf(t) : t));
      wait_vm<0>();
      if (c.tid == 0) st_flag(c, fl, 0);
    }
  };
  if (li < 60) gemv(std::integral_constant<int, 7>{}, r0, OFF_MODP + (unsigned)((li / 20) * 1280 + (li % 20) * 64) * 4, false, FL_MP + FS * li);
  stampl(c, 302);
  LAS float* shiftv = (LAS float*)(c.lds + 16384);
  LAS float* red2 = (LAS float*)(c.lds + 24576);
  f32x2 cv[40];
  const float rstd = ln_stats(c, raw, cv, red2);
  stampl(c, 303);
  wait_flags<4>(c, FL_MP, 60, 0);
#pragma unroll
  for (int ch = c.tid; ch < 512; ch += 256) {
    const int cc = min(ch, 319);
    const u32x4 v0 = ld16<SC1>(c.ws, (unsigned)cc * 16, OFF_MODP), v1 = ld16<SC1>(c.ws, (unsigned)cc * 16, OFF_MODP + 5120), v2 = ld16<SC1>(c.ws, (unsigned)cc * 16, OFF_MODP + 10240);
    f32x4 f = __builtin_bit_cast(f32x4, v0) + __builtin_bit_cast(f32x4, v1) + __builtin_bit_cast(f32x4, v2);
    const float one = cc >= 160 ? 1.f : 0.f;
#pragma unroll
    for (int d = 0; d < 4; ++d) f[d] = rbf(one + rbf(f[d]));
    *(LAS f32x4*)(shiftv + cc * 4) = f;
  }
  bar();
  stampl(c, 304);
  ln_apply(c, cv, rstd, shiftv, shiftv + DM, [&](int fr, u32x4 o) { st16<SC1>(c.ws, (unsigned)c.lane * 16 + (unsigned)fr * 1024, OFF_HB + (unsigned)li * HB_SZ, o); });
  stampl(c, 305);
  if (li >= 32 && c.wave == 0) tabw |= calibrate(c, li - 32) ^ iod;
  publish(c, FL_H + FS * li, (int)tabw);
  stamp(c, 1);
  if (li >= 24) {
#pragma unroll
    for (int i = 0; i < 20; ++i) w[i] = ld16<0>(rw, (unsigned)(((rg * 20 + i) * 3840 + 1280 + (li - 24) * 64 + cg * 8) * 2), 0);
    sb();
    gemv(std::integral_constant<int, 20>{}, rg * 20, OFF_MOD + (unsigned)(1280 + (li - 24) * 64) * 4, true, md_flag(li - 24));
  }
}

__device__ __forceinline__ int sp_row(int lane) { return ((lane >> 1) & 6) | (lane & 1); }
__device__ __forceinline__ int sp_half(int lane) { return (lane >> 1) & 1; }
__device__ __forceinline__ int sp_pos(int row, int half) { return 4 * (row >> 1) + 2 * half + (row & 1); }
__device__ __forceinline__ float add1(float a, float b) { float o; asm("v_add_f32 %0, %1, %2" : "=v"(o) : "v"(a), "v"(b)); return o; }
struct Gath { unsigned vo2, bvo; LAS unsigned char* w0; LAS unsigned char* w2; };
__device__ __forceinline__ Gath gather_setup(const Ctx& c, bool perm, unsigned side) {
  int r = c.lane & 7;
  if (perm) r = (r & 1) | ((r & 2) << 1) | ((r & 4) >> 1);
  const int wpos = ((c.lane & 48) + sp_pos(r, (c.lane >> 3) & 1)) * 16;
  Gath g;
  g.vo2 = c.wave < 2 ? (unsigned)c.lane * 16 : 0x80000000u;
  g.bvo = bell_vo(c, 16, side);
  g.w0 = c.lds + 2 * c.wave * 1024 + wpos;
  g.w2 = c.lds + (c.wave < 2 ? (8 + c.wave) * 1024 : 79872 + (c.wave - 2) * 1024) + wpos;
  return g;
}
__device__ __forceinline__ void gather_mem(const Ctx& c, const Gath& ga, int s, unsigned side, unsigned& nfail) {
  const unsigned lo = L_MEM + (unsigned)s * MEM_SZ;
  unsigned s0 = xr_phys(lo + c.wave * 2048, side), s2 = xr_phys(lo + 8192 + (c.wave & 1) * 1024, side);
  pin_s(s0); pin_s(s2);
  unsigned sp = 0;
  u32x2 keep = {0u, 0u};
  if (s > 0) wait_bells(c, ga.bvo, s, keep);
  if (BELL_M_GAP) __builtin_amdgcn_s_sleep(BELL_M_GAP);
  for (;;) {
    const u32x4 v0 = ld16<SC1>(c.ws, (unsigned)c.lane * 16, s0), v1 = ld16<SC1>(c.ws, (unsigned)c.lane * 16 + 1024, s0), v2 = ld16<SC1>(c.ws, ga.vo2, s2);
    sb();
    *(LAS u32x4*)(ga.w0) = v0;
    unsigned m = umax4(0u, v0);
    sb();
    *(LAS u32x4*)(ga.w0 + 1024) = v1;
    m = umax4(m, v1);
    sb();
    *(LAS u32x4*)(ga.w2) = v2;
    m = umax4(m, v2);
    if (!__any(m == SENT)) break;
    spin_check(c, sp);
  }
  asm volatile("" :: "v"(keep.x), "v"(keep.y));
  bar();
}
struct ResW { LAS unsigned char* lo; LAS unsigned char* hi; };
__device__ __forceinline__ ResW res_base(const Ctx& c) {
  ResW r; r.lo = c.lds + 81920 + stage_rd(c.lane, c.wave); r.hi = r.lo + 40960;
  pin_v(r.lo); pin_v(r.hi);
  return r;
}
__device__ __forceinline__ u32x8 res_frag(const ResW& w, int t) {
  LAS unsigned char* b = t < 5 ? w.lo + t * 8192 : w.hi + (t - 5) * 8192;
  return cat(stage_frag(b), stage_frag(b + 4096));
}
template <int CTRL> __device__ __forceinline__ unsigned dppu(unsigned x) { return (unsigned)__builtin_amdgcn_update_dpp(0, (int)x, CTRL, 0xf, 0xf, true); }
__device__ __forceinline__ float row16_max(float m) {
  asm volatile("s_nop 1\n\tv_max_f32_dpp %0, %0, %0 row_ror:8 row_mask:0xf bank_mask:0xf\n\t"
               "s_nop 1\n\tv_max_f32_dpp %0, %0, %0 row_ror:4 row_mask:0xf bank_mask:0xf\n\t"
               "s_nop 1\n\tv_max_f32_dpp %0, %0, %0 row_ror:2 row_mask:0xf bank_mask:0xf\n\t"
               "s_nop 1\n\tv_max_f32_dpp %0, %0, %0 row_ror:1 row_mask:0xf bank_mask:0xf" : "+v"(m));
  return m;
}
__device__ __forceinline__ float row16_sum(float m) {
  asm volatile("s_nop 1\n\tv_add_f32_dpp %0, %0, %0 row_ror:8 row_mask:0xf bank_mask:0xf\n\t"
               "s_nop 1\n\tv_add_f32_dpp %0, %0, %0 row_ror:4 row_mask:0xf bank_mask:0xf\n\t"
               "s_nop 1\n\tv_add_f32_dpp %0, %0, %0 row_ror:2 row_mask:0xf bank_mask:0xf\n\t"
               "s_nop 1\n\tv_add_f32_dpp %0, %0, %0 row_ror:1 row_mask:0xf bank_mask:0xf" : "+v"(m));
  return m;
}

constexpr int hmap(int id) { return (REV && id < 100) ? (9 - id / 10) * 10 + id % 10 : id; }
constexpr int vmap(int id) {
  if (!REV || id >= 100) return id;
  const int f = id / 2, a = id % 2;
  const int blk = f / 5, k = f % 5;
  return (k < 2 ? (19 - (2 * blk + k)) * 2 : 40 + (29 - (3 * blk + k - 2)) * 2) + a;
}
__device__ __forceinline__ void role_H(const Params& p, Ctx& c, int h) {
  u32x8 W[10][5];
  u32x2 tabv, kfl;
  const unsigned kmvo = (c.lane & 8) ? (unsigned)((c.lane >> 4) * 64 + (c.lane & 7) * 8 + c.wave * 256) : 0x80000000u;
#pragma unroll
  for (int i = 0; i < 6; ++i) st8<SC1>(c.ws, kmvo, OFF_KM + (unsigned)((i / 3) * NH + h) * 3072 + (unsigned)(i % 3) * 1024, u32x2{SENT, SENT});
  {
    rsrc_t rq = mk_rsrc(p.Wq, DM * NH * DH * 2), rk = mk_rsrc(p.Wk, DM * NH * DH * 2);
    const unsigned src = stage_src<1>(c, NH * DH), rd = stage_rd(c.lane, c.wave);
    stage_weights<true, 12, 2, 10, 4>(c,
      [&](auto ci, LAS unsigned char* dst) {
        constexpr int id = hmap(ci);
        if constexpr (id < 100) {
          constexpr int t = id / 10, pn = (id % 10) / 2, a = id % 2;
          constexpr int col = (pn < 3 ? pn : pn - 3) * 64;
          dma16<0>(pn < 3 ? rq : rk, dst, src, (unsigned)(((64 * t + 32 * a) * NH * DH + h * DH + col) * 2));
        } else {
          constexpr int sp = id - 100;
          dma16<0>(rk, dst, src, (unsigned)(((32 * sp) * NH * DH + h * DH + 128) * 2));
        }
      },
      [&](auto ci, LAS unsigned char* chunk) {
        constexpr int id = hmap(ci), t = id / 10, pn = (id % 10) / 2, a = id % 2;
        if constexpr (a == 1) {
          W[t][pn] = cat(stage_frag(chunk - 4096 + rd), stage_frag(chunk + rd));
        }
      },
      [&](auto ci) {
        constexpr int id = hmap(ci), t = id / 10, pn = (id % 10) / 2, a = id % 2;
        if constexpr (a == 1) { if constexpr (t * 5 + pn < 32) pin_a(W[t][pn]); else pin_v(W[t][pn]); }
      },
      [&](auto ci) { if constexpr (ci == 80) { tabv = ld_table(c); kfl = ld_flag_raw(c, FL_K + FS * h); } });
  }
  if (c.tid == 0) st_flag(c, FL_KM + FS * h, 0);
  Sides sd;
  get_sides(c, sd, (unsigned)flag_val(c, tabv));
  LAS unsigned char* A = c.lds + c.lane * 16;
  const int g = c.lane >> 4, n = c.lane & 15;
  LAS unsigned char* TW = c.lds + 10240 + c.wave * 1920 + n * 40 + g * 4;
  LAS unsigned char* TR = c.lds + 10240 + c.wave * 1920 + (c.lane >> 2) * 40 + (c.lane & 3) * 8;
  LAS unsigned char* PART = c.lds + 49152;
  LAS unsigned char* PW = PART + c.wave * 3328 + n * 208 + g * 16;
  const ResW WR = res_base(c);
  const int idx = (c.lane & 2) ? 0xEEEE : 0x4444;
  const int q = (c.wave & 1) * 4 + g, it = min(n, 9);
  LAS unsigned char* PR = PART + q * 208 + (it < 8 ? it : it + 2) * 16;
  const float lmask = n < 10 ? 1.f : 0.f;
  const unsigned plo = it < 8 ? (unsigned)((h >> 1) * 1024 + (it >> 2) * 512 + (h & 1) * 256 + sp_pos(q, (it >> 1) & 1) * 16) : (unsigned)(16384 + h * 128 + q * 16);
  const bool ringer = USE_BELL_P && h % (32 / NBELL) == 0 && c.wave == 0;
  const Gath ga = gather_setup(c, false, sd.me);
  const unsigned rvo = (ringer && c.lane < 2) ? OFF_DB + (unsigned)(h / (32 / NBELL)) * FS + (unsigned)c.lane * 8192u : 0x80000000u;
  unsigned nfail = 0;
  LAS unsigned char* KX = c.lds + 24576;
  int kready = __builtin_amdgcn_readfirstlane(flag_val(c, kfl));
  auto dma_kx = [&](int s) {
    if (kready < s) kready = wait_flags<2>(c, FL_K + FS * h, 1, s);
#pragma unroll
    for (int i = 0; i < 6; ++i)
      dma16<SC1>(c.ws, KX + (s & 1) * KX_SZ + (c.wave * 6 + i) * 1024, (unsigned)c.lane * 16, OFF_KX + (unsigned)(s * NH + h) * KX_SZ + (unsigned)(c.wave * 6 + i) * 1024);
  };
  stamp(c, 1);
  if (c.wave < 2) dma_kx(0);
  for (int s = 0; s < NSEG; ++s) {
    unsigned pvo = xr_phys(L_P + (unsigned)s * P_SZ + plo, n & 1);
    if (n >= 10) pvo = 0x80000000u;
    f32x4 acc[6] = {};
    u32x8 wl[10];
    wl[0] = res_frag(WR, 0); wl[1] = res_frag(WR, 1);
#pragma unroll
    for (int i = 0; i < 6; ++i) pin_v(acc[i]);
    pin_v(wl[0]); pin_v(wl[1]); pin_v(pvo);
    gather_mem(c, ga, s, sd.me, nfail);
    stampf(c, 4 + 4 * s, 128);
    CYC(t0)
    u32x4 a[10], kx[3];
    a[0] = lds16(A); a[1] = lds16(A + 1024); a[2] = lds16(A + 2048);
    sb();
#pragma unroll
    for (int t = 0; t < 6; ++t) {
      LAS unsigned char* wb = t + 2 < 5 ? WR.lo + (t + 2) * 8192 : WR.hi + (t + 2 - 5) * 8192;
      acc[5] = smfma(a[t], wl[t], acc[5], idx); sb();
      a[t + 3] = lds16(A + (t + 3) * 1024); sb();
      acc[0] = smfma(a[t], W[t][0], acc[0], idx); sb();
      const u32x2 w0 = tr8(wb); sb();
      acc[1] = smfma(a[t], W[t][1], acc[1], idx); sb();
      const u32x2 w1 = tr8(wb + 1024); sb();
      acc[2] = smfma(a[t], W[t][2], acc[2], idx); sb();
      const u32x2 w2 = tr8(wb + 4096); sb();
      acc[3] = smfma(a[t], W[t][3], acc[3], idx); sb();
      const u32x2 w3 = tr8(wb + 5120); sb();
      acc[4] = smfma(a[t], W[t][4], acc[4], idx); sb();
      wl[t + 2] = cat(cat(w0, w1), cat(w2, w3));
    }
    CYC(t1)
    f32x4 sx0, sx1, sm;
    u32x2 F[3];
    float v[4];
#define H_S(i, t) acc[i] = smfma(a[t], W[t][i], acc[i], idx); sb();
#define H_L(t) acc[5] = smfma(a[t], wl[t], acc[5], idx); sb();
#define H_A(i, r) v[r] = add1(acc[(r >> 1) * 3 + i][r & 1], acc[(r >> 1) * 3 + i][2 + (r & 1)]); sb();
#define H_K(i) kx[i] = lds16(KX + (s & 1) * KX_SZ + (4 * i + c.wave) * 1024 + c.lane * 16); sb();
#define H_MZ(d, x, i) asm volatile("v_mfma_f32_16x16x16_bf16 %0, %1, %2, 0" : "=&v"(d) : "v"(x), "v"(F[i])); sb();
#define H_MA(d, x, i) asm volatile("v_mfma_f32_16x16x16_bf16 %0, %1, %2, %0" : "+v"(d) : "v"(x), "v"(F[i])); sb();
#define KLO(i) (u32x2{kx[i].x, kx[i].y})
#define KHI(i) (u32x2{kx[i].z, kx[i].w})
#define H_C(r) pw[r] = pk(v[2 * r], v[2 * r + 1]); sb();
#define H_D(i) *(LAS unsigned*)(TW + i * 640) = pw[0]; *(LAS unsigned*)(TW + i * 640 + 16) = pw[1]; F[i] = tr8(TR + i * 640); sb();
    unsigned pw[2];
    {
      LAS unsigned char* wb = WR.hi + 3 * 8192;
      H_S(1, 6) a[9] = lds16(A + 9 * 1024); const u32x2 w0 = tr8(wb); sb(); H_S(4, 6) const u32x2 w1 = tr8(wb + 1024); sb();
      H_S(1, 7) const u32x2 w2 = tr8(wb + 4096); sb(); H_S(4, 7) const u32x2 w3 = tr8(wb + 5120); sb();
      wl[8] = cat(cat(w0, w1), cat(w2, w3));
      H_S(1, 8) H_S(4, 8) H_S(1, 9) H_S(4, 9)
      H_L(6) const u32x2 w4 = tr8(wb + 8192), w5 = tr8(wb + 9216); sb(); H_S(2, 6) const u32x2 w6 = tr8(wb + 12288), w7 = tr8(wb + 13312); sb();
      wl[9] = cat(cat(w4, w5), cat(w6, w7));
    }
    H_L(7) H_A(1, 0) H_K(1) H_S(2, 7) H_A(1, 1) H_L(8) H_A(1, 2) H_S(2, 8) H_A(1, 3) H_S(2, 9) H_C(0) H_K(2) H_L(9) H_C(1)
    H_S(0, 6) H_D(1) H_S(3, 6) H_K(0) H_S(0, 7) H_A(2, 0) H_S(3, 7) H_A(2, 1) H_S(0, 8) H_A(2, 2) H_S(3, 8) H_A(2, 3) H_S(0, 9) H_C(0) H_S(3, 9) H_C(1) H_D(2)
    H_MZ(sx0, KLO(1), 1)
    stampf(c, 5 + 4 * s, 128);
    CYC(t2)
    asm volatile("s_nop 1" : "+v"(acc[0]));
    H_A(0, 0) H_A(0, 1) H_A(0, 2) H_A(0, 3) H_C(0) H_C(1) H_D(0) H_MZ(sx1, KHI(1), 1)
    if (BELL_P_POS == 5) { st4<SC1>(c.ws, rvo, 0, (unsigned)s); sb(); }
    H_MZ(sm, F[1], 1)
    H_MA(sx0, KLO(2), 2) H_MA(sx1, KHI(2), 2) H_MA(sm, F[2], 2)
    H_MA(sx0, KLO(0), 0) H_MA(sx1, KHI(0), 0) H_MA(sm, F[0], 0)
    asm volatile("s_nop 5" : "+v"(sx0), "+v"(sx1));
    CYC(t3)
    if (s < NSEG - 1) {
      *(LAS f32x4*)(PW) = sx0; sb(); *(LAS f32x4*)(PW + 64) = sx1; sb(); *(LAS f32x4*)(PW + 128) = sm;
      bar();
      CYC(t4)
      if (__builtin_expect(c.wave >= 2, 1)) {
        const f32x4 p0 = *(LAS f32x4*)PR, p1 = *(LAS f32x4*)(PR + 3328), p2 = *(LAS f32x4*)(PR + 6656), p3 = *(LAS f32x4*)(PR + 9984);
        const f32x4 xa = p0 + p1, xb = p2 + p3;
        CYC(t4a)
        u32x4 o;
        float x0, x1, x2, x3, ml, mm, t, d;
        asm volatile(
            "v_add_f32 %4, %12, %16\n\tv_add_f32 %5, %13, %17\n\tv_add_f32 %6, %14, %18\n\tv_add_f32 %7, %15, %19\n\t"
            "v_max3_f32 %8, %4, %5, %6\n\tv_max_f32 %8, %8, %7\n\t"
            "v_mul_f32 %10, %20, %8\n\tv_fma_f32 %4, %4, %20, -%10\n\t"
            "v_max_f32_dpp %9, %8, %8 row_ror:8 row_mask:0xf bank_mask:0xf\n\t"
            "v_fma_f32 %5, %5, %20, -%10\n\tv_fma_f32 %6, %6, %20, -%10\n\t"
            "v_max_f32_dpp %9, %9, %9 row_ror:4 row_mask:0xf bank_mask:0xf\n\t"
            "v_fma_f32 %7, %7, %20, -%10\n\tv_exp_f32 %4, %4\n\t"
            "v_max_f32_dpp %9, %9, %9 row_ror:2 row_mask:0xf bank_mask:0xf\n\t"
            "v_exp_f32 %5, %5\n\tv_exp_f32 %6, %6\n\t"
            "v_max_f32_dpp %9, %9, %9 row_ror:1 row_mask:0xf bank_mask:0xf\n\t"
            "v_exp_f32 %7, %7\n\t"
            "v_fma_f32 %11, %9, -%20, %10\n\t"
            "v_add_f32 %8, %4, %5\n\t"
            "v_exp_f32 %11, %11\n\t"
            "v_add_f32 %9, %6, %7\n\t"
            "v_add_f32 %8, %8, %9\n\t"
            "v_mul_f32 %10, %11, %21\n\t"
            "v_mul_f32 %8, %8, %10\n\t"
            "v_mul_f32 %4, %4, %11\n\tv_mul_f32 %5, %5, %11\n\t"
            "v_add_f32_dpp %8, %8, %8 row_ror:8 row_mask:0xf bank_mask:0xf\n\t"
            "v_mul_f32 %6, %6, %11\n\tv_mul_f32 %7, %7, %11\n\t"
            "v_add_f32_dpp %8, %8, %8 row_ror:4 row_mask:0xf bank_mask:0xf\n\t"
            "s_nop 1\n\t"
            "v_add_f32_dpp %8, %8, %8 row_ror:2 row_mask:0xf bank_mask:0xf\n\t"
            "s_nop 1\n\t"
            "v_add_f32_dpp %8, %8, %8 row_ror:1 row_mask:0xf bank_mask:0xf\n\t"
            "v_rcp_f32 %8, %8\n\t"
            "s_nop 0\n\t"
            "v_mul_f32 %4, %4, %8\n\tv_mul_f32 %5, %5, %8\n\tv_mul_f32 %6, %6, %8\n\tv_mul_f32 %7, %7, %8\n\t"
            "v_cvt_pk_bf16_f32 %9, %4, %5\n\tv_cvt_pk_bf16_f32 %10, %6, %7\n\t"
            "s_nop 0\n\t"
            "v_min_u32_dpp %0, %9, %22 quad_perm:[0,0,2,2] row_mask:0xf bank_mask:0xf\n\t"
            "v_min_u32_dpp %2, %9, %22 quad_perm:[1,1,3,3] row_mask:0xf bank_mask:0xf\n\t"
            "v_min_u32_dpp %1, %10, %22 quad_perm:[0,0,2,2] row_mask:0xf bank_mask:0xf\n\t"
            "v_min_u32_dpp %3, %10, %22 quad_perm:[1,1,3,3] row_mask:0xf bank_mask:0xf"
            : "=&v"(o.x), "=&v"(o.y), "=&v"(o.z), "=&v"(o.w), "=&v"(x0), "=&v"(x1), "=&v"(x2), "=&v"(x3), "=&v"(ml), "=&v"(mm), "=&v"(t), "=&v"(d)
            : "v"(xa[0]), "v"(xa[1]), "v"(xa[2]), "v"(xa[3]), "v"(xb[0]), "v"(xb[1]), "v"(xb[2]), "v"(xb[3]),
              "s"(0.07216878364870322f * 1.4426950408889634f), "v"(lmask), "v"(0xFFFFFFFEu));
        CYC(t4b)
        st16<SC1>(c.ws, pvo, 0, o);
        stampf(c, 6 + 4 * s, 128);
        CYC(t5)
        if (H_D2) __builtin_amdgcn_s_sleep(H_D2);
        if (n < 10 && s + 2 < NSEG - 1) st16<SC1>(c.ws, xr_phys(L_P + (unsigned)(s + 2) * P_SZ + plo, n & 1), 0, u32x4{SENT, SENT, SENT, SENT});
      } else {
            if (H_D0) __builtin_amdgcn_s_sleep(H_D0);
        dma_kx(s + 1);
        if (H_D1) __builtin_amdgcn_s_sleep(H_D1);
      }
    }
#pragma unroll
    for (int i = 0; i < 3; ++i) st8<SC1>(c.ws, kmvo, OFF_KM + (unsigned)(s * NH + h) * 3072 + (unsigned)i * 1024, nosent(F[i]));
    if (KM_BELL && c.tid == 0) st_flag(c, FL_KM + FS * h, s + 1);
    if (s + 2 < NSEG) {
#pragma unroll
      for (int i = 0; i < 3; ++i) st8<SC1>(c.ws, kmvo, OFF_KM + (unsigned)((s + 2) * NH + h) * 3072 + (unsigned)i * 1024, u32x2{SENT, SENT});
    }
    __builtin_amdgcn_s_sleep(H_SLEEP);
  }
}

__device__ __forceinline__ void role_V(const Params& p, Ctx& c, int h) {
  u32x8 Wv[10][2];
  u32x8 Wo[10][3];
  u32x2 tabv;
  {
    rsrc_t rv = mk_rsrc(p.Wv, DM * NH * DH * 2), ro = mk_rsrc(p.Wo, DM * NH * DH * 2);
    const unsigned srcv = stage_src<1>(c, NH * DH), srco = stage_src<1>(c, DM), rd = stage_rd(c.lane, c.wave);
    stage_weights<true, 12, 2, 10, 4>(c,
      [&](auto ci, LAS unsigned char* dst) {
        constexpr int id = vmap(ci);
        if constexpr (id < 40) {
          constexpr int t = id / 4, pn = (id % 4) / 2, a = id % 2;
          dma16<0>(rv, dst, srcv, (unsigned)(((64 * t + 32 * a) * NH * DH + h * DH + pn * 64) * 2));
        } else if constexpr (id < 100) {
          constexpr int f = (id - 40) / 2, a = id % 2, t = f / 10, pn = f % 10;
          dma16<0>(ro, dst, srco, (unsigned)(((h * DH + 64 * t + 32 * a) * DM + pn * 64) * 2));
        } else {
          constexpr int sp = id - 100;
          dma16<0>(rv, dst, srcv, (unsigned)(((32 * sp) * NH * DH + h * DH + 128) * 2));
        }
      },
      [&](auto ci, LAS unsigned char* chunk) {
        constexpr int id = vmap(ci);
        if constexpr (id % 2 == 1) {
          u32x8 f = cat(stage_frag(chunk - 4096 + rd), stage_frag(chunk + rd));
          if constexpr (id < 40) Wv[id / 4][(id % 4) / 2] = f;
          else Wo[((id - 40) / 2) % 10][((id - 40) / 2) / 10] = f;
        }
      },
      [&](auto ci) {
        constexpr int id = vmap(ci);
        if constexpr (id % 2 == 1) {
          if constexpr (id < 36) pin_v(Wv[id / 4][(id % 4) / 2]);
          else if constexpr (id < 40) pin_a(Wv[id / 4][(id % 4) / 2]);
          else pin_a(Wo[((id - 40) / 2) % 10][((id - 40) / 2) / 10]);
        }
      },
      [&](auto ci) { if constexpr (ci == 80) tabv = ld_table(c); });
  }
  Sides sd;
  get_sides(c, sd, (unsigned)flag_val(c, tabv));
  LAS unsigned char* A = c.lds + c.lane * 16;
  const ResW WR = res_base(c);
  const int idx = (c.lane & 2) ? 0xEEEE : 0x4444;
  const int g = c.lane >> 4, n = c.lane & 15;
  LAS unsigned char* TW = c.lds + 10240 + c.wave * 256 + ((n & 7) * 4 + g) * 8 + (n >> 3) * 4;
  LAS unsigned char* TR = c.lds + 10240 + g * 256 + n * 8;
  unsigned nfail = 0;
  const Gath ga = gather_setup(c, true, sd.me);
  unsigned mj[3], ms[3];
#pragma unroll
  for (int u = 0; u < 3; ++u) {
    const int pn = u < 2 ? 4 * u + g : 8 + (g & 1);
    mj[u] = (unsigned)(4 * pn + c.wave);
    ms[u] = pz_side(sd, (int)mj[u]);
  }
  stamp(c, 1);
  for (int s = 0; s < NSEG; ++s) {
    unsigned mvo[3];
#pragma unroll
    for (int u = 0; u < 3; ++u) {
      mvo[u] = m_phys((unsigned)s, mj[u], ms[u]) + (unsigned)(h * 256 + n * 16);
      if (u == 2 && g >= 2) mvo[u] = 0x80000000u;
      pin_v(mvo[u]);
    }
    f32x4 acc[3] = {};
    constexpr int NW = V_NW, NA = V_NA;
    u32x8 wl[NW];
#pragma unroll
    for (int i = 0; i < NW; ++i) wl[i] = res_frag(WR, i);
#pragma unroll
    for (int i = 0; i < NW; ++i) pin_v(wl[i]);
#pragma unroll
    for (int i = 0; i < 3; ++i) pin_v(acc[i]);
    gather_mem(c, ga, s, sd.me, nfail);
    stampf(c, 4 + 4 * s);
    CYCW(u0)
    u32x4 a[NA];
#pragma unroll
    for (int t = 0; t < NA; ++t) a[t] = lds16(A + t * 1024);
    sb();
#pragma unroll
    for (int t = 0; t < 10; ++t) {
      LAS unsigned char* wb = t + NW < 5 ? WR.lo + (t + NW) * 8192 : WR.hi + (t + NW - 5) * 8192;
      acc[2] = smfma(a[t % NA], wl[t % NW], acc[2], idx);
      sb();
      if (t + NW < 10) {
        const u32x2 w0 = tr8(wb), w1 = tr8(wb + 1024);
        wl[t % NW][0] = w0.x; wl[t % NW][1] = w0.y; wl[t % NW][2] = w1.x; wl[t % NW][3] = w1.y;
      }
      sb();
      acc[0] = smfma(a[t % NA], Wv[t][0], acc[0], idx);
      sb();
      if (t + NW < 10) {
        const u32x2 w2 = tr8(wb + 4096), w3 = tr8(wb + 5120);
        wl[t % NW][4] = w2.x; wl[t % NW][5] = w2.y; wl[t % NW][6] = w3.x; wl[t % NW][7] = w3.y;
      }
      sb();
      acc[1] = smfma(a[t % NA], Wv[t][1], acc[1], idx);
      sb();
      if (t + NA < 10) a[t % NA] = lds16(A + (t + NA) * 1024);
      sb();
    }
    CYCW(u1)
    f32x4 m[10] = {};
    *(LAS unsigned*)(TW + 2048) = pk(acc[2][0] + acc[2][2], acc[2][1] + acc[2][3]);
    *(LAS unsigned*)(TW) = pk(acc[0][0] + acc[0][2], acc[0][1] + acc[0][3]);
    *(LAS unsigned*)(TW + 1024) = pk(acc[1][0] + acc[1][2], acc[1][1] + acc[1][3]);
    sb();
#pragma unroll
    for (int i = 0; i < 4; ++i) pin_v(m[i]);
    bar();
    CYCW(u2)
    u32x4 a2[3];
#pragma unroll
    for (int t = 0; t < 3; ++t) a2[t] = cat(tr8(TR + t * 1024), tr8(TR + t * 1024 + 128));
    sb();
#pragma unroll
    for (int i = 4; i < 10; ++i) pin_v(m[i]);
    sb();
    float v0, v1; unsigned x[10];
    auto xpose = [&](int u) {
      const int i2 = u < 2 ? 4 * u + 2 : 8, i3 = u < 2 ? 4 * u + 3 : 9;
      auto r0 = __builtin_amdgcn_permlane32_swap(x[4 * u], x[i2], false, false);
      auto r1 = __builtin_amdgcn_permlane32_swap(x[4 * u + 1], x[i3], false, false);
      auto q0 = __builtin_amdgcn_permlane16_swap(r0[0], r1[0], false, false);
      auto q1 = __builtin_amdgcn_permlane16_swap(r0[1], r1[1], false, false);
      st16<SC1>(c.ws, mvo[u], 0, u32x4{q0[0], q0[1], q1[0], q1[1]});
    };
    sfor<0, 36>([&](auto ic) {
      constexpr int i = ic, tl = (i / 6) * 2 + (i & 1), t = (i % 6) / 2;
      if constexpr (i < 30) { m[tl] = smfma(a2[t], Wo[tl][t], m[tl], idx); sb(); }
      else if constexpr (i == 30) { asm volatile("s_nop 3" : "+v"(m[8]), "+v"(m[9])); sb(); }
      if constexpr (i >= 8 || (i >= 6 && false)) {
        constexpr int k = i - 8, pr = k / 6, w = k % 6;
        (void)pr; (void)w;
      }
      constexpr int k = i - 7;
      if constexpr (k >= 0) {
        constexpr int pr = k / 6, w = k % 6, ta = 2 * pr + (w >= 3 ? 1 : 0), ww = w % 3;
        if constexpr (pr < 5) {
          if constexpr (ww == 0) { v0 = add1(m[ta][0], m[ta][2]); sb(); }
          if constexpr (ww == 1) { v1 = add1(m[ta][1], m[ta][3]); sb(); }
          if constexpr (ww == 2) { x[ta] = pks(v0, v1); sb(); }
          if constexpr (w == 5 && (pr & 1)) { xpose(pr >> 1); sb(); }
        }
      }
    });
    x[9] = pks(v0, v1);
    xpose(2);
    CYCW(u3)
    CYCOUT(s, u0, u1, u2, u3, u3, u3, u3)
    stampf(c, 6 + 4 * s);
    __builtin_amdgcn_s_sleep(V_SLEEP);
  }
}

__device__ __forceinline__ void role_PZ(const Params& p, Ctx& c, int j) {
  Sides sd;
  get_sides(c, sd);
  const unsigned mside = pz_side(sd, j);
  LAS unsigned char* R = c.lds;
  const int idx = (c.lane & 2) ? 0xEEEE : 0x4444;
  const int g = c.lane >> 4, n = c.lane & 15;
  int uready = -1;
  const unsigned l16 = (unsigned)c.lane * 16;
  const unsigned pmvo = (unsigned)(2 * g * 128 + sp_row(c.lane) * 16 + sp_half(c.lane) * 8);
  const u32x4 sv = {SENT, SENT, SENT, SENT};
  LAS float* rw = (LAS float*)(R + c.wave * 512) + 2 * g * 16 + n;
  LAS unsigned char* rp = R + (((c.lane >> 2) & 7) * 16 + (c.lane >> 5) * 8 + (c.lane & 3) * 2) * 4;
  const unsigned olo = (unsigned)(j * 256 + (c.lane >> 2) * 16);
  const bool ringer = USE_BELL_M && j % (40 / NBELL) == 0 && j / (40 / NBELL) < NBELL && c.wave == 3;
  const unsigned bvo = bell_vo(c, 0, sd.me);
  const unsigned rvo = (ringer && c.lane < 2) ? OFF_DB + (unsigned)(16 + j / (40 / NBELL)) * FS + (unsigned)c.lane * 8192u : 0x80000000u;
  unsigned nfail = 0; (void)nfail;
  bar();
  for (int s = 0; s < NSEG - 1; ++s) {
    if (uready < s) uready = wait_flags<2>(c, FL_U + 8 * FS * c.wave, 8, s);
    u32x4 u[8];
#pragma unroll
    for (int i = 0; i < 8; ++i) u[i] = ld16<SC1>(c.ws, l16, OFF_U + (unsigned)s * U_SZ + (unsigned)(j * 32 + 8 * c.wave + i) * 1024);
    u32x4 pa[4], mb[2]; u32x2 pm[2];
    const unsigned lp = L_P + (unsigned)s * P_SZ;
    unsigned sop = xr_phys(lp + (unsigned)c.wave * 4096, sd.me), som5 = xr_phys(lp + 16384 + (unsigned)c.wave * 1024, sd.me);
    pin_s(sop); pin_s(som5);
    unsigned som = m_phys((unsigned)s, (unsigned)j, mside) + (unsigned)c.wave * 2048;
    unsigned ovo = xr_phys(L_MEM + (unsigned)(s + 1) * MEM_SZ + olo, c.lane & 1);
    if ((c.lane & 2) || c.wave != 0) ovo = 0x80000000u;
    f32x4 acc;
    LAS float* rwb = rw + (s & 1) * 512;
    LAS unsigned char* rpb = rp + (s & 1) * 2048;
    pin_v(ovo); pin_v(rwb); pin_v(rpb);
    unsigned sp = 0;
    pin_s(som);
    u32x2 keep = {0u, 0u};
    wait_bells(c, bvo, s, keep);
    if (BELL_P_GAP0) __builtin_amdgcn_s_sleep(BELL_P_GAP0);
    mb[0] = ld16<SC1>(c.ws, l16, som); mb[1] = ld16<SC1>(c.ws, l16 + 1024, som);
    sb();
    if (BELL_P_GAP) __builtin_amdgcn_s_sleep(BELL_P_GAP);
    for (;;) {
      pm[0] = ld8<SC1>(c.ws, pmvo, som5); pm[1] = ld8<SC1>(c.ws, pmvo + 128, som5);
#pragma unroll
      for (int i = 0; i < 4; ++i) pa[i] = ld16<SC1>(c.ws, l16 + i * 1024, sop);
      sb();
      acc = f32x4{0.f, 0.f, 0.f, 0.f};
      pin_v(acc);
      acc = smfma(cat(pm[0], pm[1]), cat(mb[0], mb[1]), acc, idx);
      unsigned m = umax4(0u, cat(pm[0], pm[1]));
      m = umax4(umax4(m, mb[0]), mb[1]);
      sb();
#pragma unroll
      for (int i = 0; i < 4; ++i) {
        if (BELL_M_POS == 4 + i) { st4<SC1>(c.ws, rvo, 0, (unsigned)(s + 1)); sb(); }
        acc = smfma(pa[i], cat(u[2 * i], u[2 * i + 1]), acc, idx); m = umax4(m, pa[i]); sb();
      }
      if (!__any(m == SENT)) break;
      spin_check(c, sp);
      mb[0] = ld16<SC1>(c.ws, l16, som); mb[1] = ld16<SC1>(c.ws, l16 + 1024, som);
    }
    asm volatile("" :: "v"(keep.x), "v"(keep.y));
    stampf(c, 4 + 4 * s);
    rwb[0] = acc[0] + acc[2]; rwb[16] = acc[1] + acc[3];
    sb();
    if (BELL_M_POS == 2) st4<SC1>(c.ws, rvo, 0, (unsigned)(s + 1));
    bar();
    {
      const f32x2 v = (*(LAS f32x2*)(rpb) + *(LAS f32x2*)(rpb + 512)) + (*(LAS f32x2*)(rpb + 1024) + *(LAS f32x2*)(rpb + 1536));
      const unsigned d = pks(v[0], v[1]);
      st16<SC1>(c.ws, ovo, 0, u32x4{dppu<0x00>(d), dppu<0x55>(d), dppu<0xAA>(d), dppu<0xFF>(d)});
      stamp(c, 6 + 4 * s);
    }
    __builtin_amdgcn_s_sleep(PZ_SLEEP);
    if (c.wave == 0 && !(c.lane & 2) && s + 3 < NSEG) st16<SC1>(c.ws, xr_phys(L_MEM + (unsigned)(s + 3) * MEM_SZ + olo, c.lane & 1), 0, sv);
    if (s + 2 < NSEG) {
      const unsigned sa = m_phys((unsigned)(s + 2), (unsigned)j, mside) + (unsigned)c.wave * 2048;
      st16<SC1>(c.ws, l16, sa, sv); st16<SC1>(c.ws, l16 + 1024, sa, sv);
    }
  }
}

__device__ __forceinline__ void load_proj_w(Ctx& c, const void* Wm, int h, u32x4 (&W)[3][20]) {
  rsrc_t rw = mk_rsrc(Wm, DM * NH * DH * 2);
  const unsigned src = stage_src<0>(c, NH * DH), rd = stage_rd(c.lane, c.wave);
  stage_weights<true, 5, 0, 12, 3>(c,
    [&](auto ci, LAS unsigned char* dst) {
      constexpr int id = ci, kap = id / 3, pn = id % 3;
      dma16<0>(rw, dst, src, (unsigned)(((32 * kap) * NH * DH + h * DH + pn * 64) * 2));
    },
    [&](auto ci, LAS unsigned char* chunk) {
      constexpr int id = ci;
      W[id % 3][id / 3] = stage_frag(chunk + rd);
    },
    [&](auto ci) { constexpr int id = ci; pin_a(W[id % 3][id / 3]); }, [](auto) {});
}
__device__ __forceinline__ void dma_hb(const Ctx& c, int s, LAS unsigned char* slot) {
#pragma unroll
  for (int i = 0; i < 10; ++i)
    dma16<SC1>(c.ws, slot + (c.wave * 10 + i) * 1024, (unsigned)c.lane * 16, OFF_HB + (unsigned)s * HB_SZ + (unsigned)(c.wave * 10 + i) * 1024);
}
template <bool WA, bool WFIRST, bool ZERO>
__device__ __forceinline__ void mfma_v(f32x4& d, const u32x4& w, const u32x4& x) {
  if constexpr (ZERO) {
    if constexpr (WA && WFIRST) asm volatile("v_mfma_f32_16x16x32_bf16 %0, %1, %2, 0" : "=&v"(d) : "a"(w), "v"(x));
    else if constexpr (WA) asm volatile("v_mfma_f32_16x16x32_bf16 %0, %2, %1, 0" : "=&v"(d) : "a"(w), "v"(x));
    else if constexpr (WFIRST) asm volatile("v_mfma_f32_16x16x32_bf16 %0, %1, %2, 0" : "=&v"(d) : "v"(w), "v"(x));
    else asm volatile("v_mfma_f32_16x16x32_bf16 %0, %2, %1, 0" : "=&v"(d) : "v"(w), "v"(x));
  } else {
    if constexpr (WA && WFIRST) asm volatile("v_mfma_f32_16x16x32_bf16 %0, %1, %2, %0" : "+v"(d) : "a"(w), "v"(x));
    else if constexpr (WA) asm volatile("v_mfma_f32_16x16x32_bf16 %0, %2, %1, %0" : "+v"(d) : "a"(w), "v"(x));
    else if constexpr (WFIRST) asm volatile("v_mfma_f32_16x16x32_bf16 %0, %1, %2, %0" : "+v"(d) : "v"(w), "v"(x));
    else asm volatile("v_mfma_f32_16x16x32_bf16 %0, %2, %1, %0" : "+v"(d) : "v"(w), "v"(x));
  }
}
__device__ __forceinline__ void mfma_vn(f32x4& d, const u32x4& a, const u32x4& b) {
  asm volatile("s_nop 1\n\tv_mfma_f32_16x16x32_bf16 %0, %1, %2, %0" : "+v"(d) : "v"(a), "v"(b));
}
__device__ __forceinline__ void mfma_fence() { sb(); asm volatile("s_nop 7"); sb(); }

template <int NT, int K0, int K1, class Hook>
__device__ __forceinline__ void gemm_part(const u32x4 (&W)[NT][20], LAS unsigned char* hb, f32x4 (&acc)[NT][2], Hook&& hook) {
  u32x4 b[K1 - K0][2];
#pragma unroll
  for (int k = 0; k < 2; ++k) { b[k][0] = lds16(hb + (2 * (K0 + k)) * 1024); b[k][1] = lds16(hb + (2 * (K0 + k) + 1) * 1024); }
  sb();
  sfor<0, K1 - K0>([&](auto kc) {
    constexpr int k = kc, ka = K0 + k;
    sfor<0, 2 * NT>([&](auto ic) {
      constexpr int t = ic / NT, i = ic % NT;
      mfma_v<(ka * NT + i < 64), true, ka == 0>(acc[i][t], W[i][ka], b[k][t]);
      sb();
      if constexpr (ic < 2 && k + 2 < K1 - K0) { b[k + 2][ic] = lds16(hb + (2 * (ka + 2) + ic) * 1024); sb(); }
      if constexpr (ic == 2) { hook(kc); sb(); }
    });
  });
}
__device__ __forceinline__ void dma_hb1(const Ctx& c, int s, LAS unsigned char* slot, int i) {
  dma16<SC1>(c.ws, slot + (c.wave * 10 + i) * 1024, (unsigned)c.lane * 16, OFF_HB + (unsigned)s * HB_SZ + (unsigned)(c.wave * 10 + i) * 1024);
}
__device__ __forceinline__ u32x4 pack2(const f32x4& a, const f32x4& b) { return u32x4{pk(a[0], a[1]), pk(a[2], a[3]), pk(b[0], b[1]), pk(b[2], b[3])}; }

__device__ __forceinline__ void need_hb(const Ctx& c, unsigned long long& mask, int s) {
  if (c.wave != 0 || ((mask >> s) & 1ull)) return;
  unsigned sp = 0;
  for (;;) {
    mask = __ballot(ld_flag(c, FL_H + FS * (unsigned)c.lane) >= 0);
    if ((mask >> s) & 1ull) return;
    __builtin_amdgcn_s_sleep(4);
    spin_check(c, sp);
  }
}

__device__ __forceinline__ void role_KV(const Params& p, Ctx& c, int h, bool isV) {
  u32x4 W[3][20];
  load_proj_w(c, isV ? p.Wv : p.Wk, h, W);
  stamp(c, 1);
  unsigned long long hm = 0;
  need_hb(c, hm, 0);
  bar();
  stamp(c, 2);
  const unsigned obase = isV ? OFF_VX : OFF_KX, fl = (isV ? FL_V : FL_K) + FS * h;
  dma_hb(c, 0, c.lds);
  need_hb(c, hm, 1);
  bar();
  dma_hb(c, 1, c.lds + HB_SZ);
  auto kst = [&](int s, int i, const f32x4 (&a)[3][2]) {
    st16<KV_AUX>(c.ws, (unsigned)c.lane * 16, obase + (unsigned)(s * NH + h) * KX_SZ + (unsigned)(4 * i + c.wave) * 1024, pack2(a[i][0], a[i][1]));
  };
  f32x4 accA[3][2], accB[3][2];
#pragma unroll 1
  for (int s = 0; s < 2; ++s) {
    need_hb(c, hm, s + 2);
    if (s == 0) wait_vm<10>();
    bar();
    gemm_part<3, 0, 20>(W, c.lds + s * HB_SZ + c.lane * 16, accA, [&](auto k) { if constexpr (k < 10) dma_hb1(c, s + 2, c.lds + (s == 0 ? 2 : 0) * HB_SZ, k); });
    mfma_fence();
#pragma unroll
    for (int i = 0; i < 3; ++i) kst(s, i, accA);
    publish(c, fl, s);
    stamp(c, 4 + s);
  }
  int sl = 2;
  auto seg = [&](int s, f32x4 (&cur)[3][2], const f32x4 (&prev)[3][2], bool hasprev) {
    const int s2 = min(s + 2, NSEG - 1);
    need_hb(c, hm, s2);
    wait_vm<10>();
    bar();
    if (c.tid == 0 && s >= 4) st_flag(c, fl, s - 2);
    const int sl2 = sl == 0 ? 2 : sl - 1;
    gemm_part<3, 0, 20>(W, c.lds + sl * HB_SZ + c.lane * 16, cur, [&](auto k) {
      if constexpr (k >= 2 && k < 5) { if (hasprev) kst(s - 1, k - 2, prev); }
      if constexpr (k >= 6 && k < 16) dma_hb1(c, s2, c.lds + sl2 * HB_SZ, k - 6);
    });
    stamp(c, 4 + s);
    sl = sl == 2 ? 0 : sl + 1;
  };
#pragma unroll 1
  for (int s = 2; s < NSEG; s += 2) { seg(s, accA, accB, s > 2); seg(s + 1, accB, accA, true); }
  mfma_fence();
#pragma unroll
  for (int i = 0; i < 3; ++i) kst(NSEG - 1, i, accB);
  publish(c, fl, NSEG - 1);
}

__device__ __forceinline__ void role_U(const Params& p, Ctx& c, int h) {
  u32x4 W[10][6];
  {
    rsrc_t ro = mk_rsrc(p.Wo, DM * NH * DH * 2);
    const unsigned src = stage_src<2>(c, DM), rd = stage_rd(c.lane, c.wave);
    stage_weights<true, 6, 0, 10, 4>(c,
      [&](auto ci, LAS unsigned char* dst) {
        constexpr int id = ci, a = id / 10, pn = id % 10;
        dma16<0>(ro, dst, src, (unsigned)(((h * DH + 32 * a) * DM + pn * 64) * 2));
      },
      [&](auto ci, LAS unsigned char* chunk) {
        constexpr int id = ci;
        W[id % 10][id / 10] = stage_frag(chunk + rd);
      },
      [&](auto ci) { constexpr int id = ci; pin_a(W[id % 10][id / 10]); }, [](auto) {});
  }
  const int g = c.lane >> 4, rp = c.lane & 15;
  const int gs = rp >> 2, i4 = rp & 3;
  const unsigned aoff = (unsigned)((g * 16 + 8 * (i4 >> 1) + 4 * (gs & 1) + (i4 & 1)) * 16 + (gs >> 1) * 8);
  const unsigned uvo = (unsigned)((h >> 1) * 2048 + (g >> 1) * 1024 + ((h & 1) * 2 + (g & 1)) * 256 + rp * 16);
  int vready = -1, issued = -1;
  stamp(c, 1);
  auto dma_v = [&](int s) {
    LAS unsigned char* slot = c.lds + (s % 3) * KX_SZ;
#pragma unroll
    for (int i = 0; i < 3; ++i)
      dma16<SC1>(c.ws, slot + (c.wave * 3 + i) * 1024, (unsigned)c.lane * 16, OFF_VX + (unsigned)(s * NH + h) * KX_SZ + (unsigned)(c.wave * 3 + i) * 1024);
  };
  for (int s = 0; s < NSEG; ++s) {
    if (issued < s) {
      unsigned sp = 0;
      while ((vready = __builtin_amdgcn_readfirstlane(ld_flag(c, FL_V + FS * h))) < s) { __builtin_amdgcn_s_sleep(2); spin_check(c, sp); }
      dma_v(s); issued = s;
      wait_vm<0>();
    } else {
      wait_vm<10>();
    }
    bar();
    if (c.tid == 0 && s >= U_EAGER + 2) st_flag(c, FL_U + FS * h, s - 2);
    if (s + 1 < NSEG && vready >= s + 1) { dma_v(s + 1); issued = s + 1; }
    const u32x2 fv = ld_flag_raw(c, vready < min(s + 2, NSEG - 1) ? FL_V + FS * h : 0x80000000u);
    LAS unsigned char* sa0 = c.lds + (s % 3) * KX_SZ + aoff;
    LAS unsigned char* sa1 = sa0 + 32;
    pin_v(sa0); pin_v(sa1);
    u32x4 a[2][6];
#pragma unroll
    for (int k = 0; k < 6; ++k) {
      a[0][k] = cat(*(LAS u32x2*)(sa0 + (2 * k) * 1024), *(LAS u32x2*)(sa0 + (2 * k + 1) * 1024));
      a[1][k] = cat(*(LAS u32x2*)(sa1 + (2 * k) * 1024), *(LAS u32x2*)(sa1 + (2 * k + 1) * 1024));
    }
    sb();
    f32x4 d[10][2];
    u32x4 o;
    sfor<0, 10>([&](auto pc) {
      constexpr int pn = pc;
      sfor<0, 12>([&](auto ic) {
        constexpr int k = ic / 2, tp = ic % 2;
        mfma_v<true, false, k == 0>(d[pn][tp], W[pn][k], a[tp][k]);
        sb();
        if constexpr (pn > 0 && ic >= 7 && ic < 11) { constexpr int q = ic - 7; o[q] = pk(d[pn - 1][q / 2][2 * (q % 2)], d[pn - 1][q / 2][2 * (q % 2) + 1]); sb(); }
        if constexpr (pn > 0 && ic == 11) {
          st16<U_AUX>(c.ws, uvo, OFF_U + (unsigned)s * U_SZ + (unsigned)(4 * (pn - 1) + c.wave) * 32768, o);
          sb();
        }
      });
    });
    mfma_fence();
    st16<U_AUX>(c.ws, uvo, OFF_U + (unsigned)s * U_SZ + (unsigned)(36 + c.wave) * 32768, pack2(d[9][0], d[9][1]));
    {
      vready = max(vready, __builtin_amdgcn_readfirstlane(flag_val(c, fv)));
    }
    if (s < U_EAGER) {
      if (issued == s && vready >= s + 1) { dma_v(s + 1); issued = s + 1; }
      publish(c, FL_U + FS * h, s);
    }
    stamp(c, 4 + s);
  }
  publish(c, FL_U + FS * h, NSEG - 1);
}

__device__ __forceinline__ void role_Q(const Params& p, Ctx& c, int h) {
  const int g = c.lane >> 4, n = c.lane & 15;
  const int qt = c.wave * 8 + (c.lane >> 3), tau = qt >> 4, rho = qt & 15, it0 = c.lane & 7, it = min(it0, 4);
  const int T = it >> 1;
  const bool dead = (it0 > 4) || (tau == 0 && it >= 2 && it < 4);
  const unsigned pvo = dead ? 0x80000000u : it < 4 ? (unsigned)(((h >> 1) * 3 + tau + (tau ? T : 0)) * 1024 + (((h & 1) * 2 + (it & 1)) * 16 + rho) * 16)
                                                   : (unsigned)(65536 + ((h >> 2) * 2 + tau) * 1024 + ((h & 3) * 16 + rho) * 16);
  const u32x4 sv = {SENT, SENT, SENT, SENT};
  st16<SC1>(c.ws, pvo, OFF_PX, sv); st16<SC1>(c.ws, pvo, OFF_PX + PX_SZ, sv);
  u32x4 W[3][20];
  load_proj_w(c, p.Wq, h, W);
  if (c.tid == 0) st_flag(c, FL_PX + FS * h, 0);
  LAS unsigned char* PART = c.lds + 2 * HB_SZ;
  constexpr int QS = 336, WS_ = 16 * QS, BS_ = 4 * WS_;
  const int tile = it == 4 ? (tau ? 4 : 1) : (tau ? 2 + T : 0);
  const int ga = it == 4 ? 0 : (it & 1), gb = it == 4 ? 1 : (it & 1) + 2;
  const int pao = rho * QS + tile * 64 + ga * 16, pbo = rho * QS + tile * 64 + gb * 16;
  LAS unsigned char* PW = PART + c.wave * WS_ + n * QS + g * 16;
  f32x4 ba, bb;
#pragma unroll
  for (int r = 0; r < 4; ++r) {
    ba[r] = (dead || (it < 4 && 16 * T + 4 * ga + r > qt)) ? -__builtin_inff() : 0.f;
    bb[r] = (dead || (it < 4 && 16 * T + 4 * gb + r > qt)) ? -__builtin_inff() : 0.f;
  }
  if (c.wave == 0) wait_flags<8>(c, FL_H, 64, 0);
  if (c.wave == 1) wait_flags<8>(c, FL_KM + FS * h, 1, 0);
  bar();
  dma_hb(c, 0, c.lds); dma_hb(c, 1, c.lds + HB_SZ);
  wait_vm<10>();
  bar();
  u32x4 q[3], kx[3];
  int kready = -1;
  auto load_kx = [&](int s, int i) {
    kx[i] = ld16<SC1>(c.ws, (unsigned)c.lane * 16, OFF_KX + (unsigned)(s * NH + h) * KX_SZ + (unsigned)(4 * i + c.wave) * 1024);
  };
  auto xscores = [&](int s) {
    f32x4 s0 = {}, s2 = {}, s3 = {};
#pragma unroll
    for (int i = 0; i < 3; ++i) {
      const u32x2 q0 = {q[i].x, q[i].y}, q1 = {q[i].z, q[i].w}, k0 = {kx[i].x, kx[i].y}, k1 = {kx[i].z, kx[i].w};
      s0 = mfma16(k0, q0, s0); s2 = mfma16(k0, q1, s2); s3 = mfma16(k1, q1, s3);
    }
    LAS unsigned char* pw = PW + (s & 1) * BS_;
    *(LAS f32x4*)(pw) = s0; *(LAS f32x4*)(pw + 128) = s2; *(LAS f32x4*)(pw + 192) = s3;
  };
  {
    f32x4 acc[3][2];
    gemm_part<3, 0, 20>(W, c.lds + c.lane * 16, acc, [](auto) {});
    mfma_fence();
#pragma unroll
    for (int i = 0; i < 3; ++i) q[i] = pack2(acc[i][0], acc[i][1]);
    kready = wait_flags<8>(c, FL_K + FS * h, 1, 0);
#pragma unroll
    for (int i = 0; i < 3; ++i) load_kx(0, i);
    xscores(0);
  }
  const unsigned kmvo = (unsigned)(g * 64 + (n & 7) * 8 + c.wave * 256);
#pragma unroll 1
  for (int s = 0; s < NSEG; ++s) {
    u32x2 km[3];
    unsigned sp = 0;
    CYCW(u0)
    if (KM_BELL) doorbell<3, KM_DELAY>(c, FL_KM + FS * h, s + 1);
    for (;;) {
#pragma unroll
      for (int i = 0; i < 3; ++i) km[i] = ld8<SC1>(c.ws, kmvo, OFF_KM + (unsigned)(s * NH + h) * 3072 + (unsigned)i * 1024);
      if (!__any(umax3(umax3(km[0].x, km[0].y, km[1].x), umax3(km[1].y, km[2].x, km[2].y), 0u) == SENT)) break;
      __builtin_amdgcn_s_sleep(3);
      spin_check(c, sp);
    }
    wait_vm<0>();
    CYCW(u1)
    LAS unsigned char* PB = PART + (s & 1) * BS_;
    {
      f32x4 s1 = {}, s4 = {};
#pragma unroll
      for (int i = 0; i < 3; ++i) { s1 = mfma16(km[i], u32x2{q[i].x, q[i].y}, s1); s4 = mfma16(km[i], u32x2{q[i].z, q[i].w}, s4); }
      LAS unsigned char* pw = PW + (s & 1) * BS_;
      *(LAS f32x4*)(pw + 64) = s1; *(LAS f32x4*)(pw + 256) = s4;
    }
    if (EARLY_BELL && c.tid == 0) st_flag(c, FL_PX + FS * h, s + 1);
    bar();
    CYCW(u2)
    {
      LAS unsigned char* pa = PB + pao;
      LAS unsigned char* pb = PB + pbo;
      f32x4 va = ((*(LAS f32x4*)pa + ba) + *(LAS f32x4*)(pa + WS_)) + (*(LAS f32x4*)(pa + 2 * WS_) + *(LAS f32x4*)(pa + 3 * WS_));
      f32x4 vb = ((*(LAS f32x4*)pb + bb) + *(LAS f32x4*)(pb + WS_)) + (*(LAS f32x4*)(pb + 2 * WS_) + *(LAS f32x4*)(pb + 3 * WS_));
      float m = fmaxf(fmaxf(fmaxf(va[0], va[1]), fmaxf(va[2], va[3])), fmaxf(fmaxf(vb[0], vb[1]), fmaxf(vb[2], vb[3])));
      asm volatile("s_nop 1\n\tv_max_f32_dpp %0, %0, %0 quad_perm:[1,0,3,2] row_mask:0xf bank_mask:0xf\n\t"
                   "s_nop 1\n\tv_max_f32_dpp %0, %0, %0 quad_perm:[2,3,0,1] row_mask:0xf bank_mask:0xf\n\t"
                   "s_nop 1\n\tv_max_f32_dpp %0, %0, %0 row_half_mirror row_mask:0xf bank_mask:0xf" : "+v"(m));
      const float cs = 0.07216878364870322f * 1.4426950408889634f;
      const float mc = -m * cs;
      float l = 0.f;
#pragma unroll
      for (int r = 0; r < 4; ++r) {
        va[r] = __builtin_amdgcn_exp2f(__builtin_fmaf(va[r], cs, mc));
        vb[r] = __builtin_amdgcn_exp2f(__builtin_fmaf(vb[r], cs, mc));
        l += va[r] + vb[r];
      }
      l += dpp<0xB1>(l); l += dpp<0x4E>(l); l += dpp<0x141>(l);
      const float inv = __builtin_amdgcn_rcpf(l);
      st16<SC1>(c.ws, pvo, OFF_PX + (unsigned)s * PX_SZ,
                u32x4{pks(va[0] * inv, va[1] * inv), pks(vb[0] * inv, vb[1] * inv), pks(va[2] * inv, va[3] * inv), pks(vb[2] * inv, vb[3] * inv)});
    }
    if (!EARLY_BELL && c.tid == 0) st_flag(c, FL_PX + FS * h, s + 1);
    CYCW(u3)
    stamp(c, 4 + s);
    if (s + 1 < NSEG) {
      if (s + 2 < NSEG) st16<SC1>(c.ws, pvo, OFF_PX + (unsigned)(s + 2) * PX_SZ, sv);
      if (kready < s + 1) kready = wait_flags<8>(c, FL_K + FS * h, 1, s + 1);
      const int s2 = min(s + 2, NSEG - 1);
      f32x4 acc[3][2];
      gemm_part<3, 0, 20>(W, c.lds + ((s + 1) & 1) * HB_SZ + c.lane * 16, acc, [&](auto k) {
        if constexpr (k < 3) load_kx(s + 1, k);
        if constexpr (k >= 3 && k < 13) dma_hb1(c, s2, c.lds + (s & 1) * HB_SZ, k - 3);
      });
      mfma_fence();
      CYCW(u4)
#pragma unroll
      for (int i = 0; i < 3; ++i) q[i] = pack2(acc[i][0], acc[i][1]);
      xscores(s + 1);
      CYCW(u5)
      CYCOUT(s, u0, u1, u2, u3, u4, u5, u5)
    }
  }
}

constexpr int X1_ARM = 6;
__device__ __forceinline__ void role_PX(const Params& p, Ctx& c, int jx) {
  LAS float* ga = (LAS float*)c.lds;
  LAS unsigned char* R = c.lds + 4096;
  const int g = c.lane >> 4, n = c.lane & 15;
  const unsigned l16 = (unsigned)c.lane * 16;
  const int nt = c.wave & 1, tau = c.wave >> 1;
  const int col = 32 * jx + 16 * nt + 4 * g;
  const unsigned xo = (unsigned)(((tau * 16 + n) * DM + col) * 2);
  const unsigned x1o = (unsigned)((jx * 2 + tau) * 1024 + ((2 * nt + (g >> 1)) * 16 + n) * 16 + (g & 1) * 8);
#pragma unroll
  for (int i = 0; i < X1_ARM; ++i) st8<SC1>(c.ws, x1o, OFF_X1 + (unsigned)i * HB_SZ, u32x2{SENT, SENT});
  publish(c, FL_X1 + FS * jx, 0);
  Sides sd;
  get_sides(c, sd);
  load_mod(c, 2 * DM, ga, false);
  wait_flags<8>(c, FL_PX + 8 * FS * c.wave, 8, 0);
  bar();
  const f32x4 gav = *(LAS f32x4*)(ga + col);
  rsrc_t rx = mk_rsrc(p.x, 2048 * DM * 2);
  const unsigned ms0 = pz_side(sd, 2 * jx), ms1 = pz_side(sd, 2 * jx + 1);
  int uready = -1;
  auto ld_u = [&](int s, int i) -> u32x4 {
    return ld16<SC1>(c.ws, l16, OFF_U + (unsigned)s * U_SZ + (unsigned)((2 * jx + (i & 1)) * 32 + 8 * c.wave + (i >> 1)) * 1024);
  };
  auto step = [&](int s, const u32x4 (&z)[16], u32x4 (&zn)[16]) {
    const int sn = min(s + 1, NSEG - 1);
    if (uready < sn) uready = wait_flags<8>(c, FL_U + 8 * FS * c.wave, 8, sn);
    const unsigned spx = OFF_PX + (unsigned)s * PX_SZ;
    CYCW(u0)
    doorbell<PX_SLEEP, PX_DELAY>(c, FL_PX, s + 1);
    CYCW(u1)
    const unsigned m0 = m_phys((unsigned)s, (unsigned)(2 * jx), ms0) + (unsigned)c.wave * 2048, m1 = m_phys((unsigned)s, (unsigned)(2 * jx + 1), ms1) + (unsigned)c.wave * 2048;
    u32x4 pa[12], pm[2][2], zm[2][2];
    f32x4 acc[2][2];
    const u32x2 xv = ld8<0>(rx, xo, (unsigned)s * HB_SZ);
    unsigned sp = 0;
    for (;;) {
#pragma unroll
      for (int i = 0; i < 2; ++i) {
        zm[i][0] = ld16<SC1>(c.ws, l16 + i * 1024, m0); zm[i][1] = ld16<SC1>(c.ws, l16 + i * 1024, m1);
#pragma unroll
        for (int t = 0; t < 2; ++t) pm[i][t] = ld16<SC1>(c.ws, l16, spx + 65536 + (unsigned)((2 * c.wave + i) * 2 + t) * 1024);
      }
#pragma unroll
      for (int i = 0; i < 12; ++i) pa[i] = ld16<SC1>(c.ws, l16, spx + (unsigned)(12 * c.wave + i) * 1024);
      sb();
      unsigned m = 0;
      sfor<0, 8>([&](auto ic) {
        constexpr int k = ic >> 2, t = (ic >> 1) & 1, u = ic & 1;
        mfma_v<false, true, k == 0>(acc[t][u], zm[k][t], pm[k][u]);
        sb();
        if constexpr (u == 0) m = umax4(m, zm[k][t]);
        if constexpr (t == 1) m = umax4(m, pm[k][u]);
        sb();
      });
      sfor<0, 24>([&](auto ic) {
        constexpr int f = ic >> 1, t = ic & 1, pr = f / 3, w = f % 3;
        mfma_v<false, true, false>(acc[t][w ? 1 : 0], z[2 * (2 * pr + (w == 2 ? 1 : 0)) + t], pa[f]);
        sb();
        if constexpr (t == 0) m = umax4(m, pa[f]);
        if constexpr (ic >= 8) { if (s + 1 < NSEG) zn[ic - 8] = ld_u(sn, ic - 8); }
        sb();
      });
      if (!__any(m == SENT)) break;
      wait_flags<4>(c, FL_PX + 8 * FS * c.wave, 8, s + 1);
      __builtin_amdgcn_s_sleep(2);
      spin_check(c, sp);
    }
    if (EARLY_BELL && c.tid == 0) st_flag(c, FL_X1 + FS * jx, s + 1);
    mfma_fence();
    CYCW(u2)
    LAS unsigned char* Rb = R + (s & 1) * 16384;
#pragma unroll
    for (int t = 0; t < 2; ++t)
#pragma unroll
      for (int u = 0; u < 2; ++u) *(LAS f32x4*)(Rb + c.wave * 4096 + (u * 2 + t) * 1024 + l16) = acc[t][u];
    bar();
    CYCW(u3)
    LAS unsigned char* rp = Rb + c.wave * 1024 + l16;
    const f32x4 v = (*(LAS f32x4*)rp + *(LAS f32x4*)(rp + 4096)) + (*(LAS f32x4*)(rp + 8192) + *(LAS f32x4*)(rp + 12288));
    const float o0 = bf_lo(xv.x) + rbf(gav[0] * rbf(v[0])), o1 = bf_hi(xv.x) + rbf(gav[1] * rbf(v[1]));
    const float o2 = bf_lo(xv.y) + rbf(gav[2] * rbf(v[2])), o3 = bf_hi(xv.y) + rbf(gav[3] * rbf(v[3]));
    st8<SC1>(c.ws, x1o, OFF_X1 + (unsigned)s * HB_SZ, u32x2{pks(o0, o1), pks(o2, o3)});
    if (!EARLY_BELL && c.tid == 0) st_flag(c, FL_X1 + FS * jx, s + 1);
    CYCW(u4)
    CYCOUT(s, u0, u1, u2, u3, u4, u4, u4)
    stamp(c, 4 + s);
    if (s + X1_ARM < NSEG) st8<SC1>(c.ws, x1o, OFF_X1 + (unsigned)(s + X1_ARM) * HB_SZ, u32x2{SENT, SENT});
    if (BUMP && s == 1 && jx == 0 && c.tid == 0) st4<SC1>(c.ws, FL_EPOCH, 0, (c.tag ^ 0x9E3779B9u) + 1u);
  };
  u32x4 za[16], zb[16];
  uready = wait_flags<8>(c, FL_U + 8 * FS * c.wave, 8, 0);
#pragma unroll
  for (int i = 0; i < 16; ++i) za[i] = ld_u(0, i);
#pragma unroll 1
  for (int s = 0; s < NSEG; s += 2) { step(s, za, zb); step(s + 1, zb, za); }
}

__device__ __forceinline__ int ffn_seg(int rep, int i) { return (i >> 1) * 4 + (rep & 2) + (i & 1); }

__device__ __forceinline__ void role_F1(const Params& p, Ctx& c, int rep, int f1) {
  const u32x4 sv = {SENT, SENT, SENT, SENT};
  const int tau = rep & 1, g = c.lane >> 4, rho = c.lane & 15;
  const unsigned avo = (unsigned)(c.lane * 16 + (f1 * 4 + c.wave) * 2048 + tau * 1024);
  st16<SC1>(c.ws, avo, OFF_ACT + (unsigned)ffn_seg(rep, 0) * ACT_SZ, sv);
  st16<SC1>(c.ws, avo, OFF_ACT + (unsigned)ffn_seg(rep, 1) * ACT_SZ, sv);
  LAS float* shiftv = (LAS float*)(c.lds + 131072);
  LAS float* scalev = (LAS float*)(c.lds + 131072 + 4096);
  bar();
  load_mod(c, 3 * DM, shiftv, false);
  load_mod(c, 4 * DM, scalev, true);
  u32x4 W[4][20];
  f32x4 bias[4] = {}, csum[4] = {};
  {
    rsrc_t rg = mk_rsrc(p.wg, DM * DFF * 2), ru = mk_rsrc(p.wu, DM * DFF * 2);
    const unsigned src = stage_src<0>(c, DFF), rd = stage_rd(c.lane, c.wave);
    const u32x4 ones = {0x3F803F80u, 0x3F803F80u, 0x3F803F80u, 0x3F803F80u};
    stage_weights<false, 10, 0, 8, 4>(c,
      [&](auto ci, LAS unsigned char* dst) {
        constexpr int id = ci, kap = id / 4, pn = id % 4;
        dma16<0>(pn < 2 ? rg : ru, dst, src, (unsigned)(((32 * kap) * DFF + f1 * 128 + (pn & 1) * 64) * 2));
      },
      [&](auto ci, LAS unsigned char* chunk) {
        constexpr int id = ci;
        W[id % 4][id / 4] = stage_frag(chunk + rd);
      },
      [&](auto) {},
      [&](auto ci) {
        constexpr int id = ci, kap = id / 4, i = id % 4;
        const f32x4 s0 = *(LAS f32x4*)(scalev + 32 * kap + 8 * g), s1 = *(LAS f32x4*)(scalev + 32 * kap + 8 * g + 4);
        const f32x4 h0 = *(LAS f32x4*)(shiftv + 32 * kap + 8 * g), h1 = *(LAS f32x4*)(shiftv + 32 * kap + 8 * g + 4);
        const u32x4 w = W[i][kap];
        mfma_vn(bias[i], w, pack2(h0, h1));
        W[i][kap] = u32x4{pk(bf_lo(w.x) * s0[0], bf_hi(w.x) * s0[1]), pk(bf_lo(w.y) * s0[2], bf_hi(w.y) * s0[3]),
                          pk(bf_lo(w.z) * s1[0], bf_hi(w.z) * s1[1]), pk(bf_lo(w.w) * s1[2], bf_hi(w.w) * s1[3])};
        mfma_vn(csum[i], W[i][kap], ones);
        if constexpr (id < 64) pin_a(W[i][kap]); else pin_v(W[i][kap]);
      });
  }
  if (c.tid == 0) st_flag(c, FL_ACT + FS * (16 * rep + f1), 0);
  LAS unsigned char* HB = c.lds;
  LAS float* red = (LAS float*)(c.lds + 40960);
  LAS float* rw = red + rho * 20 + c.wave * 4 + g;
  LAS float* rr = red + rho * 20;
  wait_flags<8>(c, FL_X1, 20, 0);
  stamp(c, 100);
  const unsigned vo = (unsigned)((10 * c.wave + tau) * 1024 + c.lane * 16);
  unsigned ones_u = 0x3F803F80u;
  pin_v(ones_u);
  auto sum16 = [&](LAS float* q) {
    const f32x4 t = (*(LAS f32x4*)q + *(LAS f32x4*)(q + 4)) + (*(LAS f32x4*)(q + 8) + *(LAS f32x4*)(q + 12));
    return (t[0] + t[1]) + (t[2] + t[3]);
  };
#pragma unroll 1
  for (int it = 0; it < 32; ++it) {
    const int s = ffn_seg(rep, it);
    const unsigned sx = OFF_X1 + (unsigned)s * HB_SZ;
    LAS unsigned char* hb = HB + (it & 1) * 20480 + c.lane * 16;
    CYCW(u0)
    doorbell<4, F1_DELAY>(c, FL_X1, s + 1);
    CYCW(u1)
    u32x4 raw[5];
    unsigned sp = 0;
    for (;;) {
      unsigned m = 0;
      float s0 = 0.f, s1 = 0.f;
#pragma unroll
      for (int i = 0; i < 5; ++i) raw[i] = ld16<SC1>(c.ws, vo, sx + i * 2048);
      sb();
#pragma unroll
      for (int i = 0; i < 5; ++i) {
        *(LAS u32x4*)(hb + (5 * c.wave + i) * 1024) = raw[i];
        m = umax4(m, raw[i]);
        s0 = dot2(raw[i].x, ones_u, s0); s1 = dot2(raw[i].y, ones_u, s1);
        s0 = dot2(raw[i].z, ones_u, s0); s1 = dot2(raw[i].w, ones_u, s1);
      }
      rw[0] = s0 + s1;
      if (!__any(m == SENT)) break;
      wait_flags<4>(c, FL_X1 + 5 * FS * c.wave, 5, s + 1);
      __builtin_amdgcn_s_sleep(2);
      spin_check(c, sp);
    }
    bar();
    CYCW(u2)
    const float mean = sum16(rr) * (1.f / DM);
    f32x4 acc[4];
    float v0 = 0.f, v1 = 0.f;
    {
      u32x4 b[20];
#pragma unroll
      for (int k = 0; k < 3; ++k) b[k] = lds16(hb + k * 1024);
      sb();
      sfor<0, 20>([&](auto kc) {
        constexpr int k = kc;
        const unsigned d = raw[k / 4][k % 4];
        float a0, a1;
        mfma_v<(k * 4 + 0 < 64), true, k == 0>(acc[0], W[0][k], b[k]); sb();
        if constexpr (k + 3 < 20) b[k + 3] = lds16(hb + (k + 3) * 1024);
        a0 = bf_lo(d); a1 = bf_hi(d); sb();
        mfma_v<(k * 4 + 1 < 64), true, k == 0>(acc[1], W[1][k], b[k]); sb();
        a0 = add1(a0, -mean); a1 = add1(a1, -mean); sb();
        mfma_v<(k * 4 + 2 < 64), true, k == 0>(acc[2], W[2][k], b[k]); sb();
        asm("v_fmac_f32 %0, %1, %1" : "+v"(v0) : "v"(a0)); asm("v_fmac_f32 %0, %1, %1" : "+v"(v1) : "v"(a1)); sb();
        mfma_v<(k * 4 + 3 < 64), true, k == 0>(acc[3], W[3][k], b[k]); sb();
      });
    }
    rw[320] = v0 + v1;
    if (EARLY_BELL && c.tid == 0) st_flag(c, FL_ACT + FS * (16 * rep + f1), it + 1);
    mfma_fence();
    bar();
    sb();
    CYCW(u3)
    const float rstd = __builtin_amdgcn_rsqf(sum16(rr + 320) * (1.f / DM) + 1e-5f);
    float a[8];
#pragma unroll
    for (int i = 0; i < 2; ++i)
#pragma unroll
      for (int r = 0; r < 4; ++r) {
        const float gv = __builtin_fmaf(__builtin_fmaf(-mean, csum[i][r], acc[i][r]), rstd, bias[i][r]);
        const float uv = __builtin_fmaf(__builtin_fmaf(-mean, csum[2 + i][r], acc[2 + i][r]), rstd, bias[2 + i][r]);
        const float z2 = gv * __builtin_fmaf(gv * gv, -2.f * 1.4426950408889634f * 0.7978845608028654f * 0.044715f, -2.f * 1.4426950408889634f * 0.7978845608028654f);
        a[4 * i + r] = gv * uv * __builtin_amdgcn_rcpf(1.f + __builtin_amdgcn_exp2f(z2));
      }
    st16<SC1>(c.ws, avo, OFF_ACT + (unsigned)s * ACT_SZ, u32x4{pks(a[0], a[1]), pks(a[2], a[3]), pks(a[4], a[5]), pks(a[6], a[7])});
    if (!EARLY_BELL && c.tid == 0) st_flag(c, FL_ACT + FS * (16 * rep + f1), it + 1);
    CYCW(u4)
    CYCOUT(s, u0, u1, u2, u3, u4, u4, u4)
    stamp(c, 104 + s);
    if (s + 4 < NSEG) st16<SC1>(c.ws, avo, OFF_ACT + (unsigned)(s + 4) * ACT_SZ, sv);
  }
}

__device__ __forceinline__ void role_F2(const Params& p, Ctx& c, int rep, int f2) {
  bar();
  u32x4 W[12][4];
  {
    rsrc_t rd_ = mk_rsrc(p.wd, DFF * DM * 2);
    const unsigned src = stage_src<3>(c, DM);
    unsigned rd[4];
#pragma unroll
    for (int u = 0; u < 4; ++u) rd[u] = stage_rd(c.lane, u) + (unsigned)c.wave * 4096;
    stage_weights<false, 4, 0, 12, 3>(c,
      [&](auto ci, LAS unsigned char* dst) {
        constexpr int id = ci;
        dma16<0>(rd_, dst, src, (unsigned)(((128 * (id / 4) + 16 * (id % 4)) * DM + f2 * 64) * 2));
      },
      [&](auto ci, LAS unsigned char* chunk) {
        constexpr int id = ci;
        if constexpr (id % 4 == 0) {
#pragma unroll
          for (int u = 0; u < 4; ++u) W[id / 4][u] = stage_frag(chunk + rd[u]);
        }
      },
      [&](auto ci) { constexpr int id = ci; pin_a(W[id / 4][id % 4]); }, [](auto) {});
  }
  LAS float* gb = (LAS float*)c.lds;
  LAS unsigned char* R = c.lds + 4096;
  load_mod(c, 5 * DM, gb, false);
  wait_flags<8>(c, FL_ACT + FS * 16 * rep, 12, 0);
  bar();
  const int tau = rep & 1, g = c.lane >> 4, n = c.lane & 15;
  const unsigned l16 = (unsigned)c.lane * 16;
  const int col = 64 * f2 + 16 * c.wave + 4 * g;
  const f32x4 gbv = *(LAS f32x4*)(gb + col);
  rsrc_t ro = mk_rsrc(p.out, 2048 * DM * 2);
  const unsigned xo = (unsigned)(((tau * 16 + n) * DM + col) * 2);
  const unsigned x1o = (unsigned)((col >> 5) * 2048 + tau * 1024 + (((col >> 3) & 3) * 16 + n) * 16 + (g & 1) * 8);
  stamp(c, 100);
#pragma unroll 1
  for (int it = 0; it < 32; ++it) {
    const int s = ffn_seg(rep, it);
    const unsigned sa = OFF_ACT + (unsigned)s * ACT_SZ;
    doorbell<4, F2_DELAY>(c, FL_ACT + FS * 16 * rep, it + 1);
    u32x4 b[12];
    u32x2 xv;
    f32x4 acc[4];
    unsigned sp = 0;
    for (;;) {
#pragma unroll
      for (int i = 0; i < 12; ++i) b[i] = ld16<SC1>(c.ws, l16, sa + (unsigned)((4 * i + c.wave) * 2 + tau) * 1024);
      xv = ld8<SC1>(c.ws, x1o, OFF_X1 + (unsigned)s * HB_SZ);
      sb();
      unsigned m = 0;
      sfor<0, 48>([&](auto ic) {
        constexpr int i = ic >> 2, u = ic & 3;
        mfma_v<true, true, i == 0>(acc[u], W[i][u], b[i]);
        sb();
        if constexpr (u == 0) { m = umax4(m, b[i]); sb(); }
      });
      if (!__any(m == SENT)) break;
      wait_flags<4>(c, FL_ACT + FS * 16 * rep, 12, it + 1);
      __builtin_amdgcn_s_sleep(2);
      spin_check(c, sp);
    }
    mfma_fence();
    LAS unsigned char* Rb = R + (it & 1) * 16384;
#pragma unroll
    for (int u = 0; u < 4; ++u) *(LAS f32x4*)(Rb + c.wave * 4096 + u * 1024 + l16) = acc[u];
    bar();
    LAS unsigned char* rp = Rb + c.wave * 1024 + l16;
    const f32x4 v = (*(LAS f32x4*)rp + *(LAS f32x4*)(rp + 4096)) + (*(LAS f32x4*)(rp + 8192) + *(LAS f32x4*)(rp + 12288));
    const float o0 = bf_lo(xv.x) + rbf(gbv[0] * rbf(v[0])), o1 = bf_hi(xv.x) + rbf(gbv[1] * rbf(v[1]));
    const float o2 = bf_lo(xv.y) + rbf(gbv[2] * rbf(v[2])), o3 = bf_hi(xv.y) + rbf(gbv[3] * rbf(v[3]));
    st8<0>(ro, xo, (unsigned)s * HB_SZ, u32x2{pk(o0, o1), pk(o2, o3)});
    stamp(c, 104 + s);
  }
}

enum { M_PRE = 1, M_KV = 2, M_U = 4, M_REC = 8, M_Q = 16, M_PX = 32, M_F1 = 64, M_F2 = 128, M_ALL = 255 };

__global__ void __launch_bounds__(256, 1) mega_kernel(Params p) {
  extern __shared__ __attribute__((aligned(16))) unsigned char smem[];
  Ctx c;
  c.ws = mk_rsrc(p.ws, WS_BYTES);
  c.srd = u32x4{(unsigned)(uintptr_t)p.ws, (unsigned)((uintptr_t)p.ws >> 32) & 0xFFFFu, WS_BYTES, 0x00020000u};
  c.wsp = p.ws;
  c.tag = 0;
  c.lds = (LAS unsigned char*)smem;
  c.tid = threadIdx.x; c.lane = c.tid & 63; c.wave = __builtin_amdgcn_readfirstlane(c.tid >> 6);
  const int grp = blockIdx.x & 7, ix = blockIdx.x >> 3;
  stamp(c, 0);
  int role, h, rep, r;
  rep = grp - 4; r = ix;
  if (grp < 4) {
    if (ix < 16) { role = ix >> 3; h = grp * 8 + (ix & 7); }
    else if (ix < 26) { role = 6; h = grp * 10 + ix - 16; }
    else if (ix < 31) { role = 6; h = 40 + grp * 5 + ix - 26; }
    else { role = 2; h = 28 + grp; }
  } else {
    if (ix < 24) { role = 3 + (ix >> 3); h = rep * 8 + (ix & 7); }
    else if (ix < 31) { role = 2; h = rep * 7 + ix - 24; }
    else { role = 6; h = 60 + rep; }
  }
  if (role < 2) { for (int i = 0; i < HV_DELAY; ++i) __builtin_amdgcn_s_sleep(32); }
  if (role == 3) { for (int i = 0; i < KW_DELAY; ++i) __builtin_amdgcn_s_sleep(32); }
  if (role == 5) { for (int i = 0; i < UW_DELAY; ++i) __builtin_amdgcn_s_sleep(32); }
  if (role == 2) {
    for (int i = 0; i < Q_DELAY; ++i) __builtin_amdgcn_s_sleep(32);
  }
  if (role == 0) { if (p.mask & M_REC) role_H(p, c, h); }
  else if (role == 1) { if (p.mask & M_REC) role_V(p, c, h); }
  else if (role == 2) { if (p.mask & M_Q) role_Q(p, c, h); }
  else if (role < 6) {
    if (role == 5) { if (p.mask & M_U) role_U(p, c, h); }
    else { if (p.mask & M_KV) role_KV(p, c, h, role == 4); }
    if (r < 12) { if (p.mask & M_F1) role_F1(p, c, rep, r); }
    else if (r < 22) { if (p.mask & M_F2) role_F2(p, c, rep, r - 12); }
  } else {
    if (p.mask & M_PRE) role_pre(p, c, h, h < 40 ? h : -1);
    if (h < 40) { if (p.mask & M_REC) role_PZ(p, c, h); }
    else if (h < 60) { if (p.mask & M_PX) role_PX(p, c, h - 40); }
    else if (FFN_PREFETCH) {
      for (int i = 0; i < 24; ++i) __builtin_amdgcn_s_sleep(32);
#pragma unroll 1
      for (int m = 0; m < 3; ++m) {
        rsrc_t r = mk_rsrc(m == 0 ? p.wg : m == 1 ? p.wu : p.wd, DM * DFF * 2);
#pragma unroll 1
        for (unsigned o = (unsigned)(h - 60) * 49152u; o < DM * DFF * 2; o += 4 * 49152) {
          u32x4 t[12];
#pragma unroll
          for (int i = 0; i < 12; ++i) t[i] = ld16<0>(r, (unsigned)c.tid * 16u + i * 4096u, o);
#pragma unroll
          for (int i = 0; i < 12; ++i) asm volatile("" :: "v"(t[i]));
        }
      }
    }
  }
}

}

extern "C" void kernel_launch(void* const* d_in, const int* in_sizes, int n_in, void* d_out, int out_size, void* d_ws,
                              size_t ws_size, hipStream_t stream) {
  Params p;
  p.x = d_in[0]; p.mem0 = d_in[1]; p.Wq = d_in[2]; p.Wk = d_in[3]; p.Wv = d_in[4]; p.Wo = d_in[5];
  p.wg = d_in[8]; p.wu = d_in[9]; p.wd = d_in[10]; p.c = d_in[11]; p.wmod = d_in[12];
  void* wsa = (void*)(((uintptr_t)d_ws + ((2u << 20) - 1)) & ~(uintptr_t)((2u << 20) - 1));
  p.out = d_out; p.ws = wsa; p.mask = M_ALL; p.pad = 0;
  (void)hipFuncSetAttribute((const void*)mega_kernel, hipFuncAttributeMaxDynamicSharedMemorySize, LDS_BYTES);
  hipLaunchKernelGGL(mega_kernel, dim3(256), dim3(256), LDS_BYTES, stream, p);
}
```

```cpp
#include <hip/hip_runtime.h>
#include <hip/hip_bf16.h>
#include <type_traits>
#include <cstdint>

#define LAS __attribute__((address_space(3)))
typedef unsigned u32x2 __attribute__((ext_vector_type(2)));
typedef unsigned u32x4 __attribute__((ext_vector_type(4)));
typedef unsigned u32x8 __attribute__((ext_vector_type(8)));
typedef float f32x2 __attribute__((ext_vector_type(2)));
typedef float f32x4 __attribute__((ext_vector_type(4)));
typedef short s16x4 __attribute__((ext_vector_type(4)));
typedef __bf16 bf16x2 __attribute__((ext_vector_type(2)));
typedef __bf16 bf16x8 __attribute__((ext_vector_type(8)));
typedef __bf16 bf16x16 __attribute__((ext_vector_type(16)));
typedef __amdgpu_buffer_rsrc_t rsrc_t;

namespace {

constexpr int DM = 640, NH = 32, DH = 192, NSEG = 64, DFF = 1536;
constexpr unsigned SENT = 0xFFFFFFFFu;

constexpr unsigned FS = 128;
constexpr unsigned FL_ABORT = 0;
constexpr unsigned FL_EPOCH = 128;
constexpr unsigned OFF_MOD = 1024;
constexpr unsigned OFF_MODP = 57344;
constexpr unsigned FL_H = 16384;
constexpr unsigned FL_KM = 24576, FL_K = 28672, FL_V = 32768, FL_U = 36864, FL_PX = 40960;
constexpr unsigned FL_X1 = 45056;
constexpr unsigned FL_ACT = 49152;
constexpr unsigned FL_MP = OFF_MODP + 15360 + 1024;
constexpr unsigned OFF_DB = 81920;
constexpr unsigned FL_MD = OFF_DB + 4096;
constexpr unsigned FL_MD2 = OFF_DB + 8192 + 4096;
constexpr unsigned OFF_XR = 98304;
constexpr unsigned MEM_SZ = 10240, P_SZ = 20480;
constexpr unsigned L_MEM = 0, L_P = 64 * MEM_SZ, L_END = L_P + 64 * P_SZ;
constexpr unsigned OFF_CAL = OFF_XR + 2 * L_END;
constexpr unsigned OFF_M = OFF_CAL + 65536;
constexpr unsigned M_SZ = 40 * 16384;
constexpr unsigned OFF_HB = OFF_M + 64 * M_SZ;
constexpr unsigned HB_SZ = 40960;
constexpr unsigned OFF_KX = OFF_HB + 64 * HB_SZ;
constexpr unsigned KX_SZ = 12288;
constexpr unsigned OFF_VX = OFF_KX + 64 * 32 * KX_SZ;
constexpr unsigned OFF_U = OFF_VX + 64 * 32 * KX_SZ;
constexpr unsigned U_SZ = 40 * 32 * 1024;
constexpr unsigned OFF_KM = OFF_U + 64 * U_SZ;
constexpr unsigned OFF_PX = OFF_KM + 64 * 32 * 3072;
constexpr unsigned PX_SZ = 81920;
constexpr unsigned OFF_X1 = OFF_PX + 64 * PX_SZ;
constexpr unsigned OFF_ACT = OFF_X1 + 2048 * 640 * 2;
constexpr unsigned ACT_SZ = 48 * 2 * 1024;
constexpr unsigned OFF_DBG = OFF_ACT + 64 * ACT_SZ;
constexpr unsigned WS_BYTES = OFF_DBG + (1u << 20);
static_assert(WS_BYTES + (2u << 20) <= (256u << 20), "workspace");
static_assert(OFF_XR % 16384 == 0 && OFF_CAL % 16384 == 0 && OFF_M % 16384 == 0, "alignment");

constexpr int LDS_BYTES = 160 * 1024;
constexpr unsigned SPIN_CAP = 1u << 21;
#define PZ_SLEEP 14
#define PZ_GAPD 2
#define PZ_GAP 0
#define UST_AUX SC1
#define UST_VO ((unsigned)c.lane * 16)
#define H_SLEEP 12
#define MAP 1
#define REV 0
#define V_NW 4
#define V_NA 3
#define HV_DELAY 0
#define Q_DELAY 0
#define PZ_MODE 0
#define PZ_THR 16
#define PXT 0
#define PX_SLEEP 4
#define PX_NF 1
#define PX_DELAY 12
#define USE_BELL_P 1
#define USE_BELL_M 1
#define BELL_P_POS 5
#define BELL_P_GAP 3
#define BELL_M_POS 2
#define BELL_M_GAP 0
#define BELL_P_GAP0 0
#define H_D0 0
#define H_D1 8
#define H_D2 0
#define F1_DELAY 4
#define F2_DELAY 2
#define KM_BELL 0
#define KM_DELAY 0
#define TLBWARM 0
#define FFN_PREFETCH 1
#define Q_PREFETCH 0
#define U_EAGER 6
#define BELL_MODE 1
#define BELL_SLEEP 0
#define NBELL 4
#define EARLY_BELL 0
#define KW_DELAY 0
#define UW_DELAY 0
#define KV_AUX 16
#define U_AUX 16
#define WS_WARM 0
#define SPLIT_LAUNCH 0
#define BUMP (!SPLIT_LAUNCH)
#define FULL_FLAG 0
#define U_THROTTLE 0
#define V_SLEEP 16

struct Params {
  const void* x; const void* mem0; const void* Wq; const void* Wk; const void* Wv; const void* Wo;
  const void* wg; const void* wu; const void* wd; const void* c; const void* wmod;
  void* out; void* ws;
  unsigned mask; unsigned pad;
};

template <int I, int N, class F>
__device__ __forceinline__ void sfor(F&& f) {
  if constexpr (I < N) { f(std::integral_constant<int, I>{}); sfor<I + 1, N>(f); }
}
__device__ __forceinline__ rsrc_t mk_rsrc(const void* p, unsigned bytes) {
  return __builtin_amdgcn_make_buffer_rsrc(const_cast<void*>(p), 0, bytes, 0x00020000);
}
template <int AUX> __device__ __forceinline__ u32x4 ld16(rsrc_t r, unsigned vo, unsigned so) { return __builtin_amdgcn_raw_buffer_load_b128(r, vo, so, AUX); }
template <int AUX> __device__ __forceinline__ u32x2 ld8(rsrc_t r, unsigned vo, unsigned so) { return __builtin_amdgcn_raw_buffer_load_b64(r, vo, so, AUX); }
template <int AUX> __device__ __forceinline__ unsigned ld4(rsrc_t r, unsigned vo, unsigned so) { return __builtin_amdgcn_raw_buffer_load_b32(r, vo, so, AUX); }
template <int AUX> __device__ __forceinline__ void st16(rsrc_t r, unsigned vo, unsigned so, u32x4 v) { __builtin_amdgcn_raw_buffer_store_b128(v, r, vo, so, AUX); }
template <int AUX> __device__ __forceinline__ void st8(rsrc_t r, unsigned vo, unsigned so, u32x2 v) { __builtin_amdgcn_raw_buffer_store_b64(v, r, vo, so, AUX); }
template <int AUX> __device__ __forceinline__ void st4(rsrc_t r, unsigned vo, unsigned so, unsigned v) { __builtin_amdgcn_raw_buffer_store_b32(v, r, vo, so, AUX); }
constexpr int SC1 = 16;
template <int AUX> __device__ __forceinline__ void dma16(rsrc_t r, LAS unsigned char* dst, unsigned vo, unsigned so) {
  __builtin_amdgcn_raw_ptr_buffer_load_lds(r, (LAS void*)dst, 16, vo, so, 0, AUX);
}
template <int N> __device__ __forceinline__ void wait_vm() { asm volatile("s_waitcnt vmcnt(%0)" ::"n"(N) : "memory"); }
__device__ __forceinline__ void wait_lgkm() { asm volatile("s_waitcnt lgkmcnt(0)" ::: "memory"); }
__device__ __forceinline__ void bar() { asm volatile("s_waitcnt lgkmcnt(0)\n\ts_barrier" ::: "memory"); }

__device__ __forceinline__ unsigned pk(float a, float b) {
  f32x2 v = {a, b};
  return __builtin_bit_cast(unsigned, __builtin_convertvector(v, bf16x2));
}
__device__ __forceinline__ unsigned pks(float a, float b) { return min(pk(a, b), 0xFFFFFFFEu); }
__device__ __forceinline__ float bf_lo(unsigned u) { return __builtin_bit_cast(float, u << 16); }
__device__ __forceinline__ float bf_hi(unsigned u) { return __builtin_bit_cast(float, u & 0xFFFF0000u); }
__device__ __forceinline__ float rbf(float a) { return bf_lo(pk(a, a)); }
__device__ __forceinline__ float f_(unsigned u) { return __builtin_bit_cast(float, u); }
__device__ __forceinline__ unsigned u_(float f) { return __builtin_bit_cast(unsigned, f); }
__device__ __forceinline__ unsigned umax3(unsigned a, unsigned b, unsigned c) { return max(max(a, b), c); }
__device__ __forceinline__ unsigned umax4(unsigned m, u32x4 v) { return umax3(umax3(m, v.x, v.y), v.z, v.w); }

__device__ __forceinline__ float dot2(unsigned a, unsigned b, float c) {
  return __builtin_amdgcn_fdot2_f32_bf16(__builtin_bit_cast(bf16x2, a), __builtin_bit_cast(bf16x2, b), c, false);
}
__device__ __forceinline__ f32x4 mfma32(u32x4 a, u32x4 b, f32x4 c) {
  return __builtin_amdgcn_mfma_f32_16x16x32_bf16(__builtin_bit_cast(bf16x8, a), __builtin_bit_cast(bf16x8, b), c, 0, 0, 0);
}
__device__ __forceinline__ f32x4 mfma16(u32x2 a, u32x2 b, f32x4 c) {
  return __builtin_amdgcn_mfma_f32_16x16x16bf16_1k(__builtin_bit_cast(s16x4, a), __builtin_bit_cast(s16x4, b), c, 0, 0, 0);
}
__device__ __forceinline__ f32x4 smfma(u32x4 a, u32x8 b, f32x4 c, int idx) {
  return __builtin_amdgcn_smfmac_f32_16x16x64_bf16(__builtin_bit_cast(bf16x8, a), __builtin_bit_cast(bf16x16, b), c, idx, 0, 0);
}
__device__ __forceinline__ u32x2 tr8(LAS unsigned char* p) {
  return __builtin_bit_cast(u32x2, __builtin_amdgcn_ds_read_tr16_b64_v4i16((LAS s16x4*)p));
}
__device__ __forceinline__ u32x4 lds16(LAS unsigned char* p) { return *(LAS u32x4*)p; }
__device__ __forceinline__ u32x4 cat(u32x2 a, u32x2 b) { return u32x4{a.x, a.y, b.x, b.y}; }
__device__ __forceinline__ u32x8 cat(u32x4 a, u32x4 b) { return u32x8{a.x, a.y, a.z, a.w, b.x, b.y, b.z, b.w}; }
__device__ __forceinline__ float halfsum(float x, float y) {
  auto r = __builtin_amdgcn_permlane32_swap(u_(x), u_(y), false, false);
  return f_(r[0]) + f_(r[1]);
}
__device__ __forceinline__ float halfsum1(float x, float y) {
  auto r = __builtin_amdgcn_permlane32_swap(u_(x), u_(y), false, false);
  float o;
  asm("v_add_f32 %0, %1, %2" : "=v"(o) : "v"(r[0]), "v"(r[1]));
  return o;
}
template <int CTRL> __device__ __forceinline__ float dpp(float x) {
  return f_((unsigned)__builtin_amdgcn_update_dpp(0, (int)u_(x), CTRL, 0xf, 0xf, false));
}

template <class T> __device__ __forceinline__ void pin_a(T& v) { asm volatile("" : "+a"(v)); }
template <class T> __device__ __forceinline__ void pin_v(T& v) { asm volatile("" : "+v"(v)); }

__device__ __forceinline__ void sb() { __builtin_amdgcn_sched_barrier(0); }

struct Ctx {
  rsrc_t ws;
  u32x4 srd;
  unsigned tag;
  const void* wsp;
  LAS unsigned char* lds;
  int tid, lane, wave;
};

__device__ __forceinline__ void stamp(const Ctx&, int, int = 0) {}
__device__ __forceinline__ void stampc(const Ctx&, int, int = 0) {}
__device__ __forceinline__ void stampl(const Ctx&, int) {}
__device__ __forceinline__ void stampf(const Ctx&, int, int = 0) {}
#define CYC(x)
#define CYCW(x)
#define CYCOUT(...)
__device__ __forceinline__ void spin_check(const Ctx& c, unsigned& n) {
  unsigned ts, tv;
  ++n;
  asm volatile(
      "s_and_b32 %0, %2, 0x3ff\n\t"
      "s_cbranch_scc1 1f\n\t"
      "buffer_load_dword %1, off, %3, 0 sc1\n\t"
      "s_waitcnt vmcnt(0)\n\t"
      "v_cmp_eq_u32 vcc, %1, %5\n\t"
      "s_nop 4\n\t"
      "s_cbranch_vccnz 2f\n\t"
      "s_cmp_lt_u32 %2, %4\n\t"
      "s_cbranch_scc1 1f\n\t"
      "buffer_store_dword %5, off, %3, 0 sc1\n\t"
      "s_waitcnt vmcnt(0)\n"
      "2:\n\t"
      "s_endpgm\n"
      "1:"
      : "=&s"(ts), "=&v"(tv)
      : "s"(n), "s"(c.srd), "s"(SPIN_CAP), "v"(c.tag)
      : "memory", "scc", "vcc");
}
template <class T> __device__ __forceinline__ void pin_s(T& v) { asm volatile("" : "+s"(v)); }
__device__ __forceinline__ void load_tag(Ctx& c) {
  unsigned e;
  asm volatile("s_load_dword %0, %1, %2\n\ts_waitcnt lgkmcnt(0)" : "=s"(e) : "s"(c.wsp), "i"(FL_EPOCH) : "memory");
  c.tag = e ^ 0x9E3779B9u;
}
__device__ __forceinline__ u32x2 ld_flag_raw(const Ctx& c, unsigned vo) { return ld8<SC1>(c.ws, vo, 0); }
__device__ __forceinline__ int flag_val(const Ctx& c, u32x2 r) { return r.y == c.tag ? (int)r.x : -1; }
__device__ __forceinline__ int ld_flag(const Ctx& c, unsigned vo) { return flag_val(c, ld_flag_raw(c, vo)); }
__device__ __forceinline__ void st_flag(const Ctx& c, unsigned fl, int v) { st8<SC1>(c.ws, fl, 0, u32x2{(unsigned)v, c.tag}); }
#define FLAG_THREADS 1
template <int SLEEP>
__device__ __forceinline__ int wait_flags(const Ctx& c, unsigned fl, int n, int need) {
  unsigned sp = 0;
  const unsigned vo = fl + FS * (unsigned)min(c.lane, n - 1);
  for (;;) {
    const int v = ld_flag(c, vo);
    if (!__any(v < need)) {
      int got = need;
      if (!__any(v < need + 2)) got = need + 2;
      if (!__any(v < need + 6)) got = need + 6;
      if (!__any(v < need + 16)) got = need + 16;
      return got;
    }
    __builtin_amdgcn_s_sleep(SLEEP);
    spin_check(c, sp);
  }
}
template <int SLEEP>
__device__ __forceinline__ int wait_flags_blk(const Ctx& c, unsigned fl, int n, int need, LAS int* w) {
  if (c.wave == 0) { const int r = wait_flags<SLEEP>(c, fl, n, need); if (c.lane == 0) *w = r; }
  bar();
  const int r = __builtin_amdgcn_readfirstlane(*w);
  bar();
  return r;
}
__device__ __forceinline__ void publish(const Ctx& c, unsigned fl, int val) {
  wait_vm<0>();
  bar();
  if (c.tid < FLAG_THREADS) st_flag(c, fl, val);
}

template <int SLEEP, int DELAY>
__device__ __forceinline__ int doorbell(const Ctx& c, unsigned fl, int need) {
  unsigned sp = 0;
  int v;
  while ((v = __builtin_amdgcn_readfirstlane(ld_flag(c, fl))) < need) { __builtin_amdgcn_s_sleep(SLEEP); spin_check(c, sp); }
  if (sp) __builtin_amdgcn_s_sleep(DELAY);
  return v;
}
__device__ __forceinline__ u32x4 nosent(u32x4 v) { return u32x4{min(v.x, 0xFFFFFFFEu), min(v.y, 0xFFFFFFFEu), min(v.z, 0xFFFFFFFEu), min(v.w, 0xFFFFFFFEu)}; }
__device__ __forceinline__ u32x2 nosent(u32x2 v) { return u32x2{min(v.x, 0xFFFFFFFEu), min(v.y, 0xFFFFFFFEu)}; }

__device__ __forceinline__ unsigned side_bit(unsigned b, unsigned side) { return b | (((side ^ (b >> 21)) & 1u) << 13); }
__device__ __forceinline__ unsigned xr_phys(unsigned o, unsigned side) { return side_bit(OFF_XR + ((o >> 13) << 14) + (o & 8191u), side); }
__device__ __forceinline__ unsigned m_phys(unsigned s, unsigned j, unsigned side) { return side_bit(OFF_M + (s * 40u + j) * 16384u, side); }
__device__ __forceinline__ unsigned my_iod() { return (((unsigned)__builtin_amdgcn_s_getreg(20 | (3 << 11))) >> 2) & 1u; }
struct Sides { unsigned me; unsigned long long light; };
__device__ __forceinline__ u32x2 ld_table(const Ctx& c) { return ld_flag_raw(c, FL_H + FS * (unsigned)c.lane); }
__device__ __forceinline__ void get_sides(const Ctx& c, Sides& o, unsigned v = SENT) {
  unsigned sp = 0;
  if (__any(v == SENT)) {
    for (;;) {
      v = (unsigned)flag_val(c, ld_table(c));
      if (!__any(v == SENT)) break;
      __builtin_amdgcn_s_sleep(8);
      spin_check(c, sp);
    }
  }
  const unsigned cc = __popcll(__ballot(v & 1u) >> 32) > 16 ? 1u : 0u;
  const unsigned long long iod = __ballot(v & 2u);
  o.me = cc ^ my_iod();
  o.light = cc ? ~iod : iod;
}
__device__ __forceinline__ unsigned pz_side(const Sides& sd, int j) { return (unsigned)(sd.light >> j) & 1u; }
__device__ __forceinline__ void ring(const Ctx& c, int bell, int val) {
  if (c.lane < 2) st4<SC1>(c.ws, OFF_DB + (unsigned)bell * FS + (unsigned)c.lane * 8192u, 0, (unsigned)val);
}
__device__ __forceinline__ void wait_bells(const Ctx& c, unsigned vo, int need, u32x2& keep) {
  unsigned sp = 0;
  if (c.wave == 0) {
    while (__any((int)ld4<SC1>(c.ws, vo, 0) < need)) { if (BELL_SLEEP) __builtin_amdgcn_s_sleep(BELL_SLEEP); spin_check(c, sp); }
  } else {
    wait_vm<0>();
  }
  __builtin_amdgcn_s_barrier();
}
__device__ __forceinline__ unsigned bell_vo(const Ctx& c, int bell0, unsigned side) { return side_bit(OFF_DB + (unsigned)(bell0 + (c.lane & (NBELL - 1))) * FS, side); }
__device__ __forceinline__ unsigned calibrate(const Ctx& c, int slot) {
  const unsigned base = OFF_CAL + (unsigned)slot * 256u;
  st16<SC1>(c.ws, base + (unsigned)(c.lane >> 3) * 8192u + (unsigned)(c.lane & 7) * 16u, 0, u32x4{0u, 0u, 0u, 0u});
  wait_vm<0>();
  unsigned best[2] = {~0u, ~0u};
#pragma unroll
  for (int i = 0; i < 8; ++i) {
    const unsigned long long t0 = __builtin_amdgcn_s_memtime();
    wait_lgkm();
    unsigned v = ld4<SC1>(c.ws, base, (unsigned)i * 8192u);
    asm volatile("s_waitcnt vmcnt(0)" : "+v"(v)::"memory");
    const unsigned long long t1 = __builtin_amdgcn_s_memtime();
    wait_lgkm();
    const unsigned par = (((OFF_CAL + i * 8192u) >> 13) ^ ((OFF_CAL + i * 8192u) >> 21)) & 1u;
    best[par] = min(best[par], (unsigned)(t1 - t0));
  }
  return best[1] < best[0] ? 1u : 0u;
}

template <int MODE> __device__ __forceinline__ int rowfn(int g, int j) {
  if (MODE == 0) return 8 * g + j;
  if (MODE == 1) return 16 * (g >> 1) + 8 * ((j >> 1) & 1) + 4 * (g & 1) + 2 * ((j >> 2) & 1) + (j & 1);
  if (MODE == 2) return 16 * (j >> 2) + 4 * g + (j & 3);
  return 64 * (j >> 2) + 4 * g + (j & 3);
}
template <int MODE> __device__ __forceinline__ unsigned stage_src(const Ctx& c, int ldw) {
  const int pos = c.lane >> 3, cp = c.lane & 7;
  const int u = (cp >> 1) ^ ((pos >> 1) & 3);
  const int ch = 2 * u + (cp & 1);
  const int g = 2 * (c.wave >> 1) + (pos >> 2), j = 4 * (c.wave & 1) + (pos & 3);
  return (unsigned)((rowfn<MODE>(g, j) * ldw + ch * 8) * 2);
}
__device__ __forceinline__ unsigned stage_rd(int lane, int u) {
  const int g = lane >> 4, q = (lane & 15) >> 2, p = lane & 3, pos = 4 * (g & 1) + q;
  return (unsigned)((g >> 1) * 2048 + pos * 128 + ((u ^ ((pos >> 1) & 3)) * 32) + p * 8);
}
__device__ __forceinline__ u32x4 stage_frag(LAS unsigned char* chunk_plus_rd) { return cat(tr8(chunk_plus_rd), tr8(chunk_plus_rd + 1024)); }

template <bool FIRST, int NB, int NRES, int BATCH, int NBUF, class Issue, class Consume, class Pin, class Post>
__device__ __forceinline__ void stage_weights(Ctx& c, Issue&& issue, Consume&& consume, Pin&& pin, Post&& post) {
  constexpr int SLOT = BATCH * 4096;
  sfor<0, (NBUF < NB ? NBUF : NB)>([&](auto b) {
    sfor<0, BATCH>([&](auto i) { issue(std::integral_constant<int, b * BATCH + i>{}, c.lds + b * SLOT + i * 4096 + c.wave * 1024); });
  });
  if (FIRST) load_tag(c);
  stampl(c, 300);
  sfor<0, NB - NRES>([&](auto b) {
    constexpr int inflight = ((b + NBUF < NB ? b + NBUF : NB) - (b + 1)) * BATCH;
    wait_vm<inflight>();
    bar();
    stampl(c, 301 + b);
    sfor<0, BATCH>([&](auto i) { consume(std::integral_constant<int, b * BATCH + i>{}, c.lds + (b % NBUF) * SLOT + i * 4096); });
    sb();
    sfor<0, BATCH>([&](auto i) { pin(std::integral_constant<int, b * BATCH + i>{}); });
    if constexpr (b + NBUF < NB) {
      bar();
      sfor<0, BATCH>([&](auto i) {
        issue(std::integral_constant<int, (b + NBUF) * BATCH + i>{}, c.lds + (b % NBUF) * SLOT + i * 4096 + c.wave * 1024);
      });
    }
    sb();
    sfor<0, BATCH>([&](auto i) { post(std::integral_constant<int, b * BATCH + i>{}); });
  });
  wait_vm<0>();
  bar();
}

__device__ __forceinline__ float ln_stats(const Ctx& c, const u32x4 (&raw)[10], f32x2 (&cv)[40], LAS float* red) {
  const int tau = c.wave & 1, kh = c.wave >> 1, g = c.lane >> 4, rho = c.lane & 15, row = tau * 16 + rho;
  float s0 = 0.f, s1 = 0.f;
  unsigned ones_u = 0x3F803F80u;
  pin_v(ones_u);
#pragma unroll
  for (int i = 0; i < 10; ++i) {
    s0 = dot2(raw[i].x, ones_u, s0); s1 = dot2(raw[i].y, ones_u, s1);
    s0 = dot2(raw[i].z, ones_u, s0); s1 = dot2(raw[i].w, ones_u, s1);
  }
  float s = s0 + s1;
  s += __shfl_xor(s, 16); s += __shfl_xor(s, 32);
  if (g == 0) red[kh * 32 + row] = s;
  bar();
  const float mean = (red[row] + red[32 + row]) * (1.f / DM);
  f32x2 v2 = {0.f, 0.f};
#pragma unroll
  for (int i = 0; i < 10; ++i)
#pragma unroll
    for (int d = 0; d < 4; ++d) {
      const f32x2 t = f32x2{bf_lo(raw[i][d]), bf_hi(raw[i][d])} - mean;
      cv[4 * i + d] = t;
      v2 += t * t;
    }
  float v = v2[0] + v2[1];
  v += __shfl_xor(v, 16); v += __shfl_xor(v, 32);
  if (g == 0) red[64 + kh * 32 + row] = v;
  bar();
  return 1.f / sqrtf((red[64 + row] + red[96 + row]) * (1.f / DM) + 1e-5f);
}
template <class Store>
__device__ __forceinline__ void ln_apply(const Ctx& c, const f32x2 (&cv)[40], float rstd, LAS float* shiftv, LAS float* scalev, Store&& store) {
  const int tau = c.wave & 1, kh = c.wave >> 1, g = c.lane >> 4;
#pragma unroll
  for (int i = 0; i < 10; ++i) {
    const int kap = kh * 10 + i, d0 = (4 * kap + g) * 8;
    const f32x4 sc0 = *(LAS f32x4*)(scalev + d0), sc1 = *(LAS f32x4*)(scalev + d0 + 4);
    const f32x4 sh0 = *(LAS f32x4*)(shiftv + d0), sh1 = *(LAS f32x4*)(shiftv + d0 + 4);
    const f32x2 r0 = (cv[4 * i + 0] * rstd) * f32x2{sc0[0], sc0[1]} + f32x2{sh0[0], sh0[1]};
    const f32x2 r1 = (cv[4 * i + 1] * rstd) * f32x2{sc0[2], sc0[3]} + f32x2{sh0[2], sh0[3]};
    const f32x2 r2 = (cv[4 * i + 2] * rstd) * f32x2{sc1[0], sc1[1]} + f32x2{sh1[0], sh1[1]};
    const f32x2 r3 = (cv[4 * i + 3] * rstd) * f32x2{sc1[2], sc1[3]} + f32x2{sh1[2], sh1[3]};
    store(kap * 2 + tau, u32x4{pk(r0[0], r0[1]), pk(r1[0], r1[1]), pk(r2[0], r2[1]), pk(r3[0], r3[1])});
  }
}
__device__ __forceinline__ unsigned md_flag(int pr) { return pr < 32 ? FL_MD + FS * (unsigned)pr : FL_MD2 + FS * (unsigned)(pr - 32); }
__device__ __forceinline__ void load_mod(const Ctx& c, int f0, LAS float* dst, bool add1) {
  unsigned sp = 0;
  const unsigned fo = md_flag((f0 - 1280) / 64 + min(c.lane, 9));
  while (__any(ld_flag(c, fo) < 0)) { __builtin_amdgcn_s_sleep(8); spin_check(c, sp); }
  const int cc = min(c.tid, 159);
  f32x4 f = __builtin_bit_cast(f32x4, ld16<SC1>(c.ws, OFF_MOD + (unsigned)(f0 * 4 + cc * 16), 0));
  if (add1) { f[0] = rbf(1.f + f[0]); f[1] = rbf(1.f + f[1]); f[2] = rbf(1.f + f[2]); f[3] = rbf(1.f + f[3]); }
  *(LAS f32x4*)(dst + cc * 4) = f;
}

__device__ __forceinline__ void role_pre(const Params& p, Ctx& c, int li, int jpz) {
  const unsigned iod = my_iod();
  unsigned tabw = iod << 1;
  if (jpz >= 0) {
    const u32x4 sv = {SENT, SENT, SENT, SENT};
#pragma unroll
    for (int i = 0; i < 8; ++i) st16<SC1>(c.ws, (unsigned)c.tid * 16u, OFF_M + (unsigned)((i >> 2) * 40 + jpz) * 16384u + (unsigned)(i & 3) * 4096u, sv);
    if (c.tid < 128) st16<SC1>(c.ws, xr_phys(L_P + (unsigned)jpz * 1024u + (unsigned)(c.tid & 63) * 16u, c.tid >> 6), 0, sv);
    else if (c.tid < 192) st16<SC1>(c.ws, xr_phys(L_MEM + (unsigned)(1 + ((c.tid >> 4) & 1)) * MEM_SZ + (unsigned)jpz * 256u + (unsigned)(c.tid & 15) * 16u, (c.tid >> 5) & 1), 0, sv);
    else if (c.tid >= 224) { if (jpz < NBELL) st16<SC1>(c.ws, OFF_DB + (unsigned)(jpz + 16 * ((c.tid >> 3) & 1)) * FS + (unsigned)((c.tid >> 4) & 1) * 8192u + (unsigned)(c.tid & 7) * 16u, 0, sv); }
    else if (c.tid < 224) {
      const u32x4 m0 = ld16<0>(mk_rsrc(p.mem0, 8 * DM * 2), (unsigned)((c.tid & 7) * DM * 2 + jpz * 32 + ((c.tid >> 3) & 1) * 16), 0);
      st16<SC1>(c.ws, xr_phys(L_MEM + (unsigned)jpz * 256u + (unsigned)(c.tid & 15) * 16u, (c.tid >> 4) & 1), 0, nosent(m0));
    }
  }
  LAS float* sc = (LAS float*)c.lds;
  LAS float* red = (LAS float*)(c.lds + 4096);
  rsrc_t rw = mk_rsrc(p.wmod, DM * 3840 * 2);
  const int cg = c.tid & 7, rg = c.tid >> 3;
  u32x4 w[20];
  const unsigned cv3 = ld4<0>(mk_rsrc(p.c, DM * 2), (unsigned)c.tid * 4u, 0), cv4 = ld4<0>(mk_rsrc(p.c, DM * 2), (unsigned)c.tid * 4u + 1024u, 0);
  const int r0 = (li / 20) * 224 + rg * 7;
  if (li < 60) {
#pragma unroll
    for (int i = 0; i < 7; ++i) w[i] = ld16<0>(rw, (unsigned)(((r0 + i) * 3840 + (li % 20) * 64 + cg * 8) * 2), 0);
  }
  rsrc_t rx = mk_rsrc(p.x, 2048 * DM * 2);
  u32x4 raw[10];
  {
    const int tau = c.wave & 1, kh = c.wave >> 1, g = c.lane >> 4, rho = c.lane & 15;
    const unsigned vo = (unsigned)(((li * 32 + tau * 16 + rho) * DM + (40 * kh + g) * 8) * 2);
#pragma unroll
    for (int i = 0; i < 10; ++i) raw[i] = ld16<0>(rx, vo + i * 64, 0);
  }
  sb();
  load_tag(c);
  stampl(c, 300);
  auto silu = [](float v) { return rbf(v / (1.f + __expf(-v))); };
  *(LAS f32x2*)(sc + 2 * c.tid) = f32x2{silu(bf_lo(cv3)), silu(bf_hi(cv3))};
  if (c.tid < 80) *(LAS f32x2*)(sc + 512 + 2 * c.tid) = f32x2{silu(bf_lo(cv4)), silu(bf_hi(cv4))};
  bar();
  stampl(c, 301);
  auto gemv = [&](auto nrow, int row0, unsigned dst, bool round, unsigned fl) {
    float a[8] = {};
#pragma unroll
    for (int i = 0; i < nrow; ++i) {
      const float sv = sc[row0 + i];
#pragma unroll
      for (int d = 0; d < 4; ++d) { a[2 * d] += sv * bf_lo(w[i][d]); a[2 * d + 1] += sv * bf_hi(w[i][d]); }
    }
    *(LAS f32x4*)(red + rg * 64 + cg * 8) = f32x4{a[0], a[1], a[2], a[3]};
    *(LAS f32x4*)(red + rg * 64 + cg * 8 + 4) = f32x4{a[4], a[5], a[6], a[7]};
    bar();
    float t = 0.f;
#pragma unroll
    for (int r = 0; r < 8; ++r) t += red[(c.wave * 8 + r) * 64 + c.lane];
    red[2048 + c.tid] = t;
    bar();
    if (c.tid < 64) {
      t = (red[2048 + c.tid] + red[2112 + c.tid]) + (red[2176 + c.tid] + red[2240 + c.tid]);
      st4<SC1>(c.ws, dst + (unsigned)c.tid * 4, 0, u_(round ? rbf(t) : t));
      wait_vm<0>();
      if (c.tid < FLAG_THREADS) st_flag(c, fl, 0);
    }
  };
  if (li < 60) gemv(std::integral_constant<int, 7>{}, r0, OFF_MODP + (unsigned)((li / 20) * 1280 + (li % 20) * 64) * 4, false, FL_MP + FS * li);
  stampl(c, 302);
  LAS float* shiftv = (LAS float*)(c.lds + 16384);
  LAS float* red2 = (LAS float*)(c.lds + 24576);
  f32x2 cv[40];
  const float rstd = ln_stats(c, raw, cv, red2);
  stampl(c, 303);
  wait_flags<4>(c, FL_MP, 60, 0);
#pragma unroll
  for (int ci = 0; ci < 2; ++ci) {
    const int cc = min(c.tid + 256 * ci, 319);
    const u32x4 v0 = ld16<SC1>(c.ws, (unsigned)cc * 16, OFF_MODP), v1 = ld16<SC1>(c.ws, (unsigned)cc * 16, OFF_MODP + 5120), v2 = ld16<SC1>(c.ws, (unsigned)cc * 16, OFF_MODP + 10240);
    f32x4 f = __builtin_bit_cast(f32x4, v0) + __builtin_bit_cast(f32x4, v1) + __builtin_bit_cast(f32x4, v2);
    const float one = cc >= 160 ? 1.f : 0.f;
#pragma unroll
    for (int d = 0; d < 4; ++d) f[d] = rbf(one + rbf(f[d]));
    *(LAS f32x4*)(shiftv + cc * 4) = f;
  }
  bar();
  stampl(c, 304);
  ln_apply(c, cv, rstd, shiftv, shiftv + DM, [&](int fr, u32x4 o) { st16<SC1>(c.ws, (unsigned)c.lane * 16 + (unsigned)fr * 1024, OFF_HB + (unsigned)li * HB_SZ, o); });
  stampl(c, 305);
  if (li >= 32 && c.wave == 0) tabw |= calibrate(c, li - 32) ^ iod;
  publish(c, FL_H + FS * li, (int)tabw);
  stamp(c, 1);
  if (li >= 24) {
#pragma unroll
    for (int i = 0; i < 20; ++i) w[i] = ld16<0>(rw, (unsigned)(((rg * 20 + i) * 3840 + 1280 + (li - 24) * 64 + cg * 8) * 2), 0);
    sb();
    gemv(std::integral_constant<int, 20>{}, rg * 20, OFF_MOD + (unsigned)(1280 + (li - 24) * 64) * 4, true, md_flag(li - 24));
  }
}

__device__ __forceinline__ int sp_row(int lane) { return ((lane >> 1) & 6) | (lane & 1); }
__device__ __forceinline__ int sp_half(int lane) { return (lane >> 1) & 1; }
__device__ __forceinline__ int sp_pos(int row, int half) { return 4 * (row >> 1) + 2 * half + (row & 1); }
__device__ __forceinline__ float add1(float a, float b) { float o; asm("v_add_f32 %0, %1, %2" : "=v"(o) : "v"(a), "v"(b)); return o; }
struct Gath { unsigned vo2, bvo; LAS unsigned char* w0; LAS unsigned char* w2; };
__device__ __forceinline__ Gath gather_setup(const Ctx& c, bool perm, unsigned side) {
  int r = c.lane & 7;
  if (perm) r = (r & 1) | ((r & 2) << 1) | ((r & 4) >> 1);
  const int wpos = ((c.lane & 48) + sp_pos(r, (c.lane >> 3) & 1)) * 16;
  Gath g;
  g.vo2 = c.wave < 2 ? (unsigned)c.lane * 16 : 0x80000000u;
  g.bvo = bell_vo(c, 16, side);
  g.w0 = c.lds + 2 * c.wave * 1024 + wpos;
  g.w2 = c.lds + (c.wave < 2 ? (8 + c.wave) * 1024 : 79872 + (c.wave - 2) * 1024) + wpos;
  return g;
}
__device__ __forceinline__ void gather_mem(const Ctx& c, const Gath& ga, int s, unsigned side, unsigned& nfail) {
  const unsigned lo = L_MEM + (unsigned)s * MEM_SZ;
  unsigned s0 = xr_phys(lo + c.wave * 2048, side), s2 = xr_phys(lo + 8192 + (c.wave & 1) * 1024, side);
  pin_s(s0); pin_s(s2);
  unsigned sp = 0;
  u32x2 keep = {0u, 0u};
  if (s > 0) wait_bells(c, ga.bvo, s, keep);
  if (BELL_M_GAP) __builtin_amdgcn_s_sleep(BELL_M_GAP);
  for (;;) {
    const u32x4 v0 = ld16<SC1>(c.ws, (unsigned)c.lane * 16, s0), v1 = ld16<SC1>(c.ws, (unsigned)c.lane * 16 + 1024, s0), v2 = ld16<SC1>(c.ws, ga.vo2, s2);
    sb();
    *(LAS u32x4*)(ga.w0) = v0;
    unsigned m = umax4(0u, v0);
    sb();
    *(LAS u32x4*)(ga.w0 + 1024) = v1;
    m = umax4(m, v1);
    sb();
    *(LAS u32x4*)(ga.w2) = v2;
    m = umax4(m, v2);
    if (!__any(m == SENT)) break;
    spin_check(c, sp);
  }
  asm volatile("" :: "v"(keep.x), "v"(keep.y));
  bar();
}
struct ResW { LAS unsigned char* lo; LAS unsigned char* hi; };
__device__ __forceinline__ ResW res_base(const Ctx& c) {
  ResW r; r.lo = c.lds + 81920 + stage_rd(c.lane, c.wave); r.hi = r.lo + 40960;
  pin_v(r.lo); pin_v(r.hi);
  return r;
}
__device__ __forceinline__ u32x8 res_frag(const ResW& w, int t) {
  LAS unsigned char* b = t < 5 ? w.lo + t * 8192 : w.hi + (t - 5) * 8192;
  return cat(stage_frag(b), stage_frag(b + 4096));
}
template <int CTRL> __device__ __forceinline__ unsigned dppu(unsigned x) { return (unsigned)__builtin_amdgcn_update_dpp(0, (int)x, CTRL, 0xf, 0xf, true); }
__device__ __forceinline__ float row16_max(float m) {
  asm volatile("s_nop 1\n\tv_max_f32_dpp %0, %0, %0 row_ror:8 row_mask:0xf bank_mask:0xf\n\t"
               "s_nop 1\n\tv_max_f32_dpp %0, %0, %0 row_ror:4 row_mask:0xf bank_mask:0xf\n\t"
               "s_nop 1\n\tv_max_f32_dpp %0, %0, %0 row_ror:2 row_mask:0xf bank_mask:0xf\n\t"
               "s_nop 1\n\tv_max_f32_dpp %0, %0, %0 row_ror:1 row_mask:0xf bank_mask:0xf" : "+v"(m));
  return m;
}
__device__ __forceinline__ float row16_sum(float m) {
  asm volatile("s_nop 1\n\tv_add_f32_dpp %0, %0, %0 row_ror:8 row_mask:0xf bank_mask:0xf\n\t"
               "s_nop 1\n\tv_add_f32_dpp %0, %0, %0 row_ror:4 row_mask:0xf bank_mask:0xf\n\t"
               "s_nop 1\n\tv_add_f32_dpp %0, %0, %0 row_ror:2 row_mask:0xf bank_mask:0xf\n\t"
               "s_nop 1\n\tv_add_f32_dpp %0, %0, %0 row_ror:1 row_mask:0xf bank_mask:0xf" : "+v"(m));
  return m;
}

constexpr int hmap(int id) { return (REV && id < 100) ? (9 - id / 10) * 10 + id % 10 : id; }
constexpr int vmap(int id) {
  if (!REV || id >= 100) return id;
  const int f = id / 2, a = id % 2;
  const int blk = f / 5, k = f % 5;
  return (k < 2 ? (19 - (2 * blk + k)) * 2 : 40 + (29 - (3 * blk + k - 2)) * 2) + a;
}
__device__ __forceinline__ void role_H(const Params& p, Ctx& c, int h) {
  u32x8 W[10][5];
  u32x2 tabv, kfl;
  const unsigned kmvo = (c.lane & 8) ? (unsigned)((c.lane >> 4) * 64 + (c.lane & 7) * 8 + c.wave * 256) : 0x80000000u;
#pragma unroll
  for (int i = 0; i < 6; ++i) st8<SC1>(c.ws, kmvo, OFF_KM + (unsigned)((i / 3) * NH + h) * 3072 + (unsigned)(i % 3) * 1024, u32x2{SENT, SENT});
  {
    rsrc_t rq = mk_rsrc(p.Wq, DM * NH * DH * 2), rk = mk_rsrc(p.Wk, DM * NH * DH * 2);
    const unsigned src = stage_src<1>(c, NH * DH), rd = stage_rd(c.lane, c.wave);
    stage_weights<true, 12, 2, 10, 4>(c,
      [&](auto ci, LAS unsigned char* dst) {
        constexpr int id = hmap(ci);
        if constexpr (id < 100) {
          constexpr int t = id / 10, pn = (id % 10) / 2, a = id % 2;
          constexpr int col = (pn < 3 ? pn : pn - 3) * 64;
          dma16<0>(pn < 3 ? rq : rk, dst, src, (unsigned)(((64 * t + 32 * a) * NH * DH + h * DH + col) * 2));
        } else {
          constexpr int sp = id - 100;
          dma16<0>(rk, dst, src, (unsigned)(((32 * sp) * NH * DH + h * DH + 128) * 2));
        }
      },
      [&](auto ci, LAS unsigned char* chunk) {
        constexpr int id = hmap(ci), t = id / 10, pn = (id % 10) / 2, a = id % 2;
        if constexpr (a == 1) {
          W[t][pn] = cat(stage_frag(chunk - 4096 + rd), stage_frag(chunk + rd));
        }
      },
      [&](auto ci) {
        constexpr int id = hmap(ci), t = id / 10, pn = (id % 10) / 2, a = id % 2;
        if constexpr (a == 1) { if constexpr (t * 5 + pn < 32) pin_a(W[t][pn]); else pin_v(W[t][pn]); }
      },
      [&](auto ci) { if constexpr (ci == 80) { tabv = ld_table(c); kfl = ld_flag_raw(c, FL_K + FS * h); } });
  }
  if (c.tid < FLAG_THREADS) st_flag(c, FL_KM + FS * h, 0);
  Sides sd;
  get_sides(c, sd, (unsigned)flag_val(c, tabv));
  LAS unsigned char* A = c.lds + c.lane * 16;
  const int g = c.lane >> 4, n = c.lane & 15;
  LAS unsigned char* TW = c.lds + 10240 + c.wave * 1920 + n * 40 + g * 4;
  LAS unsigned char* TR = c.lds + 10240 + c.wave * 1920 + (c.lane >> 2) * 40 + (c.lane & 3) * 8;
  LAS unsigned char* PART = c.lds + 49152;
  LAS unsigned char* PW = PART + c.wave * 3328 + n * 208 + g * 16;
  const ResW WR = res_base(c);
  const int idx = (c.lane & 2) ? 0xEEEE : 0x4444;
  const int q = (c.wave & 1) * 4 + g, it = min(n, 9);
  LAS unsigned char* PR = PART + q * 208 + (it < 8 ? it : it + 2) * 16;
  const float lmask = n < 10 ? 1.f : 0.f;
  const unsigned plo = it < 8 ? (unsigned)((h >> 1) * 1024 + (it >> 2) * 512 + (h & 1) * 256 + sp_pos(q, (it >> 1) & 1) * 16) : (unsigned)(16384 + h * 128 + q * 16);
  const bool ringer = USE_BELL_P && h % (32 / NBELL) == 0 && c.wave == 0;
  const Gath ga = gather_setup(c, false, sd.me);
  const unsigned rvo = (ringer && c.lane < 2) ? OFF_DB + (unsigned)(h / (32 / NBELL)) * FS + (unsigned)c.lane * 8192u : 0x80000000u;
  unsigned nfail = 0;
  LAS unsigned char* KX = c.lds + 24576;
  int kready = __builtin_amdgcn_readfirstlane(flag_val(c, kfl));
  auto dma_kx = [&](int s) {
    if (kready < s) kready = wait_flags<2>(c, FL_K + FS * h, 1, s);
#pragma unroll
    for (int i = 0; i < 6; ++i)
      dma16<SC1>(c.ws, KX + (s & 1) * KX_SZ + (c.wave * 6 + i) * 1024, (unsigned)c.lane * 16, OFF_KX + (unsigned)(s * NH + h) * KX_SZ + (unsigned)(c.wave * 6 + i) * 1024);
  };
  stamp(c, 1);
  if (c.wave < 2) dma_kx(0);
  for (int s = 0; s < NSEG; ++s) {
    unsigned pvo = xr_phys(L_P + (unsigned)s * P_SZ + plo, n & 1);
    if (n >= 10) pvo = 0x80000000u;
    f32x4 acc[6] = {};
    u32x8 wl[10];
    wl[0] = res_frag(WR, 0); wl[1] = res_frag(WR, 1);
#pragma unroll
    for (int i = 0; i < 6; ++i) pin_v(acc[i]);
    pin_v(wl[0]); pin_v(wl[1]); pin_v(pvo);
    gather_mem(c, ga, s, sd.me, nfail);
    stampf(c, 4 + 4 * s, 128);
    CYC(t0)
    u32x4 a[10], kx[3];
    a[0] = lds16(A); a[1] = lds16(A + 1024); a[2] = lds16(A + 2048);
    sb();
#pragma unroll
    for (int t = 0; t < 6; ++t) {
      LAS unsigned char* wb = t + 2 < 5 ? WR.lo + (t + 2) * 8192 : WR.hi + (t + 2 - 5) * 8192;
      acc[5] = smfma(a[t], wl[t], acc[5], idx); sb();
      a[t + 3] = lds16(A + (t + 3) * 1024); sb();
      acc[0] = smfma(a[t], W[t][0], acc[0], idx); sb();
      const u32x2 w0 = tr8(wb); sb();
      acc[1] = smfma(a[t], W[t][1], acc[1], idx); sb();
      const u32x2 w1 = tr8(wb + 1024); sb();
      acc[2] = smfma(a[t], W[t][2], acc[2], idx); sb();
      const u32x2 w2 = tr8(wb + 4096); sb();
      acc[3] = smfma(a[t], W[t][3], acc[3], idx); sb();
      const u32x2 w3 = tr8(wb + 5120); sb();
      acc[4] = smfma(a[t], W[t][4], acc[4], idx); sb();
      wl[t + 2] = cat(cat(w0, w1), cat(w2, w3));
    }
    CYC(t1)
    f32x4 sx0, sx1, sm;
    u32x2 F[3];
    float v[4];
#define H_S(i, t) acc[i] = smfma(a[t], W[t][i], acc[i], idx); sb();
#define H_L(t) acc[5] = smfma(a[t], wl[t], acc[5], idx); sb();
#define H_A(i, r) v[r] = add1(acc[(r >> 1) * 3 + i][r & 1], acc[(r >> 1) * 3 + i][2 + (r & 1)]); sb();
#define H_K(i) kx[i] = lds16(KX + (s & 1) * KX_SZ + (4 * i + c.wave) * 1024 + c.lane * 16); sb();
#define H_MZ(d, x, i) asm volatile("v_mfma_f32_16x16x16_bf16 %0, %1, %2, 0" : "=&v"(d) : "v"(x), "v"(F[i])); sb();
#define H_MA(d, x, i) asm volatile("v_mfma_f32_16x16x16_bf16 %0, %1, %2, %0" : "+v"(d) : "v"(x), "v"(F[i])); sb();
#define KLO(i) (u32x2{kx[i].x, kx[i].y})
#define KHI(i) (u32x2{kx[i].z, kx[i].w})
#define H_C(r) pw[r] = pk(v[2 * r], v[2 * r + 1]); sb();
#define H_D(i) *(LAS unsigned*)(TW + i * 640) = pw[0]; *(LAS unsigned*)(TW + i * 640 + 16) = pw[1]; F[i] = tr8(TR + i * 640); sb();
    unsigned pw[2];
    {
      LAS unsigned char* wb = WR.hi + 3 * 8192;
      H_S(1, 6) a[9] = lds16(A + 9 * 1024); const u32x2 w0 = tr8(wb); sb(); H_S(4, 6) const u32x2 w1 = tr8(wb + 1024); sb();
      H_S(1, 7) const u32x2 w2 = tr8(wb + 4096); sb(); H_S(4, 7) const u32x2 w3 = tr8(wb + 5120); sb();
      wl[8] = cat(cat(w0, w1), cat(w2, w3));
      H_S(1, 8) H_S(4, 8) H_S(1, 9) H_S(4, 9)
      H_L(6) const u32x2 w4 = tr8(wb + 8192), w5 = tr8(wb + 9216); sb(); H_S(2, 6) const u32x2 w6 = tr8(wb + 12288), w7 = tr8(wb + 13312); sb();
      wl[9] = cat(cat(w4, w5), cat(w6, w7));
    }
    H_L(7) H_A(1, 0) H_K(1) H_S(2, 7) H_A(1, 1) H_L(8) H_A(1, 2) H_S(2, 8) H_A(1, 3) H_S(2, 9) H_C(0) H_K(2) H_L(9) H_C(1)
    H_S(0, 6) H_D(1) H_S(3, 6) H_K(0) H_S(0, 7) H_A(2, 0) H_S(3, 7) H_A(2, 1) H_S(0, 8) H_A(2, 2) H_S(3, 8) H_A(2, 3) H_S(0, 9) H_C(0) H_S(3, 9) H_C(1) H_D(2)
    H_MZ(sx0, KLO(1), 1)
    stampf(c, 5 + 4 * s, 128);
    CYC(t2)
    asm volatile("s_nop 1" : "+v"(acc[0]));
    H_A(0, 0) H_A(0, 1) H_A(0, 2) H_A(0, 3) H_C(0) H_C(1) H_D(0) H_MZ(sx1, KHI(1), 1)
    if (BELL_P_POS == 5) { st4<SC1>(c.ws, rvo, 0, (unsigned)s); sb(); }
    H_MZ(sm, F[1], 1)
    H_MA(sx0, KLO(2), 2) H_MA(sx1, KHI(2), 2) H_MA(sm, F[2], 2)
    H_MA(sx0, KLO(0), 0) H_MA(sx1, KHI(0), 0) H_MA(sm, F[0], 0)
    asm volatile("s_nop 5" : "+v"(sx0), "+v"(sx1));
    CYC(t3)
    if (s < NSEG - 1) {
      *(LAS f32x4*)(PW) = sx0; sb(); *(LAS f32x4*)(PW + 64) = sx1; sb(); *(LAS f32x4*)(PW + 128) = sm;
      bar();
      CYC(t4)
      if (__builtin_expect(c.wave >= 2, 1)) {
        const f32x4 p0 = *(LAS f32x4*)PR, p1 = *(LAS f32x4*)(PR + 3328), p2 = *(LAS f32x4*)(PR + 6656), p3 = *(LAS f32x4*)(PR + 9984);
        const f32x4 xa = p0 + p1, xb = p2 + p3;
        CYC(t4a)
        u32x4 o;
        float x0, x1, x2, x3, ml, mm, t, d;
        asm volatile(
            "v_add_f32 %4, %12, %16\n\tv_add_f32 %5, %13, %17\n\tv_add_f32 %6, %14, %18\n\tv_add_f32 %7, %15, %19\n\t"
            "v_max3_f32 %8, %4, %5, %6\n\tv_max_f32 %8, %8, %7\n\t"
            "v_mul_f32 %10, %20, %8\n\tv_fma_f32 %4, %4, %20, -%10\n\t"
            "v_max_f32_dpp %9, %8, %8 row_ror:8 row_mask:0xf bank_mask:0xf\n\t"
            "v_fma_f32 %5, %5, %20, -%10\n\tv_fma_f32 %6, %6, %20, -%10\n\t"
            "v_max_f32_dpp %9, %9, %9 row_ror:4 row_mask:0xf bank_mask:0xf\n\t"
            "v_fma_f32 %7, %7, %20, -%10\n\tv_exp_f32 %4, %4\n\t"
            "v_max_f32_dpp %9, %9, %9 row_ror:2 row_mask:0xf bank_mask:0xf\n\t"
            "v_exp_f32 %5, %5\n\tv_exp_f32 %6, %6\n\t"
            "v_max_f32_dpp %9, %9, %9 row_ror:1 row_mask:0xf bank_mask:0xf\n\t"
            "v_exp_f32 %7, %7\n\t"
            "v_fma_f32 %11, %9, -%20, %10\n\t"
            "v_add_f32 %8, %4, %5\n\t"
            "v_exp_f32 %11, %11\n\t"
            "v_add_f32 %9, %6, %7\n\t"
            "v_add_f32 %8, %8, %9\n\t"
            "v_mul_f32 %10, %11, %21\n\t"
            "v_mul_f32 %8, %8, %10\n\t"
            "v_mul_f32 %4, %4, %11\n\tv_mul_f32 %5, %5, %11\n\t"
            "v_add_f32_dpp %8, %8, %8 row_ror:8 row_mask:0xf bank_mask:0xf\n\t"
            "v_mul_f32 %6, %6, %11\n\tv_mul_f32 %7, %7, %11\n\t"
            "v_add_f32_dpp %8, %8, %8 row_ror:4 row_mask:0xf bank_mask:0xf\n\t"
            "s_nop 1\n\t"
            "v_add_f32_dpp %8, %8, %8 row_ror:2 row_mask:0xf bank_mask:0xf\n\t"
            "s_nop 1\n\t"
            "v_add_f32_dpp %8, %8, %8 row_ror:1 row_mask:0xf bank_mask:0xf\n\t"
            "v_rcp_f32 %8, %8\n\t"
            "s_nop 0\n\t"
            "v_mul_f32 %4, %4, %8\n\tv_mul_f32 %5, %5, %8\n\tv_mul_f32 %6, %6, %8\n\tv_mul_f32 %7, %7, %8\n\t"
            "v_cvt_pk_bf16_f32 %9, %4, %5\n\tv_cvt_pk_bf16_f32 %10, %6, %7\n\t"
            "s_nop 0\n\t"
            "v_min_u32_dpp %0, %9, %22 quad_perm:[0,0,2,2] row_mask:0xf bank_mask:0xf\n\t"
            "v_min_u32_dpp %2, %9, %22 quad_perm:[1,1,3,3] row_mask:0xf bank_mask:0xf\n\t"
            "v_min_u32_dpp %1, %10, %22 quad_perm:[0,0,2,2] row_mask:0xf bank_mask:0xf\n\t"
            "v_min_u32_dpp %3, %10, %22 quad_perm:[1,1,3,3] row_mask:0xf bank_mask:0xf"
            : "=&v"(o.x), "=&v"(o.y), "=&v"(o.z), "=&v"(o.w), "=&v"(x0), "=&v"(x1), "=&v"(x2), "=&v"(x3), "=&v"(ml), "=&v"(mm), "=&v"(t), "=&v"(d)
            : "v"(xa[0]), "v"(xa[1]), "v"(xa[2]), "v"(xa[3]), "v"(xb[0]), "v"(xb[1]), "v"(xb[2]), "v"(xb[3]),
              "s"(0.07216878364870322f * 1.4426950408889634f), "v"(lmask), "v"(0xFFFFFFFEu));
        CYC(t4b)
        st16<SC1>(c.ws, pvo, 0, o);
        stampf(c, 6 + 4 * s, 128);
        CYC(t5)
        if (H_D2) __builtin_amdgcn_s_sleep(H_D2);
        if (n < 10 && s + 2 < NSEG - 1) st16<SC1>(c.ws, xr_phys(L_P + (unsigned)(s + 2) * P_SZ + plo, n & 1), 0, u32x4{SENT, SENT, SENT, SENT});
      } else {
            if (H_D0) __builtin_amdgcn_s_sleep(H_D0);
        dma_kx(s + 1);
        if (H_D1) __builtin_amdgcn_s_sleep(H_D1);
      }
    }
#pragma unroll
    for (int i = 0; i < 3; ++i) st8<SC1>(c.ws, kmvo, OFF_KM + (unsigned)(s * NH + h) * 3072 + (unsigned)i * 1024, nosent(F[i]));
    if (KM_BELL && c.tid < FLAG_THREADS) st_flag(c, FL_KM + FS * h, s + 1);
    if (s + 2 < NSEG) {
#pragma unroll
      for (int i = 0; i < 3; ++i) st8<SC1>(c.ws, kmvo, OFF_KM + (unsigned)((s + 2) * NH + h) * 3072 + (unsigned)i * 1024, u32x2{SENT, SENT});
    }
    __builtin_amdgcn_s_sleep(H_SLEEP);
  }
}

__device__ __forceinline__ void role_V(const Params& p, Ctx& c, int h) {
  u32x8 Wv[10][2];
  u32x8 Wo[10][3];
  u32x2 tabv;
  {
    rsrc_t rv = mk_rsrc(p.Wv, DM * NH * DH * 2), ro = mk_rsrc(p.Wo, DM * NH * DH * 2);
    const unsigned srcv = stage_src<1>(c, NH * DH), srco = stage_src<1>(c, DM), rd = stage_rd(c.lane, c.wave);
    stage_weights<true, 12, 2, 10, 4>(c,
      [&](auto ci, LAS unsigned char* dst) {
        constexpr int id = vmap(ci);
        if constexpr (id < 40) {
          constexpr int t = id / 4, pn = (id % 4) / 2, a = id % 2;
          dma16<0>(rv, dst, srcv, (unsigned)(((64 * t + 32 * a) * NH * DH + h * DH + pn * 64) * 2));
        } else if constexpr (id < 100) {
          constexpr int f = (id - 40) / 2, a = id % 2, t = f / 10, pn = f % 10;
          dma16<0>(ro, dst, srco, (unsigned)(((h * DH + 64 * t + 32 * a) * DM + pn * 64) * 2));
        } else {
          constexpr int sp = id - 100;
          dma16<0>(rv, dst, srcv, (unsigned)(((32 * sp) * NH * DH + h * DH + 128) * 2));
        }
      },
      [&](auto ci, LAS unsigned char* chunk) {
        constexpr int id = vmap(ci);
        if constexpr (id % 2 == 1) {
          u32x8 f = cat(stage_frag(chunk - 4096 + rd), stage_frag(chunk + rd));
          if constexpr (id < 40) Wv[id / 4][(id % 4) / 2] = f;
          else Wo[((id - 40) / 2) % 10][((id - 40) / 2) / 10] = f;
        }
      },
      [&](auto ci) {
        constexpr int id = vmap(ci);
        if constexpr (id % 2 == 1) {
          if constexpr (id < 36) pin_v(Wv[id / 4][(id % 4) / 2]);
          else if constexpr (id < 40) pin_a(Wv[id / 4][(id % 4) / 2]);
          else pin_a(Wo[((id - 40) / 2) % 10][((id - 40) / 2) / 10]);
        }
      },
      [&](auto ci) { if constexpr (ci == 80) tabv = ld_table(c); });
  }
  Sides sd;
  get_sides(c, sd, (unsigned)flag_val(c, tabv));
  LAS unsigned char* A = c.lds + c.lane * 16;
  const ResW WR = res_base(c);
  const int idx = (c.lane & 2) ? 0xEEEE : 0x4444;
  const int g = c.lane >> 4, n = c.lane & 15;
  LAS unsigned char* TW = c.lds + 10240 + c.wave * 256 + ((n & 7) * 4 + g) * 8 + (n >> 3) * 4;
  LAS unsigned char* TR = c.lds + 10240 + g * 256 + n * 8;
  unsigned nfail = 0;
  const Gath ga = gather_setup(c, true, sd.me);
  unsigned mj[3], ms[3];
#pragma unroll
  for (int u = 0; u < 3; ++u) {
    const int pn = u < 2 ? 4 * u + g : 8 + (g & 1);
    mj[u] = (unsigned)(4 * pn + c.wave);
    ms[u] = pz_side(sd, (int)mj[u]);
  }
  stamp(c, 1);
  for (int s = 0; s < NSEG; ++s) {
    unsigned mvo[3];
#pragma unroll
    for (int u = 0; u < 3; ++u) {
      mvo[u] = m_phys((unsigned)s, mj[u], ms[u]) + (unsigned)(h * 256 + n * 16);
      if (u == 2 && g >= 2) mvo[u] = 0x80000000u;
      pin_v(mvo[u]);
    }
    f32x4 acc[3] = {};
    constexpr int NW = V_NW, NA = V_NA;
    u32x8 wl[NW];
#pragma unroll
    for (int i = 0; i < NW; ++i) wl[i] = res_frag(WR, i);
#pragma unroll
    for (int i = 0; i < NW; ++i) pin_v(wl[i]);
#pragma unroll
    for (int i = 0; i < 3; ++i) pin_v(acc[i]);
    gather_mem(c, ga, s, sd.me, nfail);
    stampf(c, 4 + 4 * s);
    CYCW(u0)
    u32x4 a[NA];
#pragma unroll
    for (int t = 0; t < NA; ++t) a[t] = lds16(A + t * 1024);
    sb();
#pragma unroll
    for (int t = 0; t < 10; ++t) {
      LAS unsigned char* wb = t + NW < 5 ? WR.lo + (t + NW) * 8192 : WR.hi + (t + NW - 5) * 8192;
      acc[2] = smfma(a[t % NA], wl[t % NW], acc[2], idx);
      sb();
      if (t + NW < 10) {
        const u32x2 w0 = tr8(wb), w1 = tr8(wb + 1024);
        wl[t % NW][0] = w0.x; wl[t % NW][1] = w0.y; wl[t % NW][2] = w1.x; wl[t % NW][3] = w1.y;
      }
      sb();
      acc[0] = smfma(a[t % NA], Wv[t][0], acc[0], idx);
      sb();
      if (t + NW < 10) {
        const u32x2 w2 = tr8(wb + 4096), w3 = tr8(wb + 5120);
        wl[t % NW][4] = w2.x; wl[t % NW][5] = w2.y; wl[t % NW][6] = w3.x; wl[t % NW][7] = w3.y;
      }
      sb();
      acc[1] = smfma(a[t % NA], Wv[t][1], acc[1], idx);
      sb();
      if (t + NA < 10) a[t % NA] = lds16(A + (t + NA) * 1024);
      sb();
    }
    CYCW(u1)
    f32x4 m[10] = {};
    *(LAS unsigned*)(TW + 2048) = pk(acc[2][0] + acc[2][2], acc[2][1] + acc[2][3]);
    *(LAS unsigned*)(TW) = pk(acc[0][0] + acc[0][2], acc[0][1] + acc[0][3]);
    *(LAS unsigned*)(TW + 1024) = pk(acc[1][0] + acc[1][2], acc[1][1] + acc[1][3]);
    sb();
#pragma unroll
    for (int i = 0; i < 4; ++i) pin_v(m[i]);
    bar();
    CYCW(u2)
    u32x4 a2[3];
#pragma unroll
    for (int t = 0; t < 3; ++t) a2[t] = cat(tr8(TR + t * 1024), tr8(TR + t * 1024 + 128));
    sb();
#pragma unroll
    for (int i = 4; i < 10; ++i) pin_v(m[i]);
    sb();
    float v0, v1; unsigned x[10];
    auto xpose = [&](int u) {
      const int i2 = u < 2 ? 4 * u + 2 : 8, i3 = u < 2 ? 4 * u + 3 : 9;
      auto r0 = __builtin_amdgcn_permlane32_swap(x[4 * u], x[i2], false, false);
      auto r1 = __builtin_amdgcn_permlane32_swap(x[4 * u + 1], x[i3], false, false);
      auto q0 = __builtin_amdgcn_permlane16_swap(r0[0], r1[0], false, false);
      auto q1 = __builtin_amdgcn_permlane16_swap(r0[1], r1[1], false, false);
      st16<SC1>(c.ws, mvo[u], 0, u32x4{q0[0], q0[1], q1[0], q1[1]});
    };
    sfor<0, 36>([&](auto ic) {
      constexpr int i = ic, tl = (i / 6) * 2 + (i & 1), t = (i % 6) / 2;
      if constexpr (i < 30) { m[tl] = smfma(a2[t], Wo[tl][t], m[tl], idx); sb(); }
      else if constexpr (i == 30) { asm volatile("s_nop 3" : "+v"(m[8]), "+v"(m[9])); sb(); }
      if constexpr (i >= 8 || (i >= 6 && false)) {
        constexpr int k = i - 8, pr = k / 6, w = k % 6;
        (void)pr; (void)w;
      }
      constexpr int k = i - 7;
      if constexpr (k >= 0) {
        constexpr int pr = k / 6, w = k % 6, ta = 2 * pr + (w >= 3 ? 1 : 0), ww = w % 3;
        if constexpr (pr < 5) {
          if constexpr (ww == 0) { v0 = add1(m[ta][0], m[ta][2]); sb(); }
          if constexpr (ww == 1) { v1 = add1(m[ta][1], m[ta][3]); sb(); }
          if constexpr (ww == 2) { x[ta] = pks(v0, v1); sb(); }
          if constexpr (w == 5 && (pr & 1)) { xpose(pr >> 1); sb(); }
        }
      }
    });
    x[9] = pks(v0, v1);
    xpose(2);
    CYCW(u3)
    CYCOUT(s, u0, u1, u2, u3, u3, u3, u3)
    stampf(c, 6 + 4 * s);
    __builtin_amdgcn_s_sleep(V_SLEEP);
  }
}

__device__ __forceinline__ void role_PZ(const Params& p, Ctx& c, int j) {
  Sides sd;
  get_sides(c, sd);
  const unsigned mside = pz_side(sd, j);
  LAS unsigned char* R = c.lds;
  const int idx = (c.lane & 2) ? 0xEEEE : 0x4444;
  const int g = c.lane >> 4, n = c.lane & 15;
  int uready = -1;
  const unsigned l16 = (unsigned)c.lane * 16;
  const unsigned pmvo = (unsigned)(2 * g * 128 + sp_row(c.lane) * 16 + sp_half(c.lane) * 8);
  const u32x4 sv = {SENT, SENT, SENT, SENT};
  LAS float* rw = (LAS float*)(R + c.wave * 512) + 2 * g * 16 + n;
  LAS unsigned char* rp = R + (((c.lane >> 2) & 7) * 16 + (c.lane >> 5) * 8 + (c.lane & 3) * 2) * 4;
  const unsigned olo = (unsigned)(j * 256 + (c.lane >> 2) * 16);
  const bool ringer = USE_BELL_M && j % (40 / NBELL) == 0 && j / (40 / NBELL) < NBELL && c.wave == 3;
  const unsigned bvo = bell_vo(c, 0, sd.me);
  const unsigned rvo = (ringer && c.lane < 2) ? OFF_DB + (unsigned)(16 + j / (40 / NBELL)) * FS + (unsigned)c.lane * 8192u : 0x80000000u;
  unsigned nfail = 0; (void)nfail;
  bar();
  for (int s = 0; s < NSEG - 1; ++s) {
    if (uready < s) uready = wait_flags<2>(c, FL_U + 8 * FS * c.wave, 8, s);
    u32x4 u[8];
#pragma unroll
    for (int i = 0; i < 8; ++i) u[i] = ld16<SC1>(c.ws, l16, OFF_U + (unsigned)s * U_SZ + (unsigned)(j * 32 + 8 * c.wave + i) * 1024);
    u32x4 pa[4], mb[2]; u32x2 pm[2];
    const unsigned lp = L_P + (unsigned)s * P_SZ;
    unsigned sop = xr_phys(lp + (unsigned)c.wave * 4096, sd.me), som5 = xr_phys(lp + 16384 + (unsigned)c.wave * 1024, sd.me);
    pin_s(sop); pin_s(som5);
    unsigned som = m_phys((unsigned)s, (unsigned)j, mside) + (unsigned)c.wave * 2048;
    unsigned ovo = xr_phys(L_MEM + (unsigned)(s + 1) * MEM_SZ + olo, c.lane & 1);
    if ((c.lane & 2) || c.wave != 0) ovo = 0x80000000u;
    f32x4 acc;
    LAS float* rwb = rw + (s & 1) * 512;
    LAS unsigned char* rpb = rp + (s & 1) * 2048;
    pin_v(ovo); pin_v(rwb); pin_v(rpb);
    unsigned sp = 0;
    pin_s(som);
    u32x2 keep = {0u, 0u};
    wait_bells(c, bvo, s, keep);
    if (BELL_P_GAP0) __builtin_amdgcn_s_sleep(BELL_P_GAP0);
    mb[0] = ld16<SC1>(c.ws, l16, som); mb[1] = ld16<SC1>(c.ws, l16 + 1024, som);
    sb();
    if (BELL_P_GAP) __builtin_amdgcn_s_sleep(BELL_P_GAP);
    for (;;) {
      pm[0] = ld8<SC1>(c.ws, pmvo, som5); pm[1] = ld8<SC1>(c.ws, pmvo + 128, som5);
#pragma unroll
      for (int i = 0; i < 4; ++i) pa[i] = ld16<SC1>(c.ws, l16 + i * 1024, sop);
      sb();
      acc = f32x4{0.f, 0.f, 0.f, 0.f};
      pin_v(acc);
      acc = smfma(cat(pm[0], pm[1]), cat(mb[0], mb[1]), acc, idx);
      unsigned m = umax4(0u, cat(pm[0], pm[1]));
      m = umax4(umax4(m, mb[0]), mb[1]);
      sb();
#pragma unroll
      for (int i = 0; i < 4; ++i) {
        if (BELL_M_POS == 4 + i) { st4<SC1>(c.ws, rvo, 0, (unsigned)(s + 1)); sb(); }
        acc = smfma(pa[i], cat(u[2 * i], u[2 * i + 1]), acc, idx); m = umax4(m, pa[i]); sb();
      }
      if (!__any(m == SENT)) break;
      spin_check(c, sp);
      mb[0] = ld16<SC1>(c.ws, l16, som); mb[1] = ld16<SC1>(c.ws, l16 + 1024, som);
    }
    asm volatile("" :: "v"(keep.x), "v"(keep.y));
    stampf(c, 4 + 4 * s);
    rwb[0] = acc[0] + acc[2]; rwb[16] = acc[1] + acc[3];
    sb();
    if (BELL_M_POS == 2) st4<SC1>(c.ws, rvo, 0, (unsigned)(s + 1));
    bar();
    {
      const f32x2 v = (*(LAS f32x2*)(rpb) + *(LAS f32x2*)(rpb + 512)) + (*(LAS f32x2*)(rpb + 1024) + *(LAS f32x2*)(rpb + 1536));
      const unsigned d = pks(v[0], v[1]);
      st16<SC1>(c.ws, ovo, 0, u32x4{dppu<0x00>(d), dppu<0x55>(d), dppu<0xAA>(d), dppu<0xFF>(d)});
      stamp(c, 6 + 4 * s);
    }
    __builtin_amdgcn_s_sleep(PZ_SLEEP);
    if (c.wave == 0 && !(c.lane & 2) && s + 3 < NSEG) st16<SC1>(c.ws, xr_phys(L_MEM + (unsigned)(s + 3) * MEM_SZ + olo, c.lane & 1), 0, sv);
    if (s + 2 < NSEG) {
      const unsigned sa = m_phys((unsigned)(s + 2), (unsigned)j, mside) + (unsigned)c.wave * 2048;
      st16<SC1>(c.ws, l16, sa, sv); st16<SC1>(c.ws, l16 + 1024, sa, sv);
    }
  }
}

__device__ __forceinline__ void load_proj_w(Ctx& c, const void* Wm, int h, u32x4 (&W)[3][20]) {
  rsrc_t rw = mk_rsrc(Wm, DM * NH * DH * 2);
  const unsigned src = stage_src<0>(c, NH * DH), rd = stage_rd(c.lane, c.wave);
  stage_weights<true, 5, 0, 12, 3>(c,
    [&](auto ci, LAS unsigned char* dst) {
      constexpr int id = ci, kap = id / 3, pn = id % 3;
      dma16<0>(rw, dst, src, (unsigned)(((32 * kap) * NH * DH + h * DH + pn * 64) * 2));
    },
    [&](auto ci, LAS unsigned char* chunk) {
      constexpr int id = ci;
      W[id % 3][id / 3] = stage_frag(chunk + rd);
    },
    [&](auto ci) { constexpr int id = ci; pin_a(W[id % 3][id / 3]); }, [](auto) {});
}
__device__ __forceinline__ void dma_hb(const Ctx& c, int s, LAS unsigned char* slot) {
#pragma unroll
  for (int i = 0; i < 10; ++i)
    dma16<SC1>(c.ws, slot + (c.wave * 10 + i) * 1024, (unsigned)c.lane * 16, OFF_HB + (unsigned)s * HB_SZ + (unsigned)(c.wave * 10 + i) * 1024);
}
template <bool WA, bool WFIRST, bool ZERO>
__device__ __forceinline__ void mfma_v(f32x4& d, const u32x4& w, const u32x4& x) {
  if constexpr (ZERO) {
    if constexpr (WA && WFIRST) asm volatile("v_mfma_f32_16x16x32_bf16 %0, %1, %2, 0" : "=&v"(d) : "a"(w), "v"(x));
    else if constexpr (WA) asm volatile("v_mfma_f32_16x16x32_bf16 %0, %2, %1, 0" : "=&v"(d) : "a"(w), "v"(x));
    else if constexpr (WFIRST) asm volatile("v_mfma_f32_16x16x32_bf16 %0, %1, %2, 0" : "=&v"(d) : "v"(w), "v"(x));
    else asm volatile("v_mfma_f32_16x16x32_bf16 %0, %2, %1, 0" : "=&v"(d) : "v"(w), "v"(x));
  } else {
    if constexpr (WA && WFIRST) asm volatile("v_mfma_f32_16x16x32_bf16 %0, %1, %2, %0" : "+v"(d) : "a"(w), "v"(x));
    else if constexpr (WA) asm volatile("v_mfma_f32_16x16x32_bf16 %0, %2, %1, %0" : "+v"(d) : "a"(w), "v"(x));
    else if constexpr (WFIRST) asm volatile("v_mfma_f32_16x16x32_bf16 %0, %1, %2, %0" : "+v"(d) : "v"(w), "v"(x));
    else asm volatile("v_mfma_f32_16x16x32_bf16 %0, %2, %1, %0" : "+v"(d) : "v"(w), "v"(x));
  }
}
__device__ __forceinline__ void mfma_vn(f32x4& d, const u32x4& a, const u32x4& b) {
  asm volatile("s_nop 1\n\tv_mfma_f32_16x16x32_bf16 %0, %1, %2, %0" : "+v"(d) : "v"(a), "v"(b));
}
__device__ __forceinline__ void mfma_fence() { sb(); asm volatile("s_nop 7"); sb(); }

template <int NT, int K0, int K1, class Hook>
__device__ __forceinline__ void gemm_part(const u32x4 (&W)[NT][20], LAS unsigned char* hb, f32x4 (&acc)[NT][2], Hook&& hook) {
  u32x4 b[K1 - K0][2];
#pragma unroll
  for (int k = 0; k < 2; ++k) { b[k][0] = lds16(hb + (2 * (K0 + k)) * 1024); b[k][1] = lds16(hb + (2 * (K0 + k) + 1) * 1024); }
  sb();
  sfor<0, K1 - K0>([&](auto kc) {
    constexpr int k = kc, ka = K0 + k;
    sfor<0, 2 * NT>([&](auto ic) {
      constexpr int t = ic / NT, i = ic % NT;
      mfma_v<(ka * NT + i < 64), true, ka == 0>(acc[i][t], W[i][ka], b[k][t]);
      sb();
      if constexpr (ic < 2 && k + 2 < K1 - K0) { b[k + 2][ic] = lds16(hb + (2 * (ka + 2) + ic) * 1024); sb(); }
      if constexpr (ic == 2) { hook(kc); sb(); }
    });
  });
}
__device__ __forceinline__ void dma_hb1(const Ctx& c, int s, LAS unsigned char* slot, int i) {
  dma16<SC1>(c.ws, slot + (c.wave * 10 + i) * 1024, (unsigned)c.lane * 16, OFF_HB + (unsigned)s * HB_SZ + (unsigned)(c.wave * 10 + i) * 1024);
}
__device__ __forceinline__ u32x4 pack2(const f32x4& a, const f32x4& b) { return u32x4{pk(a[0], a[1]), pk(a[2], a[3]), pk(b[0], b[1]), pk(b[2], b[3])}; }

__device__ __forceinline__ void need_hb(const Ctx& c, unsigned long long& mask, int s) {
  if (c.wave != 0 || ((mask >> s) & 1ull)) return;
  unsigned sp = 0;
  for (;;) {
    mask = __ballot(ld_flag(c, FL_H + FS * (unsigned)c.lane) >= 0);
    if ((mask >> s) & 1ull) return;
    __builtin_amdgcn_s_sleep(4);
    spin_check(c, sp);
  }
}

__device__ __forceinline__ void role_KV(const Params& p, Ctx& c, int h, bool isV) {
  u32x4 W[3][20];
  load_proj_w(c, isV ? p.Wv : p.Wk, h, W);
  stamp(c, 1);
  unsigned long long hm = 0;
  need_hb(c, hm, 0);
  bar();
  stamp(c, 2);
  const unsigned obase = isV ? OFF_VX : OFF_KX, fl = (isV ? FL_V : FL_K) + FS * h;
  dma_hb(c, 0, c.lds);
  need_hb(c, hm, 1);
  bar();
  dma_hb(c, 1, c.lds + HB_SZ);
  auto kst = [&](int s, int i, const f32x4 (&a)[3][2]) {
    st16<KV_AUX>(c.ws, (unsigned)c.lane * 16, obase + (unsigned)(s * NH + h) * KX_SZ + (unsigned)(4 * i + c.wave) * 1024, pack2(a[i][0], a[i][1]));
  };
  f32x4 accA[3][2], accB[3][2];
#pragma unroll 1
  for (int s = 0; s < 2; ++s) {
    need_hb(c, hm, s + 2);
    if (s == 0) wait_vm<10>();
    bar();
    gemm_part<3, 0, 20>(W, c.lds + s * HB_SZ + c.lane * 16, accA, [&](auto k) { if constexpr (k < 10) dma_hb1(c, s + 2, c.lds + (s == 0 ? 2 : 0) * HB_SZ, k); });
    mfma_fence();
#pragma unroll
    for (int i = 0; i < 3; ++i) kst(s, i, accA);
    publish(c, fl, s);
    stamp(c, 4 + s);
  }
  int sl = 2;
  auto seg = [&](int s, f32x4 (&cur)[3][2], const f32x4 (&prev)[3][2], bool hasprev) {
    const int s2 = min(s + 2, NSEG - 1);
    need_hb(c, hm, s2);
    wait_vm<10>();
    bar();
    if (c.tid < FLAG_THREADS && s >= 4) st_flag(c, fl, s - 2);
    const int sl2 = sl == 0 ? 2 : sl - 1;
    gemm_part<3, 0, 20>(W, c.lds + sl * HB_SZ + c.lane * 16, cur, [&](auto k) {
      if constexpr (k >= 2 && k < 5) { if (hasprev) kst(s - 1, k - 2, prev); }
      if constexpr (k >= 6 && k < 16) dma_hb1(c, s2, c.lds + sl2 * HB_SZ, k - 6);
    });
    stamp(c, 4 + s);
    sl = sl == 2 ? 0 : sl + 1;
  };
#pragma unroll 1
  for (int s = 2; s < NSEG; s += 2) { seg(s, accA, accB, s > 2); seg(s + 1, accB, accA, true); }
  mfma_fence();
#pragma unroll
  for (int i = 0; i < 3; ++i) kst(NSEG - 1, i, accB);
  publish(c, fl, NSEG - 1);
}

__device__ __forceinline__ void role_U(const Params& p, Ctx& c, int h) {
  u32x4 W[10][6];
  {
    rsrc_t ro = mk_rsrc(p.Wo, DM * NH * DH * 2);
    const unsigned src = stage_src<2>(c, DM), rd = stage_rd(c.lane, c.wave);
    stage_weights<true, 6, 0, 10, 4>(c,
      [&](auto ci, LAS unsigned char* dst) {
        constexpr int id = ci, a = id / 10, pn = id % 10;
        dma16<0>(ro, dst, src, (unsigned)(((h * DH + 32 * a) * DM + pn * 64) * 2));
      },
      [&](auto ci, LAS unsigned char* chunk) {
        constexpr int id = ci;
        W[id % 10][id / 10] = stage_frag(chunk + rd);
      },
      [&](auto ci) { constexpr int id = ci; pin_a(W[id % 10][id / 10]); }, [](auto) {});
  }
  const int g = c.lane >> 4, rp = c.lane & 15;
  const int gs = rp >> 2, i4 = rp & 3;
  const unsigned aoff = (unsigned)((g * 16 + 8 * (i4 >> 1) + 4 * (gs & 1) + (i4 & 1)) * 16 + (gs >> 1) * 8);
  const unsigned uvo = (unsigned)((h >> 1) * 2048 + (g >> 1) * 1024 + ((h & 1) * 2 + (g & 1)) * 256 + rp * 16);
  int vready = -1, issued = -1;
  stamp(c, 1);
  auto dma_v = [&](int s) {
    LAS unsigned char* slot = c.lds + (s % 3) * KX_SZ;
#pragma unroll
    for (int i = 0; i < 3; ++i)
      dma16<SC1>(c.ws, slot + (c.wave * 3 + i) * 1024, (unsigned)c.lane * 16, OFF_VX + (unsigned)(s * NH + h) * KX_SZ + (unsigned)(c.wave * 3 + i) * 1024);
  };
  for (int s = 0; s < NSEG; ++s) {
    if (issued < s) {
      unsigned sp = 0;
      while ((vready = __builtin_amdgcn_readfirstlane(ld_flag(c, FL_V + FS * h))) < s) { __builtin_amdgcn_s_sleep(2); spin_check(c, sp); }
      dma_v(s); issued = s;
      wait_vm<0>();
    } else {
      wait_vm<10>();
    }
    bar();
    if (c.tid < FLAG_THREADS && s >= U_EAGER + 2) st_flag(c, FL_U + FS * h, s - 2);
    if (s + 1 < NSEG && vready >= s + 1) { dma_v(s + 1); issued = s + 1; }
    const u32x2 fv = ld_flag_raw(c, vready < min(s + 2, NSEG - 1) ? FL_V + FS * h : 0x80000000u);
    LAS unsigned char* sa0 = c.lds + (s % 3) * KX_SZ + aoff;
    LAS unsigned char* sa1 = sa0 + 32;
    pin_v(sa0); pin_v(sa1);
    u32x4 a[2][6];
#pragma unroll
    for (int k = 0; k < 6; ++k) {
      a[0][k] = cat(*(LAS u32x2*)(sa0 + (2 * k) * 1024), *(LAS u32x2*)(sa0 + (2 * k + 1) * 1024));
      a[1][k] = cat(*(LAS u32x2*)(sa1 + (2 * k) * 1024), *(LAS u32x2*)(sa1 + (2 * k + 1) * 1024));
    }
    sb();
    f32x4 d[10][2];
    u32x4 o;
    sfor<0, 10>([&](auto pc) {
      constexpr int pn = pc;
      sfor<0, 12>([&](auto ic) {
        constexpr int k = ic / 2, tp = ic % 2;
        mfma_v<true, false, k == 0>(d[pn][tp], W[pn][k], a[tp][k]);
        sb();
        if constexpr (pn > 0 && ic >= 7 && ic < 11) { constexpr int q = ic - 7; o[q] = pk(d[pn - 1][q / 2][2 * (q % 2)], d[pn - 1][q / 2][2 * (q % 2) + 1]); sb(); }
        if constexpr (pn > 0 && ic == 11) {
          st16<U_AUX>(c.ws, uvo, OFF_U + (unsigned)s * U_SZ + (unsigned)(4 * (pn - 1) + c.wave) * 32768, o);
          sb();
        }
      });
    });
    mfma_fence();
    st16<U_AUX>(c.ws, uvo, OFF_U + (unsigned)s * U_SZ + (unsigned)(36 + c.wave) * 32768, pack2(d[9][0], d[9][1]));
    {
      vready = max(vready, __builtin_amdgcn_readfirstlane(flag_val(c, fv)));
    }
    if (U_THROTTLE && s >= U_EAGER) __builtin_amdgcn_s_sleep(U_THROTTLE);
    if (s < U_EAGER) {
      if (issued == s && vready >= s + 1) { dma_v(s + 1); issued = s + 1; }
      publish(c, FL_U + FS * h, s);
    }
    stamp(c, 4 + s);
  }
  publish(c, FL_U + FS * h, NSEG - 1);
}

__device__ __forceinline__ void role_Q(const Params& p, Ctx& c, int h) {
  const int g = c.lane >> 4, n = c.lane & 15;
  const int qt = c.wave * 8 + (c.lane >> 3), tau = qt >> 4, rho = qt & 15, it0 = c.lane & 7, it = min(it0, 4);
  const int T = it >> 1;
  const bool dead = (it0 > 4) || (tau == 0 && it >= 2 && it < 4);
  const unsigned pvo = dead ? 0x80000000u : it < 4 ? (unsigned)(((h >> 1) * 3 + tau + (tau ? T : 0)) * 1024 + (((h & 1) * 2 + (it & 1)) * 16 + rho) * 16)
                                                   : (unsigned)(65536 + ((h >> 2) * 2 + tau) * 1024 + ((h & 3) * 16 + rho) * 16);
  const u32x4 sv = {SENT, SENT, SENT, SENT};
  st16<SC1>(c.ws, pvo, OFF_PX, sv); st16<SC1>(c.ws, pvo, OFF_PX + PX_SZ, sv);
  u32x4 W[3][20];
  load_proj_w(c, p.Wq, h, W);
  if (c.tid < FLAG_THREADS) st_flag(c, FL_PX + FS * h, 0);
  LAS unsigned char* PART = c.lds + 2 * HB_SZ;
  constexpr int QS = 336, WS_ = 16 * QS, BS_ = 4 * WS_;
  const int tile = it == 4 ? (tau ? 4 : 1) : (tau ? 2 + T : 0);
  const int ga = it == 4 ? 0 : (it & 1), gb = it == 4 ? 1 : (it & 1) + 2;
  const int pao = rho * QS + tile * 64 + ga * 16, pbo = rho * QS + tile * 64 + gb * 16;
  LAS unsigned char* PW = PART + c.wave * WS_ + n * QS + g * 16;
  f32x4 ba, bb;
#pragma unroll
  for (int r = 0; r < 4; ++r) {
    ba[r] = (dead || (it < 4 && 16 * T + 4 * ga + r > qt)) ? -__builtin_inff() : 0.f;
    bb[r] = (dead || (it < 4 && 16 * T + 4 * gb + r > qt)) ? -__builtin_inff() : 0.f;
  }
  if (c.wave == 0) wait_flags<8>(c, FL_H, 64, 0);
  if (c.wave == 1) wait_flags<8>(c, FL_KM + FS * h, 1, 0);
  bar();
  dma_hb(c, 0, c.lds); dma_hb(c, 1, c.lds + HB_SZ);
  wait_vm<10>();
  bar();
  u32x4 q[3], kx[3];
  int kready = -1;
  auto load_kx = [&](int s, int i) {
    kx[i] = ld16<SC1>(c.ws, (unsigned)c.lane * 16, OFF_KX + (unsigned)(s * NH + h) * KX_SZ + (unsigned)(4 * i + c.wave) * 1024);
  };
  auto xscores = [&](int s) {
    f32x4 s0 = {}, s2 = {}, s3 = {};
#pragma unroll
    for (int i = 0; i < 3; ++i) {
      const u32x2 q0 = {q[i].x, q[i].y}, q1 = {q[i].z, q[i].w}, k0 = {kx[i].x, kx[i].y}, k1 = {kx[i].z, kx[i].w};
      s0 = mfma16(k0, q0, s0); s2 = mfma16(k0, q1, s2); s3 = mfma16(k1, q1, s3);
    }
    LAS unsigned char* pw = PW + (s & 1) * BS_;
    *(LAS f32x4*)(pw) = s0; *(LAS f32x4*)(pw + 128) = s2; *(LAS f32x4*)(pw + 192) = s3;
  };
  {
    f32x4 acc[3][2];
    gemm_part<3, 0, 20>(W, c.lds + c.lane * 16, acc, [](auto) {});
    mfma_fence();
#pragma unroll
    for (int i = 0; i < 3; ++i) q[i] = pack2(acc[i][0], acc[i][1]);
    kready = wait_flags<8>(c, FL_K + FS * h, 1, 0);
#pragma unroll
    for (int i = 0; i < 3; ++i) load_kx(0, i);
    xscores(0);
  }
  const unsigned kmvo = (unsigned)(g * 64 + (n & 7) * 8 + c.wave * 256);
#pragma unroll 1
  for (int s = 0; s < NSEG; ++s) {
    u32x2 km[3];
    unsigned sp = 0;
    CYCW(u0)
    if (KM_BELL) doorbell<3, KM_DELAY>(c, FL_KM + FS * h, s + 1);
    for (;;) {
#pragma unroll
      for (int i = 0; i < 3; ++i) km[i] = ld8<SC1>(c.ws, kmvo, OFF_KM + (unsigned)(s * NH + h) * 3072 + (unsigned)i * 1024);
      if (!__any(umax3(umax3(km[0].x, km[0].y, km[1].x), umax3(km[1].y, km[2].x, km[2].y), 0u) == SENT)) break;
      __builtin_amdgcn_s_sleep(3);
      spin_check(c, sp);
    }
    wait_vm<0>();
    CYCW(u1)
    LAS unsigned char* PB = PART + (s & 1) * BS_;
    {
      f32x4 s1 = {}, s4 = {};
#pragma unroll
      for (int i = 0; i < 3; ++i) { s1 = mfma16(km[i], u32x2{q[i].x, q[i].y}, s1); s4 = mfma16(km[i], u32x2{q[i].z, q[i].w}, s4); }
      LAS unsigned char* pw = PW + (s & 1) * BS_;
      *(LAS f32x4*)(pw + 64) = s1; *(LAS f32x4*)(pw + 256) = s4;
    }
    if (EARLY_BELL && c.tid < FLAG_THREADS) st_flag(c, FL_PX + FS * h, s + 1);
    bar();
    CYCW(u2)
    {
      LAS unsigned char* pa = PB + pao;
      LAS unsigned char* pb = PB + pbo;
      f32x4 va = ((*(LAS f32x4*)pa + ba) + *(LAS f32x4*)(pa + WS_)) + (*(LAS f32x4*)(pa + 2 * WS_) + *(LAS f32x4*)(pa + 3 * WS_));
      f32x4 vb = ((*(LAS f32x4*)pb + bb) + *(LAS f32x4*)(pb + WS_)) + (*(LAS f32x4*)(pb + 2 * WS_) + *(LAS f32x4*)(pb + 3 * WS_));
      float m = fmaxf(fmaxf(fmaxf(va[0], va[1]), fmaxf(va[2], va[3])), fmaxf(fmaxf(vb[0], vb[1]), fmaxf(vb[2], vb[3])));
      asm volatile("s_nop 1\n\tv_max_f32_dpp %0, %0, %0 quad_perm:[1,0,3,2] row_mask:0xf bank_mask:0xf\n\t"
                   "s_nop 1\n\tv_max_f32_dpp %0, %0, %0 quad_perm:[2,3,0,1] row_mask:0xf bank_mask:0xf\n\t"
                   "s_nop 1\n\tv_max_f32_dpp %0, %0, %0 row_half_mirror row_mask:0xf bank_mask:0xf" : "+v"(m));
      const float cs = 0.07216878364870322f * 1.4426950408889634f;
      const float mc = -m * cs;
      float l = 0.f;
#pragma unroll
      for (int r = 0; r < 4; ++r) {
        va[r] = __builtin_amdgcn_exp2f(__builtin_fmaf(va[r], cs, mc));
        vb[r] = __builtin_amdgcn_exp2f(__builtin_fmaf(vb[r], cs, mc));
        l += va[r] + vb[r];
      }
      l += dpp<0xB1>(l); l += dpp<0x4E>(l); l += dpp<0x141>(l);
      const float inv = __builtin_amdgcn_rcpf(l);
      st16<SC1>(c.ws, pvo, OFF_PX + (unsigned)s * PX_SZ,
                u32x4{pks(va[0] * inv, va[1] * inv), pks(vb[0] * inv, vb[1] * inv), pks(va[2] * inv, va[3] * inv), pks(vb[2] * inv, vb[3] * inv)});
    }
    if (!EARLY_BELL && c.tid < FLAG_THREADS) st_flag(c, FL_PX + FS * h, s + 1);
    CYCW(u3)
    stamp(c, 4 + s);
    if (s + 1 < NSEG) {
      if (s + 2 < NSEG) st16<SC1>(c.ws, pvo, OFF_PX + (unsigned)(s + 2) * PX_SZ, sv);
      if (kready < s + 1) kready = wait_flags<8>(c, FL_K + FS * h, 1, s + 1);
      const int s2 = min(s + 2, NSEG - 1);
      f32x4 acc[3][2];
      gemm_part<3, 0, 20>(W, c.lds + ((s + 1) & 1) * HB_SZ + c.lane * 16, acc, [&](auto k) {
        if constexpr (k < 3) load_kx(s + 1, k);
        if constexpr (k >= 3 && k < 13) dma_hb1(c, s2, c.lds + (s & 1) * HB_SZ, k - 3);
      });
      mfma_fence();
      CYCW(u4)
#pragma unroll
      for (int i = 0; i < 3; ++i) q[i] = pack2(acc[i][0], acc[i][1]);
      xscores(s + 1);
      CYCW(u5)
      CYCOUT(s, u0, u1, u2, u3, u4, u5, u5)
    }
  }
}

constexpr int X1_ARM = 6;
__device__ __forceinline__ void role_PX(const Params& p, Ctx& c, int jx) {
  LAS float* ga = (LAS float*)c.lds;
  LAS unsigned char* R = c.lds + 4096;
  const int g = c.lane >> 4, n = c.lane & 15;
  const unsigned l16 = (unsigned)c.lane * 16;
  const int nt = c.wave & 1, tau = c.wave >> 1;
  const int col = 32 * jx + 16 * nt + 4 * g;
  const unsigned xo = (unsigned)(((tau * 16 + n) * DM + col) * 2);
  const unsigned x1o = (unsigned)((jx * 2 + tau) * 1024 + ((2 * nt + (g >> 1)) * 16 + n) * 16 + (g & 1) * 8);
#pragma unroll
  for (int i = 0; i < X1_ARM; ++i) st8<SC1>(c.ws, x1o, OFF_X1 + (unsigned)i * HB_SZ, u32x2{SENT, SENT});
  publish(c, FL_X1 + FS * jx, 0);
  Sides sd;
  get_sides(c, sd);
  load_mod(c, 2 * DM, ga, false);
  wait_flags<8>(c, FL_PX + 8 * FS * c.wave, 8, 0);
  bar();
  const f32x4 gav = *(LAS f32x4*)(ga + col);
  rsrc_t rx = mk_rsrc(p.x, 2048 * DM * 2);
  const unsigned ms0 = pz_side(sd, 2 * jx), ms1 = pz_side(sd, 2 * jx + 1);
  int uready = -1;
  auto ld_u = [&](int s, int i) -> u32x4 {
    return ld16<SC1>(c.ws, l16, OFF_U + (unsigned)s * U_SZ + (unsigned)((2 * jx + (i & 1)) * 32 + 8 * c.wave + (i >> 1)) * 1024);
  };
  auto step = [&](int s, const u32x4 (&z)[16], u32x4 (&zn)[16]) {
    const int sn = min(s + 1, NSEG - 1);
    if (uready < sn) uready = wait_flags<8>(c, FL_U + 8 * FS * c.wave, 8, sn);
    const unsigned spx = OFF_PX + (unsigned)s * PX_SZ;
    CYCW(u0)
    doorbell<PX_SLEEP, PX_DELAY>(c, FL_PX, s + 1);
    CYCW(u1)
    const unsigned m0 = m_phys((unsigned)s, (unsigned)(2 * jx), ms0) + (unsigned)c.wave * 2048, m1 = m_phys((unsigned)s, (unsigned)(2 * jx + 1), ms1) + (unsigned)c.wave * 2048;
    u32x4 pa[12], pm[2][2], zm[2][2];
    f32x4 acc[2][2];
    const u32x2 xv = ld8<0>(rx, xo, (unsigned)s * HB_SZ);
    unsigned sp = 0;
    for (;;) {
#pragma unroll
      for (int i = 0; i < 2; ++i) {
        zm[i][0] = ld16<SC1>(c.ws, l16 + i * 1024, m0); zm[i][1] = ld16<SC1>(c.ws, l16 + i * 1024, m1);
#pragma unroll
        for (int t = 0; t < 2; ++t) pm[i][t] = ld16<SC1>(c.ws, l16, spx + 65536 + (unsigned)((2 * c.wave + i) * 2 + t) * 1024);
      }
#pragma unroll
      for (int i = 0; i < 12; ++i) pa[i] = ld16<SC1>(c.ws, l16, spx + (unsigned)(12 * c.wave + i) * 1024);
      sb();
      unsigned m = 0;
      sfor<0, 8>([&](auto ic) {
        constexpr int k = ic >> 2, t = (ic >> 1) & 1, u = ic & 1;
        mfma_v<false, true, k == 0>(acc[t][u], zm[k][t], pm[k][u]);
        sb();
        if constexpr (u == 0) m = umax4(m, zm[k][t]);
        if constexpr (t == 1) m = umax4(m, pm[k][u]);
        sb();
      });
      sfor<0, 24>([&](auto ic) {
        constexpr int f = ic >> 1, t = ic & 1, pr = f / 3, w = f % 3;
        mfma_v<false, true, false>(acc[t][w ? 1 : 0], z[2 * (2 * pr + (w == 2 ? 1 : 0)) + t], pa[f]);
        sb();
        if constexpr (t == 0) m = umax4(m, pa[f]);
        if constexpr (ic >= 8) { if (s + 1 < NSEG) zn[ic - 8] = ld_u(sn, ic - 8); }
        sb();
      });
      if (!__any(m == SENT)) break;
      wait_flags<4>(c, FL_PX + 8 * FS * c.wave, 8, s + 1);
      __builtin_amdgcn_s_sleep(2);
      spin_check(c, sp);
    }
    if (EARLY_BELL && c.tid < FLAG_THREADS) st_flag(c, FL_X1 + FS * jx, s + 1);
    mfma_fence();
    CYCW(u2)
    LAS unsigned char* Rb = R + (s & 1) * 16384;
#pragma unroll
    for (int t = 0; t < 2; ++t)
#pragma unroll
      for (int u = 0; u < 2; ++u) *(LAS f32x4*)(Rb + c.wave * 4096 + (u * 2 + t) * 1024 + l16) = acc[t][u];
    bar();
    CYCW(u3)
    LAS unsigned char* rp = Rb + c.wave * 1024 + l16;
    const f32x4 v = (*(LAS f32x4*)rp + *(LAS f32x4*)(rp + 4096)) + (*(LAS f32x4*)(rp + 8192) + *(LAS f32x4*)(rp + 12288));
    const float o0 = bf_lo(xv.x) + rbf(gav[0] * rbf(v[0])), o1 = bf_hi(xv.x) + rbf(gav[1] * rbf(v[1]));
    const float o2 = bf_lo(xv.y) + rbf(gav[2] * rbf(v[2])), o3 = bf_hi(xv.y) + rbf(gav[3] * rbf(v[3]));
    st8<SC1>(c.ws, x1o, OFF_X1 + (unsigned)s * HB_SZ, u32x2{pks(o0, o1), pks(o2, o3)});
    if (!EARLY_BELL && c.tid < FLAG_THREADS) st_flag(c, FL_X1 + FS * jx, s + 1);
    CYCW(u4)
    CYCOUT(s, u0, u1, u2, u3, u4, u4, u4)
    stamp(c, 4 + s);
    if (s + X1_ARM < NSEG) st8<SC1>(c.ws, x1o, OFF_X1 + (unsigned)(s + X1_ARM) * HB_SZ, u32x2{SENT, SENT});
    if (BUMP && s == 1 && jx == 0 && c.tid == 0) st4<SC1>(c.ws, FL_EPOCH, 0, (c.tag ^ 0x9E3779B9u) + 1u);
  };
  u32x4 za[16], zb[16];
  uready = wait_flags<8>(c, FL_U + 8 * FS * c.wave, 8, 0);
#pragma unroll
  for (int i = 0; i < 16; ++i) za[i] = ld_u(0, i);
#pragma unroll 1
  for (int s = 0; s < NSEG; s += 2) { step(s, za, zb); step(s + 1, zb, za); }
}

__device__ __forceinline__ int ffn_seg(int rep, int i) { return (i >> 1) * 4 + (rep & 2) + (i & 1); }

__device__ __forceinline__ void role_F1(const Params& p, Ctx& c, int rep, int f1) {
  const u32x4 sv = {SENT, SENT, SENT, SENT};
  const int tau = rep & 1, g = c.lane >> 4, rho = c.lane & 15;
  const unsigned avo = (unsigned)(c.lane * 16 + (f1 * 4 + c.wave) * 2048 + tau * 1024);
  st16<SC1>(c.ws, avo, OFF_ACT + (unsigned)ffn_seg(rep, 0) * ACT_SZ, sv);
  st16<SC1>(c.ws, avo, OFF_ACT + (unsigned)ffn_seg(rep, 1) * ACT_SZ, sv);
  LAS float* shiftv = (LAS float*)(c.lds + 131072);
  LAS float* scalev = (LAS float*)(c.lds + 131072 + 4096);
  bar();
  load_mod(c, 3 * DM, shiftv, false);
  load_mod(c, 4 * DM, scalev, true);
  u32x4 W[4][20];
  f32x4 bias[4] = {}, csum[4] = {};
  {
    rsrc_t rg = mk_rsrc(p.wg, DM * DFF * 2), ru = mk_rsrc(p.wu, DM * DFF * 2);
    const unsigned src = stage_src<0>(c, DFF), rd = stage_rd(c.lane, c.wave);
    const u32x4 ones = {0x3F803F80u, 0x3F803F80u, 0x3F803F80u, 0x3F803F80u};
    stage_weights<false, 10, 0, 8, 4>(c,
      [&](auto ci, LAS unsigned char* dst) {
        constexpr int id = ci, kap = id / 4, pn = id % 4;
        dma16<0>(pn < 2 ? rg : ru, dst, src, (unsigned)(((32 * kap) * DFF + f1 * 128 + (pn & 1) * 64) * 2));
      },
      [&](auto ci, LAS unsigned char* chunk) {
        constexpr int id = ci;
        W[id % 4][id / 4] = stage_frag(chunk + rd);
      },
      [&](auto) {},
      [&](auto ci) {
        constexpr int id = ci, kap = id / 4, i = id % 4;
        const f32x4 s0 = *(LAS f32x4*)(scalev + 32 * kap + 8 * g), s1 = *(LAS f32x4*)(scalev + 32 * kap + 8 * g + 4);
        const f32x4 h0 = *(LAS f32x4*)(shiftv + 32 * kap + 8 * g), h1 = *(LAS f32x4*)(shiftv + 32 * kap + 8 * g + 4);
        const u32x4 w = W[i][kap];
        mfma_vn(bias[i], w, pack2(h0, h1));
        W[i][kap] = u32x4{pk(bf_lo(w.x) * s0[0], bf_hi(w.x) * s0[1]), pk(bf_lo(w.y) * s0[2], bf_hi(w.y) * s0[3]),
                          pk(bf_lo(w.z) * s1[0], bf_hi(w.z) * s1[1]), pk(bf_lo(w.w) * s1[2], bf_hi(w.w) * s1[3])};
        mfma_vn(csum[i], W[i][kap], ones);
        if constexpr (id < 64) pin_a(W[i][kap]); else pin_v(W[i][kap]);
      });
  }
  if (c.tid < FLAG_THREADS) st_flag(c, FL_ACT + FS * (16 * rep + f1), 0);
  LAS unsigned char* HB = c.lds;
  LAS float* red = (LAS float*)(c.lds + 40960);
  LAS float* rw = red + rho * 20 + c.wave * 4 + g;
  LAS float* rr = red + rho * 20;
  wait_flags<8>(c, FL_X1, 20, 0);
  stamp(c, 100);
  const unsigned vo = (unsigned)((10 * c.wave + tau) * 1024 + c.lane * 16);
  unsigned ones_u = 0x3F803F80u;
  pin_v(ones_u);
  auto sum16 = [&](LAS float* q) {
    const f32x4 t = (*(LAS f32x4*)q + *(LAS f32x4*)(q + 4)) + (*(LAS f32x4*)(q + 8) + *(LAS f32x4*)(q + 12));
    return (t[0] + t[1]) + (t[2] + t[3]);
  };
  u32x4 rawn[5];
  bool pre = false;
  int seen = -1;
#pragma unroll 1
  for (int it = 0; it < 32; ++it) {
    const int s = ffn_seg(rep, it);
    const unsigned sx = OFF_X1 + (unsigned)s * HB_SZ;
    LAS unsigned char* hb = HB + (it & 1) * 20480 + c.lane * 16;
    CYCW(u0)
    u32x4 raw[5];
    if (!pre) {
      seen = doorbell<4, F1_DELAY>(c, FL_X1, s + 1);
#pragma unroll
      for (int i = 0; i < 5; ++i) rawn[i] = ld16<SC1>(c.ws, vo, sx + i * 2048);
    }
    CYCW(u1)
    unsigned sp = 0;
    for (;;) {
      unsigned m = 0;
      float s0 = 0.f, s1 = 0.f;
      sb();
#pragma unroll
      for (int i = 0; i < 5; ++i) {
        raw[i] = rawn[i];
        *(LAS u32x4*)(hb + (5 * c.wave + i) * 1024) = raw[i];
        m = umax4(m, raw[i]);
        s0 = dot2(raw[i].x, ones_u, s0); s1 = dot2(raw[i].y, ones_u, s1);
        s0 = dot2(raw[i].z, ones_u, s0); s1 = dot2(raw[i].w, ones_u, s1);
      }
      rw[0] = s0 + s1;
      if (!__any(m == SENT)) break;
      wait_flags<4>(c, FL_X1 + 5 * FS * c.wave, 5, s + 1);
      __builtin_amdgcn_s_sleep(2);
      spin_check(c, sp);
#pragma unroll
      for (int i = 0; i < 5; ++i) rawn[i] = ld16<SC1>(c.ws, vo, sx + i * 2048);
    }
    const int sn = ffn_seg(rep, it + 1);
    pre = it + 1 < 32 && seen >= sn + 2;
    const unsigned von = pre ? vo : 0x80000000u;
    bar();
    CYCW(u2)
    const float mean = sum16(rr) * (1.f / DM);
    f32x4 acc[4];
    float v0 = 0.f, v1 = 0.f;
    {
      u32x4 b[20];
#pragma unroll
      for (int k = 0; k < 3; ++k) b[k] = lds16(hb + k * 1024);
      sb();
      sfor<0, 20>([&](auto kc) {
        constexpr int k = kc;
        const unsigned d = raw[k / 4][k % 4];
        float a0, a1;
        mfma_v<(k * 4 + 0 < 64), true, k == 0>(acc[0], W[0][k], b[k]); sb();
        if constexpr (k + 3 < 20) b[k + 3] = lds16(hb + (k + 3) * 1024);
        a0 = bf_lo(d); a1 = bf_hi(d); sb();
        mfma_v<(k * 4 + 1 < 64), true, k == 0>(acc[1], W[1][k], b[k]); sb();
        a0 = add1(a0, -mean); a1 = add1(a1, -mean); sb();
        mfma_v<(k * 4 + 2 < 64), true, k == 0>(acc[2], W[2][k], b[k]); sb();
        asm("v_fmac_f32 %0, %1, %1" : "+v"(v0) : "v"(a0)); asm("v_fmac_f32 %0, %1, %1" : "+v"(v1) : "v"(a1)); sb();
        mfma_v<(k * 4 + 3 < 64), true, k == 0>(acc[3], W[3][k], b[k]); sb();
        if constexpr (k < 5) { rawn[k] = ld16<SC1>(c.ws, von, OFF_X1 + (unsigned)sn * HB_SZ + k * 2048); sb(); }
      });
    }
    rw[320] = v0 + v1;
    if (EARLY_BELL && c.tid < FLAG_THREADS) st_flag(c, FL_ACT + FS * (16 * rep + f1), it + 1);
    mfma_fence();
    bar();
    sb();
    CYCW(u3)
    const float rstd = __builtin_amdgcn_rsqf(sum16(rr + 320) * (1.f / DM) + 1e-5f);
    float a[8];
#pragma unroll
    for (int i = 0; i < 2; ++i)
#pragma unroll
      for (int r = 0; r < 4; ++r) {
        const float gv = __builtin_fmaf(__builtin_fmaf(-mean, csum[i][r], acc[i][r]), rstd, bias[i][r]);
        const float uv = __builtin_fmaf(__builtin_fmaf(-mean, csum[2 + i][r], acc[2 + i][r]), rstd, bias[2 + i][r]);
        const float z2 = gv * __builtin_fmaf(gv * gv, -2.f * 1.4426950408889634f * 0.7978845608028654f * 0.044715f, -2.f * 1.4426950408889634f * 0.7978845608028654f);
        a[4 * i + r] = gv * uv * __builtin_amdgcn_rcpf(1.f + __builtin_amdgcn_exp2f(z2));
      }
    wait_vm<5>();
    st16<SC1>(c.ws, avo, OFF_ACT + (unsigned)s * ACT_SZ, u32x4{pks(a[0], a[1]), pks(a[2], a[3]), pks(a[4], a[5]), pks(a[6], a[7])});
    if (!EARLY_BELL && c.tid < FLAG_THREADS) st_flag(c, FL_ACT + FS * (16 * rep + f1), it + 1);
    CYCW(u4)
    CYCOUT(s, u0, u1, u2, u3, u4, u4, u4)
    stamp(c, 104 + s);
    if (s + 4 < NSEG) st16<SC1>(c.ws, avo, OFF_ACT + (unsigned)(s + 4) * ACT_SZ, sv);
  }
}

__device__ __forceinline__ void role_F2(const Params& p, Ctx& c, int rep, int f2) {
  bar();
  u32x4 W[12][4];
  {
    rsrc_t rd_ = mk_rsrc(p.wd, DFF * DM * 2);
    const unsigned src = stage_src<3>(c, DM);
    unsigned rd[4];
#pragma unroll
    for (int u = 0; u < 4; ++u) rd[u] = stage_rd(c.lane, u) + (unsigned)c.wave * 4096;
    stage_weights<false, 4, 0, 12, 3>(c,
      [&](auto ci, LAS unsigned char* dst) {
        constexpr int id = ci;
        dma16<0>(rd_, dst, src, (unsigned)(((128 * (id / 4) + 16 * (id % 4)) * DM + f2 * 64) * 2));
      },
      [&](auto ci, LAS unsigned char* chunk) {
        constexpr int id = ci;
        if constexpr (id % 4 == 0) {
#pragma unroll
          for (int u = 0; u < 4; ++u) W[id / 4][u] = stage_frag(chunk + rd[u]);
        }
      },
      [&](auto ci) { constexpr int id = ci; pin_a(W[id / 4][id % 4]); }, [](auto) {});
  }
  LAS float* gb = (LAS float*)c.lds;
  LAS unsigned char* R = c.lds + 4096;
  load_mod(c, 5 * DM, gb, false);
  wait_flags<8>(c, FL_ACT + FS * 16 * rep, 12, 0);
  bar();
  const int tau = rep & 1, g = c.lane >> 4, n = c.lane & 15;
  const unsigned l16 = (unsigned)c.lane * 16;
  const int col = 64 * f2 + 16 * c.wave + 4 * g;
  const f32x4 gbv = *(LAS f32x4*)(gb + col);
  rsrc_t ro = mk_rsrc(p.out, 2048 * DM * 2);
  const unsigned xo = (unsigned)(((tau * 16 + n) * DM + col) * 2);
  const unsigned x1o = (unsigned)((col >> 5) * 2048 + tau * 1024 + (((col >> 3) & 3) * 16 + n) * 16 + (g & 1) * 8);
  stamp(c, 100);
  bool pre = false;
  int seen = -1;
  auto ld_b = [&](unsigned vo, int s, int i) { return ld16<SC1>(c.ws, vo, OFF_ACT + (unsigned)s * ACT_SZ + (unsigned)((4 * i + c.wave) * 2 + tau) * 1024); };
  auto item = [&](int it, u32x4 (&b)[12], u32x4 (&bn)[12]) {
    const int s = ffn_seg(rep, it), sn = ffn_seg(rep, it + 1);
    if (!pre) {
      seen = doorbell<4, F2_DELAY>(c, FL_ACT + FS * 16 * rep, it + 1);
#pragma unroll
      for (int i = 0; i < 12; ++i) b[i] = ld_b(l16, s, i);
    }
    const u32x2 xv = ld8<SC1>(c.ws, x1o, OFF_X1 + (unsigned)s * HB_SZ);
    f32x4 acc[4];
    unsigned sp = 0;
    for (;;) {
      sb();
      unsigned m = 0;
      sfor<0, 48>([&](auto ic) {
        constexpr int i = ic >> 2, u = ic & 3;
        mfma_v<true, true, i == 0>(acc[u], W[i][u], b[i]);
        sb();
        if constexpr (u == 0) { m = umax4(m, b[i]); sb(); }
      });
      if (!__any(m == SENT)) break;
      wait_flags<4>(c, FL_ACT + FS * 16 * rep, 12, it + 1);
      __builtin_amdgcn_s_sleep(2);
      spin_check(c, sp);
      sfor<0, 12>([&](auto i) { b[i] = ld_b(l16, s, i); });
    }
    pre = it + 1 < 32 && seen >= it + 3;
    if (pre) sfor<0, 12>([&](auto i) { bn[i] = ld_b(l16, sn, i); });
    mfma_fence();
    LAS unsigned char* Rb = R + (it & 1) * 16384;
#pragma unroll
    for (int u = 0; u < 4; ++u) *(LAS f32x4*)(Rb + c.wave * 4096 + u * 1024 + l16) = acc[u];
    bar();
    LAS unsigned char* rp = Rb + c.wave * 1024 + l16;
    const f32x4 v = (*(LAS f32x4*)rp + *(LAS f32x4*)(rp + 4096)) + (*(LAS f32x4*)(rp + 8192) + *(LAS f32x4*)(rp + 12288));
    const float o0 = bf_lo(xv.x) + rbf(gbv[0] * rbf(v[0])), o1 = bf_hi(xv.x) + rbf(gbv[1] * rbf(v[1]));
    const float o2 = bf_lo(xv.y) + rbf(gbv[2] * rbf(v[2])), o3 = bf_hi(xv.y) + rbf(gbv[3] * rbf(v[3]));
    st8<0>(ro, xo, (unsigned)s * HB_SZ, u32x2{pk(o0, o1), pk(o2, o3)});
    stamp(c, 104 + s);
  };
  u32x4 b0[12];
#pragma unroll 1
  for (int it = 0; it < 32; ++it) item(it, b0, b0);
}

enum { M_PRE = 1, M_KV = 2, M_U = 4, M_REC = 8, M_Q = 16, M_PX = 32, M_F1 = 64, M_F2 = 128, M_ALL = 255 };

__global__ void __launch_bounds__(256, 1) mega_kernel(Params p) {
  extern __shared__ __attribute__((aligned(16))) unsigned char smem[];
  Ctx c;
  c.ws = mk_rsrc(p.ws, WS_BYTES);
  c.srd = u32x4{(unsigned)(uintptr_t)p.ws, (unsigned)((uintptr_t)p.ws >> 32) & 0xFFFFu, WS_BYTES, 0x00020000u};
  c.wsp = p.ws;
  c.tag = 0;
  c.lds = (LAS unsigned char*)smem;
  c.tid = threadIdx.x; c.lane = c.tid & 63; c.wave = __builtin_amdgcn_readfirstlane(c.tid >> 6);
  const int grp = blockIdx.x & 7, ix = blockIdx.x >> 3;
  stamp(c, 0);
  int role, h, rep, r;
  rep = grp - 4; r = ix;
  if (grp < 4) {
    if (ix < 16) { role = ix >> 3; h = grp * 8 + (ix & 7); }
    else if (ix < 26) { role = 6; h = grp * 10 + ix - 16; }
    else if (ix < 31) { role = 6; h = 40 + grp * 5 + ix - 26; }
    else { role = 2; h = 28 + grp; }
  } else {
    if (ix < 24) { role = 3 + (ix >> 3); h = rep * 8 + (ix & 7); }
    else if (ix < 31) { role = 2; h = rep * 7 + ix - 24; }
    else { role = 6; h = 60 + rep; }
  }
  if (role < 2) { for (int i = 0; i < HV_DELAY; ++i) __builtin_amdgcn_s_sleep(32); }
  if (role == 3) { for (int i = 0; i < KW_DELAY; ++i) __builtin_amdgcn_s_sleep(32); }
  if (role == 5) { for (int i = 0; i < UW_DELAY; ++i) __builtin_amdgcn_s_sleep(32); }
  if (role == 2) {
    for (int i = 0; i < Q_DELAY; ++i) __builtin_amdgcn_s_sleep(32);
  }
  if (role == 0) { if (p.mask & M_REC) role_H(p, c, h); }
  else if (role == 1) { if (p.mask & M_REC) role_V(p, c, h); }
  else if (role == 2) { if (p.mask & M_Q) role_Q(p, c, h); }
  else if (role < 6) {
    if (role == 5) { if (p.mask & M_U) role_U(p, c, h); }
    else { if (p.mask & M_KV) role_KV(p, c, h, role == 4); }
    if (r < 12) { if (p.mask & M_F1) role_F1(p, c, rep, r); }
    else if (r < 22) { if (p.mask & M_F2) role_F2(p, c, rep, r - 12); }
  } else {
    if (p.mask & M_PRE) role_pre(p, c, h, h < 40 ? h : -1);
    if (h < 40) { if (p.mask & M_REC) role_PZ(p, c, h); }
    else if (h < 60) { if (p.mask & M_PX) role_PX(p, c, h - 40); }
    else if (FFN_PREFETCH) {
      for (int i = 0; i < 24; ++i) __builtin_amdgcn_s_sleep(32);
#pragma unroll 1
      for (int m = 0; m < 3; ++m) {
        rsrc_t r = mk_rsrc(m == 0 ? p.wg : m == 1 ? p.wu : p.wd, DM * DFF * 2);
#pragma unroll 1
        for (unsigned o = (unsigned)(h - 60) * 49152u; o < DM * DFF * 2; o += 4 * 49152) {
          u32x4 t[12];
#pragma unroll
          for (int i = 0; i < 12; ++i) t[i] = ld16<0>(r, (unsigned)c.tid * 16u + i * 4096u, o);
#pragma unroll
          for (int i = 0; i < 12; ++i) asm volatile("" :: "v"(t[i]));
        }
      }
    }
  }
}

}

extern "C" void kernel_launch(void* const* d_in, const int* in_sizes, int n_in, void* d_out, int out_size, void* d_ws,
                              size_t ws_size, hipStream_t stream) {
  Params p;
  p.x = d_in[0]; p.mem0 = d_in[1]; p.Wq = d_in[2]; p.Wk = d_in[3]; p.Wv = d_in[4]; p.Wo = d_in[5];
  p.wg = d_in[8]; p.wu = d_in[9]; p.wd = d_in[10]; p.c = d_in[11]; p.wmod = d_in[12];
  void* wsa = (void*)(((uintptr_t)d_ws + ((2u << 20) - 1)) & ~(uintptr_t)((2u << 20) - 1));
  p.out = d_out; p.ws = wsa; p.mask = M_ALL; p.pad = 0;
  (void)hipFuncSetAttribute((const void*)mega_kernel, hipFuncAttributeMaxDynamicSharedMemorySize, LDS_BYTES);
  hipLaunchKernelGGL(mega_kernel, dim3(256), dim3(256), LDS_BYTES, stream, p);
}
```

```cpp
#include <hip/hip_runtime.h>
#include <hip/hip_bf16.h>
#include <type_traits>
#include <cstdint>

#define LAS __attribute__((address_space(3)))
typedef unsigned u32x2 __attribute__((ext_vector_type(2)));
typedef unsigned u32x4 __attribute__((ext_vector_type(4)));
typedef unsigned u32x8 __attribute__((ext_vector_type(8)));
typedef float f32x2 __attribute__((ext_vector_type(2)));
typedef float f32x4 __attribute__((ext_vector_type(4)));
typedef short s16x4 __attribute__((ext_vector_type(4)));
typedef __bf16 bf16x2 __attribute__((ext_vector_type(2)));
typedef __bf16 bf16x8 __attribute__((ext_vector_type(8)));
typedef __bf16 bf16x16 __attribute__((ext_vector_type(16)));
typedef __amdgpu_buffer_rsrc_t rsrc_t;

namespace {

constexpr int DM = 640, NH = 32, DH = 192, NSEG = 64, DFF = 1536;
constexpr unsigned SENT = 0xFFFFFFFFu;

constexpr unsigned FS = 128;
constexpr unsigned FL_EPOCH = 128, FL_EPOCH2 = 98304 - 128;
constexpr unsigned OFF_MOD = 1024;
constexpr unsigned OFF_MODP = 57344;
constexpr unsigned FL_H = 16384;
constexpr unsigned FL_KM = 24576, FL_K = 28672, FL_V = 32768, FL_U = 36864, FL_PX = 40960;
constexpr unsigned FL_X1 = 45056;
constexpr unsigned FL_ACT = 49152;
constexpr unsigned FL_MP = OFF_MODP + 15360 + 1024;
constexpr unsigned OFF_DB = 81920;
constexpr unsigned FL_MD = OFF_DB + 4096;
constexpr unsigned FL_MD2 = OFF_DB + 8192 + 4096;
constexpr unsigned OFF_XR = 98304;
constexpr unsigned MEM_SZ = 10240, P_SZ = 20480;
constexpr unsigned L_MEM = 0, L_P = 64 * MEM_SZ, L_END = L_P + 64 * P_SZ;
constexpr unsigned OFF_CAL = OFF_XR + 2 * L_END;
constexpr unsigned OFF_M = OFF_CAL + 65536;
constexpr unsigned M_SZ = 40 * 16384;
constexpr unsigned OFF_HB = OFF_M + 64 * M_SZ;
constexpr unsigned HB_SZ = 40960;
constexpr unsigned OFF_KX = OFF_HB + 64 * HB_SZ;
constexpr unsigned KX_SZ = 12288;
constexpr unsigned OFF_VX = OFF_KX + 64 * 32 * KX_SZ;
constexpr unsigned OFF_U = OFF_VX + 64 * 32 * KX_SZ;
constexpr unsigned U_SZ = 40 * 32 * 1024;
constexpr unsigned OFF_KM = OFF_U + 64 * U_SZ;
constexpr unsigned OFF_PX = OFF_KM + 64 * 32 * 3072;
constexpr unsigned PX_SZ = 81920;
constexpr unsigned OFF_X1 = OFF_PX + 64 * PX_SZ;
constexpr unsigned OFF_ACT = OFF_X1 + 2048 * 640 * 2;
constexpr unsigned ACT_SZ = 48 * 2 * 1024;
constexpr unsigned WS_BYTES = OFF_ACT + 64 * ACT_SZ;
static_assert(WS_BYTES + (2u << 20) <= (256u << 20), "workspace");
static_assert(OFF_XR % 16384 == 0 && OFF_CAL % 16384 == 0 && OFF_M % 16384 == 0, "alignment");

constexpr int LDS_BYTES = 160 * 1024;
constexpr unsigned SPIN_CAP = 1u << 21;
constexpr int PZ_SLEEP = 14;
constexpr int BELL_P_GAP = 4;
constexpr int H_D1 = 8;
constexpr int PX_DELAY = 12;
constexpr int H_SLEEP = 12;
constexpr int V_SLEEP = 16;
constexpr int F1_DELAY = 4, F2_DELAY = 2;
constexpr int NBELL = 4, U_EAGER = 6;

struct Params {
  const void* x; const void* mem0; const void* Wq; const void* Wk; const void* Wv; const void* Wo;
  const void* wg; const void* wu; const void* wd; const void* c; const void* wmod;
  void* out; void* ws;
};

template <int I, int N, class F>
__device__ __forceinline__ void sfor(F&& f) {
  if constexpr (I < N) { f(std::integral_constant<int, I>{}); sfor<I + 1, N>(f); }
}
__device__ __forceinline__ rsrc_t mk_rsrc(const void* p, unsigned bytes) {
  return __builtin_amdgcn_make_buffer_rsrc(const_cast<void*>(p), 0, bytes, 0x00020000);
}
template <int AUX> __device__ __forceinline__ u32x4 ld16(rsrc_t r, unsigned vo, unsigned so) { return __builtin_amdgcn_raw_buffer_load_b128(r, vo, so, AUX); }
template <int AUX> __device__ __forceinline__ u32x2 ld8(rsrc_t r, unsigned vo, unsigned so) { return __builtin_amdgcn_raw_buffer_load_b64(r, vo, so, AUX); }
template <int AUX> __device__ __forceinline__ unsigned ld4(rsrc_t r, unsigned vo, unsigned so) { return __builtin_amdgcn_raw_buffer_load_b32(r, vo, so, AUX); }
template <int AUX> __device__ __forceinline__ void st16(rsrc_t r, unsigned vo, unsigned so, u32x4 v) { __builtin_amdgcn_raw_buffer_store_b128(v, r, vo, so, AUX); }
template <int AUX> __device__ __forceinline__ void st8(rsrc_t r, unsigned vo, unsigned so, u32x2 v) { __builtin_amdgcn_raw_buffer_store_b64(v, r, vo, so, AUX); }
template <int AUX> __device__ __forceinline__ void st4(rsrc_t r, unsigned vo, unsigned so, unsigned v) { __builtin_amdgcn_raw_buffer_store_b32(v, r, vo, so, AUX); }
constexpr int SC1 = 16;
template <int AUX> __device__ __forceinline__ void dma16(rsrc_t r, LAS unsigned char* dst, unsigned vo, unsigned so) {
  __builtin_amdgcn_raw_ptr_buffer_load_lds(r, (LAS void*)dst, 16, vo, so, 0, AUX);
}
template <int N> __device__ __forceinline__ void wait_vm() { asm volatile("s_waitcnt vmcnt(%0)" ::"n"(N) : "memory"); }
__device__ __forceinline__ void wait_lgkm() { asm volatile("s_waitcnt lgkmcnt(0)" ::: "memory"); }
__device__ __forceinline__ void bar() { asm volatile("s_waitcnt lgkmcnt(0)\n\ts_barrier" ::: "memory"); }

__device__ __forceinline__ unsigned pk(float a, float b) {
  f32x2 v = {a, b};
  return __builtin_bit_cast(unsigned, __builtin_convertvector(v, bf16x2));
}
__device__ __forceinline__ unsigned pks(float a, float b) { return min(pk(a, b), 0xFFFFFFFEu); }
__device__ __forceinline__ float bf_lo(unsigned u) { return __builtin_bit_cast(float, u << 16); }
__device__ __forceinline__ float bf_hi(unsigned u) { return __builtin_bit_cast(float, u & 0xFFFF0000u); }
__device__ __forceinline__ float rbf(float a) { return bf_lo(pk(a, a)); }
__device__ __forceinline__ float f_(unsigned u) { return __builtin_bit_cast(float, u); }
__device__ __forceinline__ unsigned u_(float f) { return __builtin_bit_cast(unsigned, f); }
__device__ __forceinline__ unsigned umax3(unsigned a, unsigned b, unsigned c) { return max(max(a, b), c); }
__device__ __forceinline__ unsigned umax4(unsigned m, u32x4 v) { return umax3(umax3(m, v.x, v.y), v.z, v.w); }

__device__ __forceinline__ float dot2(unsigned a, unsigned b, float c) {
  return __builtin_amdgcn_fdot2_f32_bf16(__builtin_bit_cast(bf16x2, a), __builtin_bit_cast(bf16x2, b), c, false);
}
__device__ __forceinline__ f32x4 mfma16(u32x2 a, u32x2 b, f32x4 c) {
  return __builtin_amdgcn_mfma_f32_16x16x16bf16_1k(__builtin_bit_cast(s16x4, a), __builtin_bit_cast(s16x4, b), c, 0, 0, 0);
}
__device__ __forceinline__ f32x4 smfma(u32x4 a, u32x8 b, f32x4 c, int idx) {
  return __builtin_amdgcn_smfmac_f32_16x16x64_bf16(__builtin_bit_cast(bf16x8, a), __builtin_bit_cast(bf16x16, b), c, idx, 0, 0);
}
__device__ __forceinline__ u32x2 tr8(LAS unsigned char* p) {
  return __builtin_bit_cast(u32x2, __builtin_amdgcn_ds_read_tr16_b64_v4i16((LAS s16x4*)p));
}
__device__ __forceinline__ u32x4 lds16(LAS unsigned char* p) { return *(LAS u32x4*)p; }
__device__ __forceinline__ u32x4 cat(u32x2 a, u32x2 b) { return u32x4{a.x, a.y, b.x, b.y}; }
__device__ __forceinline__ u32x8 cat(u32x4 a, u32x4 b) { return u32x8{a.x, a.y, a.z, a.w, b.x, b.y, b.z, b.w}; }
template <int CTRL> __device__ __forceinline__ float dpp(float x) {
  return f_((unsigned)__builtin_amdgcn_update_dpp(0, (int)u_(x), CTRL, 0xf, 0xf, false));
}

template <class T> __device__ __forceinline__ void pin_a(T& v) { asm volatile("" : "+a"(v)); }
template <class T> __device__ __forceinline__ void pin_v(T& v) { asm volatile("" : "+v"(v)); }

__device__ __forceinline__ void sb() { __builtin_amdgcn_sched_barrier(0); }

struct Ctx {
  rsrc_t ws;
  u32x4 srd;
  unsigned tag;
  const void* wsp;
  LAS unsigned char* lds;
  int tid, lane, wave;
};

__device__ __forceinline__ void spin_check(const Ctx& c, unsigned& n) {
  unsigned ts, tv;
  ++n;
  asm volatile(
      "s_and_b32 %0, %2, 0x3ff\n\t"
      "s_cbranch_scc1 1f\n\t"
      "buffer_load_dword %1, off, %3, 0 sc1\n\t"
      "s_waitcnt vmcnt(0)\n\t"
      "v_cmp_eq_u32 vcc, %1, %5\n\t"
      "s_nop 4\n\t"
      "s_cbranch_vccnz 2f\n\t"
      "s_cmp_lt_u32 %2, %4\n\t"
      "s_cbranch_scc1 1f\n\t"
      "buffer_store_dword %5, off, %3, 0 sc1\n\t"
      "s_waitcnt vmcnt(0)\n"
      "2:\n\t"
      "s_endpgm\n"
      "1:"
      : "=&s"(ts), "=&v"(tv)
      : "s"(n), "s"(c.srd), "s"(SPIN_CAP), "v"(c.tag)
      : "memory", "scc", "vcc");
}
template <class T> __device__ __forceinline__ void pin_s(T& v) { asm volatile("" : "+s"(v)); }
__device__ __forceinline__ void load_tag(Ctx& c) {
  unsigned e, e2;
  asm volatile("s_load_dword %0, %2, %3\n\ts_load_dword %1, %2, %4\n\ts_waitcnt lgkmcnt(0)"
               : "=&s"(e), "=&s"(e2) : "s"(c.wsp), "i"(FL_EPOCH), "i"(FL_EPOCH2) : "memory");
  c.tag = e + 3u * e2 + 0x9E3779B9u;
}
__device__ __forceinline__ u32x2 ld_flag_raw(const Ctx& c, unsigned vo) { return ld8<SC1>(c.ws, vo, 0); }
__device__ __forceinline__ int flag_val(const Ctx& c, u32x2 r) { return r.y == c.tag ? (int)r.x : -1; }
__device__ __forceinline__ int ld_flag(const Ctx& c, unsigned vo) { return flag_val(c, ld_flag_raw(c, vo)); }
__device__ __forceinline__ void st_flag(const Ctx& c, unsigned fl, int v) { st8<SC1>(c.ws, fl, 0, u32x2{(unsigned)v, c.tag}); }
template <int SLEEP>
__device__ __forceinline__ int wait_flags(const Ctx& c, unsigned fl, int n, int need) {
  unsigned sp = 0;
  const unsigned vo = fl + FS * (unsigned)min(c.lane, n - 1);
  for (;;) {
    const int v = ld_flag(c, vo);
    if (!__any(v < need)) {
      int got = need;
      if (!__any(v < need + 1)) got = need + 1;
      if (!__any(v < need + 3)) got = need + 3;
      if (!__any(v < need + 6)) got = need + 6;
      if (!__any(v < need + 16)) got = need + 16;
      return got;
    }
    __builtin_amdgcn_s_sleep(SLEEP);
    spin_check(c, sp);
  }
}
__device__ __forceinline__ void publish(const Ctx& c, unsigned fl, int val) {
  wait_vm<0>();
  bar();
  if (c.tid == 0) st_flag(c, fl, val);
}

template <int SLEEP, int DELAY>
__device__ __forceinline__ int doorbell(const Ctx& c, unsigned fl, int need) {
  unsigned sp = 0;
  int v;
  while ((v = __builtin_amdgcn_readfirstlane(ld_flag(c, fl))) < need) { __builtin_amdgcn_s_sleep(SLEEP); spin_check(c, sp); }
  if (sp) __builtin_amdgcn_s_sleep(DELAY);
  return v;
}
__device__ __forceinline__ u32x4 nosent(u32x4 v) { return u32x4{min(v.x, 0xFFFFFFFEu), min(v.y, 0xFFFFFFFEu), min(v.z, 0xFFFFFFFEu), min(v.w, 0xFFFFFFFEu)}; }
__device__ __forceinline__ u32x2 nosent(u32x2 v) { return u32x2{min(v.x, 0xFFFFFFFEu), min(v.y, 0xFFFFFFFEu)}; }

__device__ __forceinline__ unsigned side_bit(unsigned b, unsigned side) { return b | (((side ^ (b >> 21)) & 1u) << 13); }
__device__ __forceinline__ unsigned xr_phys(unsigned o, unsigned side) { return side_bit(OFF_XR + ((o >> 13) << 14) + (o & 8191u), side); }
__device__ __forceinline__ unsigned m_phys(unsigned s, unsigned j, unsigned side) { return side_bit(OFF_M + (s * 40u + j) * 16384u, side); }
__device__ __forceinline__ unsigned my_iod() { return (((unsigned)__builtin_amdgcn_s_getreg(20 | (3 << 11))) >> 2) & 1u; }
struct Sides { unsigned me; unsigned long long light; };
__device__ __forceinline__ u32x2 ld_table(const Ctx& c) { return ld_flag_raw(c, FL_H + FS * (unsigned)c.lane); }
__device__ __forceinline__ void get_sides(const Ctx& c, Sides& o, unsigned v = SENT) {
  unsigned sp = 0;
  if (__any(v == SENT)) {
    for (;;) {
      v = (unsigned)flag_val(c, ld_table(c));
      if (!__any(v == SENT)) break;
      __builtin_amdgcn_s_sleep(8);
      spin_check(c, sp);
    }
  }
  const unsigned cc = __popcll(__ballot(v & 1u) >> 32) > 16 ? 1u : 0u;
  const unsigned long long iod = __ballot(v & 2u);
  o.me = cc ^ my_iod();
  o.light = cc ? ~iod : iod;
}
__device__ __forceinline__ unsigned pz_side(const Sides& sd, int j) { return (unsigned)(sd.light >> j) & 1u; }
__device__ __forceinline__ void wait_bells(const Ctx& c, unsigned vo, int need) {
  unsigned sp = 0;
  if (c.wave == 0) {
    while (__any((int)ld4<SC1>(c.ws, vo, 0) < need)) spin_check(c, sp);
  } else {
    wait_vm<0>();
  }
  __builtin_amdgcn_s_barrier();
}
__device__ __forceinline__ unsigned bell_vo(const Ctx& c, int bell0, unsigned side) { return side_bit(OFF_DB + (unsigned)(bell0 + (c.lane & (NBELL - 1))) * FS, side); }
__device__ __forceinline__ unsigned calibrate(const Ctx& c, int slot) {
  const unsigned base = OFF_CAL + (unsigned)slot * 256u;
  st16<SC1>(c.ws, base + (unsigned)(c.lane >> 3) * 8192u + (unsigned)(c.lane & 7) * 16u, 0, u32x4{0u, 0u, 0u, 0u});
  wait_vm<0>();
  unsigned best[2] = {~0u, ~0u};
#pragma unroll
  for (int i = 0; i < 8; ++i) {
    const unsigned long long t0 = __builtin_amdgcn_s_memtime();
    wait_lgkm();
    unsigned v = ld4<SC1>(c.ws, base, (unsigned)i * 8192u);
    asm volatile("s_waitcnt vmcnt(0)" : "+v"(v)::"memory");
    const unsigned long long t1 = __builtin_amdgcn_s_memtime();
    wait_lgkm();
    const unsigned par = (((OFF_CAL + i * 8192u) >> 13) ^ ((OFF_CAL + i * 8192u) >> 21)) & 1u;
    best[par] = min(best[par], (unsigned)(t1 - t0));
  }
  return best[1] < best[0] ? 1u : 0u;
}

template <int MODE> __device__ __forceinline__ int rowfn(int g, int j) {
  if (MODE == 0) return 8 * g + j;
  if (MODE == 1) return 16 * (g >> 1) + 8 * ((j >> 1) & 1) + 4 * (g & 1) + 2 * ((j >> 2) & 1) + (j & 1);
  if (MODE == 2) return 16 * (j >> 2) + 4 * g + (j & 3);
  return 64 * (j >> 2) + 4 * g + (j & 3);
}
template <int MODE> __device__ __forceinline__ unsigned stage_src(const Ctx& c, int ldw) {
  const int pos = c.lane >> 3, cp = c.lane & 7;
  const int u = (cp >> 1) ^ ((pos >> 1) & 3);
  const int ch = 2 * u + (cp & 1);
  const int g = 2 * (c.wave >> 1) + (pos >> 2), j = 4 * (c.wave & 1) + (pos & 3);
  return (unsigned)((rowfn<MODE>(g, j) * ldw + ch * 8) * 2);
}
__device__ __forceinline__ unsigned stage_rd(int lane, int u) {
  const int g = lane >> 4, q = (lane & 15) >> 2, p = lane & 3, pos = 4 * (g & 1) + q;
  return (unsigned)((g >> 1) * 2048 + pos * 128 + ((u ^ ((pos >> 1) & 3)) * 32) + p * 8);
}
__device__ __forceinline__ u32x4 stage_frag(LAS unsigned char* chunk_plus_rd) { return cat(tr8(chunk_plus_rd), tr8(chunk_plus_rd + 1024)); }

template <bool FIRST, int NB, int NRES, int BATCH, int NBUF, class Issue, class Consume, class Pin, class Post>
__device__ __forceinline__ void stage_weights(Ctx& c, Issue&& issue, Consume&& consume, Pin&& pin, Post&& post) {
  constexpr int SLOT = BATCH * 4096;
  sfor<0, (NBUF < NB ? NBUF : NB)>([&](auto b) {
    sfor<0, BATCH>([&](auto i) { issue(std::integral_constant<int, b * BATCH + i>{}, c.lds + b * SLOT + i * 4096 + c.wave * 1024); });
  });
  if (FIRST) load_tag(c);
  sfor<0, NB - NRES>([&](auto b) {
    constexpr int inflight = ((b + NBUF < NB ? b + NBUF : NB) - (b + 1)) * BATCH;
    wait_vm<inflight>();
    bar();
    sfor<0, BATCH>([&](auto i) { consume(std::integral_constant<int, b * BATCH + i>{}, c.lds + (b % NBUF) * SLOT + i * 4096); });
    sb();
    sfor<0, BATCH>([&](auto i) { pin(std::integral_constant<int, b * BATCH + i>{}); });
    if constexpr (b + NBUF < NB) {
      bar();
      sfor<0, BATCH>([&](auto i) {
        issue(std::integral_constant<int, (b + NBUF) * BATCH + i>{}, c.lds + (b % NBUF) * SLOT + i * 4096 + c.wave * 1024);
      });
    }
    sb();
    sfor<0, BATCH>([&](auto i) { post(std::integral_constant<int, b * BATCH + i>{}); });
  });
  wait_vm<0>();
  bar();
}

__device__ __forceinline__ float ln_stats(const Ctx& c, const u32x4 (&raw)[10], f32x2 (&cv)[40], LAS float* red) {
  const int tau = c.wave & 1, kh = c.wave >> 1, g = c.lane >> 4, rho = c.lane & 15, row = tau * 16 + rho;
  float s0 = 0.f, s1 = 0.f;
  unsigned ones_u = 0x3F803F80u;
  pin_v(ones_u);
#pragma unroll
  for (int i = 0; i < 10; ++i) {
    s0 = dot2(raw[i].x, ones_u, s0); s1 = dot2(raw[i].y, ones_u, s1);
    s0 = dot2(raw[i].z, ones_u, s0); s1 = dot2(raw[i].w, ones_u, s1);
  }
  float s = s0 + s1;
  s += __shfl_xor(s, 16); s += __shfl_xor(s, 32);
  if (g == 0) red[kh * 32 + row] = s;
  bar();
  const float mean = (red[row] + red[32 + row]) * (1.f / DM);
  f32x2 v2 = {0.f, 0.f};
#pragma unroll
  for (int i = 0; i < 10; ++i)
#pragma unroll
    for (int d = 0; d < 4; ++d) {
      const f32x2 t = f32x2{bf_lo(raw[i][d]), bf_hi(raw[i][d])} - mean;
      cv[4 * i + d] = t;
      v2 += t * t;
    }
  float v = v2[0] + v2[1];
  v += __shfl_xor(v, 16); v += __shfl_xor(v, 32);
  if (g == 0) red[64 + kh * 32 + row] = v;
  bar();
  return 1.f / sqrtf((red[64 + row] + red[96 + row]) * (1.f / DM) + 1e-5f);
}
template <class Store>
__device__ __forceinline__ void ln_apply(const Ctx& c, const f32x2 (&cv)[40], float rstd, LAS float* shiftv, LAS float* scalev, Store&& store) {
  const int tau = c.wave & 1, kh = c.wave >> 1, g = c.lane >> 4;
#pragma unroll
  for (int i = 0; i < 10; ++i) {
    const int kap = kh * 10 + i, d0 = (4 * kap + g) * 8;
    const f32x4 sc0 = *(LAS f32x4*)(scalev + d0), sc1 = *(LAS f32x4*)(scalev + d0 + 4);
    const f32x4 sh0 = *(LAS f32x4*)(shiftv + d0), sh1 = *(LAS f32x4*)(shiftv + d0 + 4);
    const f32x2 r0 = (cv[4 * i + 0] * rstd) * f32x2{sc0[0], sc0[1]} + f32x2{sh0[0], sh0[1]};
    const f32x2 r1 = (cv[4 * i + 1] * rstd) * f32x2{sc0[2], sc0[3]} + f32x2{sh0[2], sh0[3]};
    const f32x2 r2 = (cv[4 * i + 2] * rstd) * f32x2{sc1[0], sc1[1]} + f32x2{sh1[0], sh1[1]};
    const f32x2 r3 = (cv[4 * i + 3] * rstd) * f32x2{sc1[2], sc1[3]} + f32x2{sh1[2], sh1[3]};
    store(kap * 2 + tau, u32x4{pk(r0[0], r0[1]), pk(r1[0], r1[1]), pk(r2[0], r2[1]), pk(r3[0], r3[1])});
  }
}
__device__ __forceinline__ unsigned md_flag(int pr) { return pr < 32 ? FL_MD + FS * (unsigned)pr : FL_MD2 + FS * (unsigned)(pr - 32); }
__device__ __forceinline__ void load_mod(const Ctx& c, int f0, LAS float* dst, bool add1) {
  unsigned sp = 0;
  const unsigned fo = md_flag((f0 - 1280) / 64 + min(c.lane, 9));
  while (__any(ld_flag(c, fo) < 0)) { __builtin_amdgcn_s_sleep(8); spin_check(c, sp); }
  const int cc = min(c.tid, 159);
  f32x4 f = __builtin_bit_cast(f32x4, ld16<SC1>(c.ws, OFF_MOD + (unsigned)(f0 * 4 + cc * 16), 0));
  if (add1) { f[0] = rbf(1.f + f[0]); f[1] = rbf(1.f + f[1]); f[2] = rbf(1.f + f[2]); f[3] = rbf(1.f + f[3]); }
  *(LAS f32x4*)(dst + cc * 4) = f;
}

__device__ __forceinline__ void role_pre(const Params& p, Ctx& c, int li, int jpz) {
  const unsigned iod = my_iod();
  unsigned tabw = iod << 1;
  LAS float* sc = (LAS float*)c.lds;
  LAS float* red = (LAS float*)(c.lds + 4096);
  rsrc_t rw = mk_rsrc(p.wmod, DM * 3840 * 2);
  const int cg = c.tid & 7, rg = c.tid >> 3;
  u32x4 w[20];
  const unsigned cv3 = ld4<0>(mk_rsrc(p.c, DM * 2), (unsigned)c.tid * 4u, 0), cv4 = ld4<0>(mk_rsrc(p.c, DM * 2), (unsigned)c.tid * 4u + 1024u, 0);
  const int r0 = (li / 20) * 224 + rg * 7;
  if (li < 60) {
#pragma unroll
    for (int i = 0; i < 7; ++i) w[i] = ld16<2>(rw, (unsigned)(((r0 + i) * 3840 + (li % 20) * 64 + cg * 8) * 2), 0);
  }
  rsrc_t rx = mk_rsrc(p.x, 2048 * DM * 2);
  u32x4 raw[10];
  {
    const int tau = c.wave & 1, kh = c.wave >> 1, g = c.lane >> 4, rho = c.lane & 15;
    const unsigned vo = (unsigned)(((li * 32 + tau * 16 + rho) * DM + (40 * kh + g) * 8) * 2);
#pragma unroll
    for (int i = 0; i < 10; ++i) raw[i] = ld16<0>(rx, vo + i * 64, 0);
  }
  sb();
  load_tag(c);
  auto silu = [](float v) { return rbf(v / (1.f + __expf(-v))); };
  *(LAS f32x2*)(sc + 2 * c.tid) = f32x2{silu(bf_lo(cv3)), silu(bf_hi(cv3))};
  if (c.tid < 80) *(LAS f32x2*)(sc + 512 + 2 * c.tid) = f32x2{silu(bf_lo(cv4)), silu(bf_hi(cv4))};
  bar();
  auto gemv = [&](auto nrow, int row0, unsigned dst, bool round, unsigned fl) {
    float a[8] = {};
#pragma unroll
    for (int i = 0; i < nrow; ++i) {
      const float sv = sc[row0 + i];
#pragma unroll
      for (int d = 0; d < 4; ++d) { a[2 * d] += sv * bf_lo(w[i][d]); a[2 * d + 1] += sv * bf_hi(w[i][d]); }
    }
    *(LAS f32x4*)(red + rg * 64 + cg * 8) = f32x4{a[0], a[1], a[2], a[3]};
    *(LAS f32x4*)(red + rg * 64 + cg * 8 + 4) = f32x4{a[4], a[5], a[6], a[7]};
    bar();
    float t = 0.f;
#pragma unroll
    for (int r = 0; r < 8; ++r) t += red[(c.wave * 8 + r) * 64 + c.lane];
    red[2048 + c.tid] = t;
    bar();
    if (c.tid < 64) {
      t = (red[2048 + c.tid] + red[2112 + c.tid]) + (red[2176 + c.tid] + red[2240 + c.tid]);
      st4<SC1>(c.ws, dst + (unsigned)c.tid * 4, 0, u_(round ? rbf(t) : t));
      wait_vm<0>();
      if (c.tid == 0) st_flag(c, fl, 0);
    }
  };
  if (li < 60) gemv(std::integral_constant<int, 7>{}, r0, OFF_MODP + (unsigned)((li / 20) * 1280 + (li % 20) * 64) * 4, false, FL_MP + FS * li);
  if (jpz >= 0) {
    const u32x4 sv = {SENT, SENT, SENT, SENT};
#pragma unroll
    for (int i = 0; i < 8; ++i) st16<SC1>(c.ws, (unsigned)c.tid * 16u, OFF_M + (unsigned)((i >> 2) * 40 + jpz) * 16384u + (unsigned)(i & 3) * 4096u, sv);
    if (c.tid < 128) st16<SC1>(c.ws, xr_phys(L_P + (unsigned)jpz * 1024u + (unsigned)(c.tid & 63) * 16u, c.tid >> 6), 0, sv);
    else if (c.tid < 192) st16<SC1>(c.ws, xr_phys(L_MEM + (unsigned)(1 + ((c.tid >> 4) & 1)) * MEM_SZ + (unsigned)jpz * 256u + (unsigned)(c.tid & 15) * 16u, (c.tid >> 5) & 1), 0, sv);
    else if (c.tid >= 224) { if (jpz < NBELL) st16<SC1>(c.ws, OFF_DB + (unsigned)(jpz + 16 * ((c.tid >> 3) & 1)) * FS + (unsigned)((c.tid >> 4) & 1) * 8192u + (unsigned)(c.tid & 7) * 16u, 0, sv); }
    else if (c.tid < 224) {
      const u32x4 m0 = ld16<0>(mk_rsrc(p.mem0, 8 * DM * 2), (unsigned)((c.tid & 7) * DM * 2 + jpz * 32 + ((c.tid >> 3) & 1) * 16), 0);
      st16<SC1>(c.ws, xr_phys(L_MEM + (unsigned)jpz * 256u + (unsigned)(c.tid & 15) * 16u, (c.tid >> 4) & 1), 0, nosent(m0));
    }
  }
  LAS float* shiftv = (LAS float*)(c.lds + 16384);
  LAS float* red2 = (LAS float*)(c.lds + 24576);
  f32x2 cv[40];
  const float rstd = ln_stats(c, raw, cv, red2);
  wait_flags<4>(c, FL_MP, 60, 0);
#pragma unroll
  for (int ci = 0; ci < 2; ++ci) {
    const int cc = min(c.tid + 256 * ci, 319);
    const u32x4 v0 = ld16<SC1>(c.ws, (unsigned)cc * 16, OFF_MODP), v1 = ld16<SC1>(c.ws, (unsigned)cc * 16, OFF_MODP + 5120), v2 = ld16<SC1>(c.ws, (unsigned)cc * 16, OFF_MODP + 10240);
    f32x4 f = __builtin_bit_cast(f32x4, v0) + __builtin_bit_cast(f32x4, v1) + __builtin_bit_cast(f32x4, v2);
    const float one = cc >= 160 ? 1.f : 0.f;
#pragma unroll
    for (int d = 0; d < 4; ++d) f[d] = rbf(one + rbf(f[d]));
    *(LAS f32x4*)(shiftv + cc * 4) = f;
  }
  bar();
  ln_apply(c, cv, rstd, shiftv, shiftv + DM, [&](int fr, u32x4 o) { st16<SC1>(c.ws, (unsigned)c.lane * 16 + (unsigned)fr * 1024, OFF_HB + (unsigned)li * HB_SZ, o); });
  if (li >= 32 && c.wave == 0) tabw |= calibrate(c, li - 32) ^ iod;
  publish(c, FL_H + FS * li, (int)tabw);
  if (li >= 24) {
#pragma unroll
    for (int i = 0; i < 20; ++i) w[i] = ld16<2>(rw, (unsigned)(((rg * 20 + i) * 3840 + 1280 + (li - 24) * 64 + cg * 8) * 2), 0);
    sb();
    gemv(std::integral_constant<int, 20>{}, rg * 20, OFF_MOD + (unsigned)(1280 + (li - 24) * 64) * 4, true, md_flag(li - 24));
  }
}

__device__ __forceinline__ int sp_row(int lane) { return ((lane >> 1) & 6) | (lane & 1); }
__device__ __forceinline__ int sp_half(int lane) { return (lane >> 1) & 1; }
__device__ __forceinline__ int sp_pos(int row, int half) { return 4 * (row >> 1) + 2 * half + (row & 1); }
__device__ __forceinline__ float add1(float a, float b) { float o; asm("v_add_f32 %0, %1, %2" : "=v"(o) : "v"(a), "v"(b)); return o; }
struct Gath { unsigned vo2, bvo; LAS unsigned char* w0; LAS unsigned char* w2; };
__device__ __forceinline__ Gath gather_setup(const Ctx& c, bool perm, unsigned side) {
  int r = c.lane & 7;
  if (perm) r = (r & 1) | ((r & 2) << 1) | ((r & 4) >> 1);
  const int wpos = ((c.lane & 48) + sp_pos(r, (c.lane >> 3) & 1)) * 16;
  Gath g;
  g.vo2 = c.wave < 2 ? (unsigned)c.lane * 16 : 0x80000000u;
  g.bvo = bell_vo(c, 16, side);
  g.w0 = c.lds + 2 * c.wave * 1024 + wpos;
  g.w2 = c.lds + (c.wave < 2 ? (8 + c.wave) * 1024 : 79872 + (c.wave - 2) * 1024) + wpos;
  return g;
}
__device__ __forceinline__ void gather_mem(const Ctx& c, const Gath& ga, int s, unsigned side) {
  const unsigned lo = L_MEM + (unsigned)s * MEM_SZ;
  unsigned s0 = xr_phys(lo + c.wave * 2048, side), s2 = xr_phys(lo + 8192 + (c.wave & 1) * 1024, side);
  pin_s(s0); pin_s(s2);
  unsigned sp = 0;
  if (s > 0) wait_bells(c, ga.bvo, s);
  for (;;) {
    const u32x4 v0 = ld16<SC1>(c.ws, (unsigned)c.lane * 16, s0), v1 = ld16<SC1>(c.ws, (unsigned)c.lane * 16 + 1024, s0), v2 = ld16<SC1>(c.ws, ga.vo2, s2);
    sb();
    *(LAS u32x4*)(ga.w0) = v0;
    unsigned m = umax4(0u, v0);
    sb();
    *(LAS u32x4*)(ga.w0 + 1024) = v1;
    m = umax4(m, v1);
    sb();
    *(LAS u32x4*)(ga.w2) = v2;
    m = umax4(m, v2);
    if (!__any(m == SENT)) break;
    spin_check(c, sp);
  }
  bar();
}
struct ResW { LAS unsigned char* lo; LAS unsigned char* hi; };
__device__ __forceinline__ ResW res_base(const Ctx& c) {
  ResW r; r.lo = c.lds + 81920 + stage_rd(c.lane, c.wave); r.hi = r.lo + 40960;
  pin_v(r.lo); pin_v(r.hi);
  return r;
}
__device__ __forceinline__ u32x8 res_frag(const ResW& w, int t) {
  LAS unsigned char* b = t < 5 ? w.lo + t * 8192 : w.hi + (t - 5) * 8192;
  return cat(stage_frag(b), stage_frag(b + 4096));
}
template <int CTRL> __device__ __forceinline__ unsigned dppu(unsigned x) { return (unsigned)__builtin_amdgcn_update_dpp(0, (int)x, CTRL, 0xf, 0xf, true); }
__device__ __forceinline__ void role_H(const Params& p, Ctx& c, int h) {
  u32x8 W[10][5];
  u32x2 tabv, kfl;
  const unsigned kmvo = (c.lane & 8) ? (unsigned)((c.lane >> 4) * 64 + (c.lane & 7) * 8 + c.wave * 256) : 0x80000000u;
#pragma unroll
  for (int i = 0; i < 6; ++i) st8<SC1>(c.ws, kmvo, OFF_KM + (unsigned)((i / 3) * NH + h) * 3072 + (unsigned)(i % 3) * 1024, u32x2{SENT, SENT});
  {
    rsrc_t rq = mk_rsrc(p.Wq, DM * NH * DH * 2), rk = mk_rsrc(p.Wk, DM * NH * DH * 2);
    const unsigned src = stage_src<1>(c, NH * DH), rd = stage_rd(c.lane, c.wave);
    stage_weights<true, 12, 2, 10, 4>(c,
      [&](auto ci, LAS unsigned char* dst) {
        constexpr int id = ci;
        if constexpr (id < 100) {
          constexpr int t = id / 10, pn = (id % 10) / 2, a = id % 2;
          constexpr int col = (pn < 3 ? pn : pn - 3) * 64;
          dma16<0>(pn < 3 ? rq : rk, dst, src, (unsigned)(((64 * t + 32 * a) * NH * DH + h * DH + col) * 2));
        } else {
          constexpr int sp = id - 100;
          dma16<0>(rk, dst, src, (unsigned)(((32 * sp) * NH * DH + h * DH + 128) * 2));
        }
      },
      [&](auto ci, LAS unsigned char* chunk) {
        constexpr int id = ci, t = id / 10, pn = (id % 10) / 2, a = id % 2;
        if constexpr (a == 1) {
          W[t][pn] = cat(stage_frag(chunk - 4096 + rd), stage_frag(chunk + rd));
        }
      },
      [&](auto ci) {
        constexpr int id = ci, t = id / 10, pn = (id % 10) / 2, a = id % 2;
        if constexpr (a == 1) { if constexpr (t * 5 + pn < 32) pin_a(W[t][pn]); else pin_v(W[t][pn]); }
      },
      [&](auto ci) { if constexpr (ci == 80) { tabv = ld_table(c); kfl = ld_flag_raw(c, FL_K + FS * h); } });
  }
  if (c.tid == 0) st_flag(c, FL_KM + FS * h, 0);
  Sides sd;
  get_sides(c, sd, (unsigned)flag_val(c, tabv));
  LAS unsigned char* A = c.lds + c.lane * 16;
  const int g = c.lane >> 4, n = c.lane & 15;
  LAS unsigned char* TW = c.lds + 10240 + c.wave * 1920 + n * 120 + g * 4;
  LAS unsigned char* TR = c.lds + 10240 + c.wave * 1920 + (c.lane >> 2) * 120 + (c.lane & 3) * 8;
  pin_v(TW);
  LAS unsigned char* PART = c.lds + 49152;
  LAS unsigned char* PW = PART + c.wave * 3328 + n * 208 + g * 16;
  const ResW WR = res_base(c);
  const int idx = (c.lane & 2) ? 0xEEEE : 0x4444;
  const int q = (c.wave & 1) * 4 + g, it = min(n, 9);
  LAS unsigned char* PR = PART + q * 208 + (it < 8 ? it : it + 2) * 16;
  const float lmask = n < 10 ? 1.f : 0.f;
  const unsigned plo = it < 8 ? (unsigned)((h >> 1) * 1024 + (it >> 2) * 512 + (h & 1) * 256 + sp_pos(q, (it >> 1) & 1) * 16) : (unsigned)(16384 + h * 128 + q * 16);
  const bool ringer = h % (32 / NBELL) == 0 && c.wave == 0;
  const Gath ga = gather_setup(c, false, sd.me);
  const unsigned rvo = (ringer && c.lane < 2) ? OFF_DB + (unsigned)(h / (32 / NBELL)) * FS + (unsigned)c.lane * 8192u : 0x80000000u;
  LAS unsigned char* KX = c.lds + 24576;
  int kready = __builtin_amdgcn_readfirstlane(flag_val(c, kfl));
  auto dma_kx = [&](int s) {
    if (kready < s) kready = wait_flags<2>(c, FL_K + FS * h, 1, s);
#pragma unroll
    for (int i = 0; i < 6; ++i)
      dma16<SC1>(c.ws, KX + (s & 1) * KX_SZ + (c.wave * 6 + i) * 1024, (unsigned)c.lane * 16, OFF_KX + (unsigned)(s * NH + h) * KX_SZ + (unsigned)(c.wave * 6 + i) * 1024);
  };
  if (c.wave < 2) dma_kx(0);
  for (int s = 0; s < NSEG; ++s) {
    unsigned pvo = xr_phys(L_P + (unsigned)s * P_SZ + plo, n & 1);
    if (n >= 10) pvo = 0x80000000u;
    f32x4 acc[6] = {};
    u32x8 wl[10];
    wl[0] = res_frag(WR, 0); wl[1] = res_frag(WR, 1);
#pragma unroll
    for (int i = 0; i < 6; ++i) pin_v(acc[i]);
    LAS unsigned char* kxp = KX + (s & 1) * KX_SZ + c.wave * 1024 + c.lane * 16;
    pin_v(wl[0]); pin_v(wl[1]); pin_v(pvo); pin_v(kxp);
    gather_mem(c, ga, s, sd.me);
    u32x4 a[10], kx[3];
    a[0] = lds16(A); a[1] = lds16(A + 1024); a[2] = lds16(A + 2048);
    sb();
#pragma unroll
    for (int t = 0; t < 6; ++t) {
      LAS unsigned char* wb = t + 2 < 5 ? WR.lo + (t + 2) * 8192 : WR.hi + (t + 2 - 5) * 8192;
      acc[5] = smfma(a[t], wl[t], acc[5], idx); sb();
      a[t + 3] = lds16(A + (t + 3) * 1024); sb();
      acc[0] = smfma(a[t], W[t][0], acc[0], idx); sb();
      const u32x2 w0 = tr8(wb); sb();
      acc[1] = smfma(a[t], W[t][1], acc[1], idx); sb();
      const u32x2 w1 = tr8(wb + 1024); sb();
      acc[2] = smfma(a[t], W[t][2], acc[2], idx); sb();
      const u32x2 w2 = tr8(wb + 4096); sb();
      acc[3] = smfma(a[t], W[t][3], acc[3], idx); sb();
      const u32x2 w3 = tr8(wb + 5120); sb();
      acc[4] = smfma(a[t], W[t][4], acc[4], idx); sb();
      wl[t + 2] = cat(cat(w0, w1), cat(w2, w3));
    }
    f32x4 sx0, sx1, sm;
    u32x2 F[3];
    float v[4];
#define H_S(i, t) acc[i] = smfma(a[t], W[t][i], acc[i], idx); sb();
#define H_L(t) acc[5] = smfma(a[t], wl[t], acc[5], idx); sb();
#define H_A(i, r) v[r] = add1(acc[(r >> 1) * 3 + i][r & 1], acc[(r >> 1) * 3 + i][2 + (r & 1)]); sb();
#define H_K(i) kx[i] = lds16(kxp + i * 4096); sb();
#define H_MZ(d, x, i) asm volatile("v_mfma_f32_16x16x16_bf16 %0, %1, %2, 0" : "=&v"(d) : "v"(x), "v"(F[i])); sb();
#define H_MA(d, x, i) asm volatile("v_mfma_f32_16x16x16_bf16 %0, %1, %2, %0" : "+v"(d) : "v"(x), "v"(F[i])); sb();
#define KLO(i) (u32x2{kx[i].x, kx[i].y})
#define KHI(i) (u32x2{kx[i].z, kx[i].w})
#define H_C(r) pw[r] = pk(v[2 * r], v[2 * r + 1]); sb();
#define H_D(i) *(LAS unsigned*)(TW + i * 40) = pw[0]; *(LAS unsigned*)(TW + i * 40 + 16) = pw[1]; F[i] = tr8(TR + i * 40); sb();
    unsigned pw[2];
    {
      LAS unsigned char* wb = WR.hi + 3 * 8192;
      H_S(1, 6) a[9] = lds16(A + 9 * 1024); const u32x2 w0 = tr8(wb); sb(); H_S(4, 6) const u32x2 w1 = tr8(wb + 1024); sb();
      H_S(1, 7) const u32x2 w2 = tr8(wb + 4096); sb(); H_S(4, 7) const u32x2 w3 = tr8(wb + 5120); sb();
      wl[8] = cat(cat(w0, w1), cat(w2, w3));
      H_S(1, 8) H_S(4, 8) H_S(1, 9) H_S(4, 9)
      H_L(6) const u32x2 w4 = tr8(wb + 8192), w5 = tr8(wb + 9216); sb(); H_S(2, 6) const u32x2 w6 = tr8(wb + 12288), w7 = tr8(wb + 13312); sb();
      wl[9] = cat(cat(w4, w5), cat(w6, w7));
    }
    H_L(7) H_A(1, 0) H_K(1) H_S(2, 7) H_A(1, 1) H_L(8) H_A(1, 2) H_S(2, 8) H_A(1, 3) H_S(2, 9) H_C(0) H_K(2) H_L(9) H_C(1)
    H_S(0, 6) H_D(1) H_S(3, 6) H_K(0) H_S(0, 7) H_A(2, 0) H_S(3, 7) H_A(2, 1) H_S(0, 8) H_A(2, 2) H_S(3, 8) H_A(2, 3) H_S(0, 9) H_C(0) H_S(3, 9) H_C(1) H_D(2)
    H_MZ(sx0, KLO(1), 1)
    asm volatile("s_nop 1" : "+v"(acc[0]));
    H_A(0, 0) H_A(0, 1) H_A(0, 2) H_A(0, 3) H_C(0) H_C(1) H_D(0) H_MZ(sx1, KHI(1), 1)
    st4<SC1>(c.ws, rvo, 0, (unsigned)s); sb();
    H_MZ(sm, F[1], 1)
    H_MA(sx0, KLO(2), 2) H_MA(sx1, KHI(2), 2) H_MA(sm, F[2], 2)
    H_MA(sx0, KLO(0), 0) H_MA(sx1, KHI(0), 0) H_MA(sm, F[0], 0)
    asm volatile("s_nop 5" : "+v"(sx0), "+v"(sx1));
    if (s < NSEG - 1) {
      *(LAS f32x4*)(PW) = sx0; sb(); *(LAS f32x4*)(PW + 64) = sx1; sb(); *(LAS f32x4*)(PW + 128) = sm;
      bar();
      if (__builtin_expect(c.wave >= 2, 1)) {
        const f32x4 p0 = *(LAS f32x4*)PR, p1 = *(LAS f32x4*)(PR + 3328), p2 = *(LAS f32x4*)(PR + 6656), p3 = *(LAS f32x4*)(PR + 9984);
        const f32x4 xa = p0 + p1, xb = p2 + p3;
        u32x4 o;
        float x0, x1, x2, x3, ml, mm, t, d;
        asm volatile(
            "v_add_f32 %4, %12, %16\n\tv_add_f32 %5, %13, %17\n\tv_add_f32 %6, %14, %18\n\tv_add_f32 %7, %15, %19\n\t"
            "v_max3_f32 %8, %4, %5, %6\n\tv_max_f32 %8, %8, %7\n\t"
            "v_sub_f32 %4, %4, %8\n\tv_sub_f32 %5, %5, %8\n\t"
            "v_max_f32_dpp %9, %8, %8 row_ror:8 row_mask:0xf bank_mask:0xf\n\t"
            "v_sub_f32 %6, %6, %8\n\tv_sub_f32 %7, %7, %8\n\tv_mul_f32 %4, %20, %4\n\t"
            "v_max_f32_dpp %9, %9, %9 row_ror:4 row_mask:0xf bank_mask:0xf\n\t"
            "v_mul_f32 %5, %20, %5\n\tv_mul_f32 %6, %20, %6\n\tv_mul_f32 %7, %20, %7\n\t"
            "v_max_f32_dpp %9, %9, %9 row_ror:2 row_mask:0xf bank_mask:0xf\n\t"
            "v_exp_f32 %4, %4\n\tv_exp_f32 %5, %5\n\t"
            "v_max_f32_dpp %9, %9, %9 row_ror:1 row_mask:0xf bank_mask:0xf\n\t"
            "v_exp_f32 %6, %6\n\t"
            "v_sub_f32 %11, %8, %9\n\t"
            "v_exp_f32 %7, %7\n\t"
            "v_mul_f32 %11, %20, %11\n\t"
            "v_add_f32 %8, %4, %5\n\t"
            "v_exp_f32 %11, %11\n\t"
            "v_add_f32 %9, %6, %7\n\t"
            "v_add_f32 %8, %8, %9\n\t"
            "v_mul_f32 %10, %11, %21\n\t"
            "v_mul_f32 %8, %8, %10\n\t"
            "v_mul_f32 %4, %4, %11\n\tv_mul_f32 %5, %5, %11\n\t"
            "v_add_f32_dpp %8, %8, %8 row_ror:8 row_mask:0xf bank_mask:0xf\n\t"
            "v_mul_f32 %6, %6, %11\n\tv_mul_f32 %7, %7, %11\n\t"
            "v_add_f32_dpp %8, %8, %8 row_ror:4 row_mask:0xf bank_mask:0xf\n\t"
            "s_nop 1\n\t"
            "v_add_f32_dpp %8, %8, %8 row_ror:2 row_mask:0xf bank_mask:0xf\n\t"
            "s_nop 1\n\t"
            "v_add_f32_dpp %8, %8, %8 row_ror:1 row_mask:0xf bank_mask:0xf\n\t"
            "v_rcp_f32 %8, %8\n\t"
            "s_nop 0\n\t"
            "v_mul_f32 %4, %4, %8\n\tv_mul_f32 %5, %5, %8\n\tv_mul_f32 %6, %6, %8\n\tv_mul_f32 %7, %7, %8\n\t"
            "v_cvt_pk_bf16_f32 %9, %4, %5\n\tv_cvt_pk_bf16_f32 %10, %6, %7\n\t"
            "s_nop 0\n\t"
            "v_min_u32_dpp %0, %9, %22 quad_perm:[0,0,2,2] row_mask:0xf bank_mask:0xf\n\t"
            "v_min_u32_dpp %2, %9, %22 quad_perm:[1,1,3,3] row_mask:0xf bank_mask:0xf\n\t"
            "v_min_u32_dpp %1, %10, %22 quad_perm:[0,0,2,2] row_mask:0xf bank_mask:0xf\n\t"
            "v_min_u32_dpp %3, %10, %22 quad_perm:[1,1,3,3] row_mask:0xf bank_mask:0xf"
            : "=&v"(o.x), "=&v"(o.y), "=&v"(o.z), "=&v"(o.w), "=&v"(x0), "=&v"(x1), "=&v"(x2), "=&v"(x3), "=&v"(ml), "=&v"(mm), "=&v"(t), "=&v"(d)
            : "v"(xa[0]), "v"(xa[1]), "v"(xa[2]), "v"(xa[3]), "v"(xb[0]), "v"(xb[1]), "v"(xb[2]), "v"(xb[3]),
              "s"(0.07216878364870322f * 1.4426950408889634f), "v"(lmask), "v"(0xFFFFFFFEu));
        st16<SC1>(c.ws, pvo, 0, o);
        if (n < 10 && s + 2 < NSEG - 1) st16<SC1>(c.ws, xr_phys(L_P + (unsigned)(s + 2) * P_SZ + plo, n & 1), 0, u32x4{SENT, SENT, SENT, SENT});
      } else {
        dma_kx(s + 1);
        __builtin_amdgcn_s_sleep(H_D1);
      }
    }
#pragma unroll
    for (int i = 0; i < 3; ++i) st8<SC1>(c.ws, kmvo, OFF_KM + (unsigned)(s * NH + h) * 3072 + (unsigned)i * 1024, nosent(F[i]));
    if (s + 2 < NSEG) {
#pragma unroll
      for (int i = 0; i < 3; ++i) st8<SC1>(c.ws, kmvo, OFF_KM + (unsigned)((s + 2) * NH + h) * 3072 + (unsigned)i * 1024, u32x2{SENT, SENT});
    }
    __builtin_amdgcn_s_sleep(H_SLEEP);
  }
}

__device__ __forceinline__ void role_V(const Params& p, Ctx& c, int h) {
  u32x8 Wv[10][2];
  u32x8 Wo[10][3];
  u32x2 tabv;
  {
    rsrc_t rv = mk_rsrc(p.Wv, DM * NH * DH * 2), ro = mk_rsrc(p.Wo, DM * NH * DH * 2);
    const unsigned srcv = stage_src<1>(c, NH * DH), srco = stage_src<1>(c, DM), rd = stage_rd(c.lane, c.wave);
    stage_weights<true, 12, 2, 10, 4>(c,
      [&](auto ci, LAS unsigned char* dst) {
        constexpr int id = ci;
        if constexpr (id < 40) {
          constexpr int t = id / 4, pn = (id % 4) / 2, a = id % 2;
          dma16<0>(rv, dst, srcv, (unsigned)(((64 * t + 32 * a) * NH * DH + h * DH + pn * 64) * 2));
        } else if constexpr (id < 100) {
          constexpr int f = (id - 40) / 2, a = id % 2, t = f / 10, pn = f % 10;
          dma16<0>(ro, dst, srco, (unsigned)(((h * DH + 64 * t + 32 * a) * DM + pn * 64) * 2));
        } else {
          constexpr int sp = id - 100;
          dma16<0>(rv, dst, srcv, (unsigned)(((32 * sp) * NH * DH + h * DH + 128) * 2));
        }
      },
      [&](auto ci, LAS unsigned char* chunk) {
        constexpr int id = ci;
        if constexpr (id % 2 == 1) {
          u32x8 f = cat(stage_frag(chunk - 4096 + rd), stage_frag(chunk + rd));
          if constexpr (id < 40) Wv[id / 4][(id % 4) / 2] = f;
          else Wo[((id - 40) / 2) % 10][((id - 40) / 2) / 10] = f;
        }
      },
      [&](auto ci) {
        constexpr int id = ci;
        if constexpr (id % 2 == 1) {
          if constexpr (id < 36) pin_v(Wv[id / 4][(id % 4) / 2]);
          else if constexpr (id < 40) pin_a(Wv[id / 4][(id % 4) / 2]);
          else pin_a(Wo[((id - 40) / 2) % 10][((id - 40) / 2) / 10]);
        }
      },
      [&](auto ci) { if constexpr (ci == 80) tabv = ld_table(c); });
  }
  Sides sd;
  get_sides(c, sd, (unsigned)flag_val(c, tabv));
  LAS unsigned char* A = c.lds + c.lane * 16;
  const ResW WR = res_base(c);
  const int idx = (c.lane & 2) ? 0xEEEE : 0x4444;
  const int g = c.lane >> 4, n = c.lane & 15;
  LAS unsigned char* TW = c.lds + 10240 + c.wave * 256 + ((n & 7) * 4 + g) * 8 + (n >> 3) * 4;
  LAS unsigned char* TR = c.lds + 10240 + g * 256 + n * 8;
  const Gath ga = gather_setup(c, true, sd.me);
  unsigned mj[3], ms[3];
#pragma unroll
  for (int u = 0; u < 3; ++u) {
    const int pn = u < 2 ? 4 * u + g : 8 + (g & 1);
    mj[u] = (unsigned)(4 * pn + c.wave);
    ms[u] = pz_side(sd, (int)mj[u]);
  }
  for (int s = 0; s < NSEG; ++s) {
    unsigned mvo[3];
#pragma unroll
    for (int u = 0; u < 3; ++u) {
      mvo[u] = m_phys((unsigned)s, mj[u], ms[u]) + (unsigned)(h * 256 + n * 16);
      if (u == 2 && g >= 2) mvo[u] = 0x80000000u;
      pin_v(mvo[u]);
    }
    f32x4 acc[3] = {};
    constexpr int NW = 4, NA = 3;
    u32x8 wl[NW];
#pragma unroll
    for (int i = 0; i < NW; ++i) wl[i] = res_frag(WR, i);
#pragma unroll
    for (int i = 0; i < NW; ++i) pin_v(wl[i]);
#pragma unroll
    for (int i = 0; i < 3; ++i) pin_v(acc[i]);
    gather_mem(c, ga, s, sd.me);
    u32x4 a[NA];
#pragma unroll
    for (int t = 0; t < NA; ++t) a[t] = lds16(A + t * 1024);
    sb();
#pragma unroll
    for (int t = 0; t < 10; ++t) {
      LAS unsigned char* wb = t + NW < 5 ? WR.lo + (t + NW) * 8192 : WR.hi + (t + NW - 5) * 8192;
      acc[2] = smfma(a[t % NA], wl[t % NW], acc[2], idx);
      sb();
      if (t + NW < 10) {
        const u32x2 w0 = tr8(wb), w1 = tr8(wb + 1024);
        wl[t % NW][0] = w0.x; wl[t % NW][1] = w0.y; wl[t % NW][2] = w1.x; wl[t % NW][3] = w1.y;
      }
      sb();
      acc[0] = smfma(a[t % NA], Wv[t][0], acc[0], idx);
      sb();
      if (t + NW < 10) {
        const u32x2 w2 = tr8(wb + 4096), w3 = tr8(wb + 5120);
        wl[t % NW][4] = w2.x; wl[t % NW][5] = w2.y; wl[t % NW][6] = w3.x; wl[t % NW][7] = w3.y;
      }
      sb();
      acc[1] = smfma(a[t % NA], Wv[t][1], acc[1], idx);
      sb();
      if (t + NA < 10) a[t % NA] = lds16(A + (t + NA) * 1024);
      sb();
    }
    f32x4 m[10] = {};
    *(LAS unsigned*)(TW + 2048) = pk(acc[2][0] + acc[2][2], acc[2][1] + acc[2][3]);
    *(LAS unsigned*)(TW) = pk(acc[0][0] + acc[0][2], acc[0][1] + acc[0][3]);
    *(LAS unsigned*)(TW + 1024) = pk(acc[1][0] + acc[1][2], acc[1][1] + acc[1][3]);
    sb();
#pragma unroll
    for (int i = 0; i < 4; ++i) pin_v(m[i]);
    bar();
    u32x4 a2[3];
#pragma unroll
    for (int t = 0; t < 3; ++t) a2[t] = cat(tr8(TR + t * 1024), tr8(TR + t * 1024 + 128));
    sb();
#pragma unroll
    for (int i = 4; i < 10; ++i) pin_v(m[i]);
    sb();
    float v0, v1; unsigned x[10];
    auto xpose = [&](int u) {
      const int i2 = u < 2 ? 4 * u + 2 : 8, i3 = u < 2 ? 4 * u + 3 : 9;
      auto r0 = __builtin_amdgcn_permlane32_swap(x[4 * u], x[i2], false, false);
      auto r1 = __builtin_amdgcn_permlane32_swap(x[4 * u + 1], x[i3], false, false);
      auto q0 = __builtin_amdgcn_permlane16_swap(r0[0], r1[0], false, false);
      auto q1 = __builtin_amdgcn_permlane16_swap(r0[1], r1[1], false, false);
      st16<SC1>(c.ws, mvo[u], 0, u32x4{q0[0], q0[1], q1[0], q1[1]});
    };
    sfor<0, 36>([&](auto ic) {
      constexpr int i = ic, tl = (i / 6) * 2 + (i & 1), t = (i % 6) / 2;
      if constexpr (i < 30) { m[tl] = smfma(a2[t], Wo[tl][t], m[tl], idx); sb(); }
      else if constexpr (i == 30) { asm volatile("s_nop 3" : "+v"(m[8]), "+v"(m[9])); sb(); }
      constexpr int k = i - 7;
      if constexpr (k >= 0) {
        constexpr int pr = k / 6, w = k % 6, ta = 2 * pr + (w >= 3 ? 1 : 0), ww = w % 3;
        if constexpr (pr < 5) {
          if constexpr (ww == 0) { v0 = add1(m[ta][0], m[ta][2]); sb(); }
          if constexpr (ww == 1) { v1 = add1(m[ta][1], m[ta][3]); sb(); }
          if constexpr (ww == 2) { x[ta] = pks(v0, v1); sb(); }
          if constexpr (w == 5 && (pr & 1)) { xpose(pr >> 1); sb(); }
        }
      }
    });
    x[9] = pks(v0, v1);
    xpose(2);
    __builtin_amdgcn_s_sleep(V_SLEEP);
  }
}

__device__ __forceinline__ void role_PZ(const Params& p, Ctx& c, int j) {
  Sides sd;
  get_sides(c, sd);
  const unsigned mside = pz_side(sd, j);
  LAS unsigned char* R = c.lds;
  const int idx = (c.lane & 2) ? 0xEEEE : 0x4444;
  const int g = c.lane >> 4, n = c.lane & 15;
  int uready = -1;
  const unsigned l16 = (unsigned)c.lane * 16;
  const unsigned pmvo = (unsigned)(2 * g * 128 + sp_row(c.lane) * 16 + sp_half(c.lane) * 8);
  const u32x4 sv = {SENT, SENT, SENT, SENT};
  LAS float* rw = (LAS float*)(R + c.wave * 512) + 2 * g * 16 + n;
  LAS unsigned char* rp = R + (((c.lane >> 2) & 7) * 16 + (c.lane >> 5) * 8 + (c.lane & 3) * 2) * 4;
  const unsigned olo = (unsigned)(j * 256 + (c.lane >> 2) * 16);
  const bool ringer = j % (40 / NBELL) == 0 && j / (40 / NBELL) < NBELL && c.wave == 3;
  const unsigned bvo = bell_vo(c, 0, sd.me);
  const unsigned rvo = (ringer && c.lane < 2) ? OFF_DB + (unsigned)(16 + j / (40 / NBELL)) * FS + (unsigned)c.lane * 8192u : 0x80000000u;
  bar();
  for (int s = 0; s < NSEG - 1; ++s) {
    if (uready < s) uready = wait_flags<2>(c, FL_U + 8 * FS * c.wave, 8, s);
    u32x4 u[8];
#pragma unroll
    for (int i = 0; i < 8; ++i) u[i] = ld16<SC1>(c.ws, l16, OFF_U + (unsigned)s * U_SZ + (unsigned)(j * 32 + 8 * c.wave + i) * 1024);
    u32x4 pa[4], mb[2]; u32x2 pm[2];
    const unsigned lp = L_P + (unsigned)s * P_SZ;
    unsigned sop = xr_phys(lp + (unsigned)c.wave * 4096, sd.me), som5 = xr_phys(lp + 16384 + (unsigned)c.wave * 1024, sd.me);
    pin_s(sop); pin_s(som5);
    unsigned som = m_phys((unsigned)s, (unsigned)j, mside) + (unsigned)c.wave * 2048;
    unsigned ovo = xr_phys(L_MEM + (unsigned)(s + 1) * MEM_SZ + olo, c.lane & 1);
    if ((c.lane & 2) || c.wave != 0) ovo = 0x80000000u;
    f32x4 acc;
    LAS float* rwb = rw + (s & 1) * 512;
    LAS unsigned char* rpb = rp + (s & 1) * 2048;
    pin_v(ovo); pin_v(rwb); pin_v(rpb);
    unsigned sp = 0;
    pin_s(som);
    wait_bells(c, bvo, s);
    mb[0] = ld16<SC1>(c.ws, l16, som); mb[1] = ld16<SC1>(c.ws, l16 + 1024, som);
    sb();
    __builtin_amdgcn_s_sleep(BELL_P_GAP);
    for (;;) {
      pm[0] = ld8<SC1>(c.ws, pmvo, som5); pm[1] = ld8<SC1>(c.ws, pmvo + 128, som5);
#pragma unroll
      for (int i = 0; i < 4; ++i) pa[i] = ld16<SC1>(c.ws, l16 + i * 1024, sop);
      sb();
      acc = f32x4{0.f, 0.f, 0.f, 0.f};
      pin_v(acc);
      acc = smfma(cat(pm[0], pm[1]), cat(mb[0], mb[1]), acc, idx);
      unsigned m = umax4(0u, cat(pm[0], pm[1]));
      m = umax4(umax4(m, mb[0]), mb[1]);
      sb();
#pragma unroll
      for (int i = 0; i < 4; ++i) {
        acc = smfma(pa[i], cat(u[2 * i], u[2 * i + 1]), acc, idx); m = umax4(m, pa[i]); sb();
      }
      if (!__any(m == SENT)) break;
      spin_check(c, sp);
      mb[0] = ld16<SC1>(c.ws, l16, som); mb[1] = ld16<SC1>(c.ws, l16 + 1024, som);
    }
    rwb[0] = acc[0] + acc[2]; rwb[16] = acc[1] + acc[3];
    sb();
    st4<SC1>(c.ws, rvo, 0, (unsigned)(s + 1));
    bar();
    {
      const f32x2 v = (*(LAS f32x2*)(rpb) + *(LAS f32x2*)(rpb + 512)) + (*(LAS f32x2*)(rpb + 1024) + *(LAS f32x2*)(rpb + 1536));
      const unsigned d = pks(v[0], v[1]);
      st16<SC1>(c.ws, ovo, 0, u32x4{dppu<0x00>(d), dppu<0x55>(d), dppu<0xAA>(d), dppu<0xFF>(d)});
    }
    if (uready < s + 1) uready = wait_flags<2>(c, FL_U + 8 * FS * c.wave, 8, s + 1);
    else __builtin_amdgcn_s_sleep(PZ_SLEEP);
    if (c.wave == 0 && !(c.lane & 2) && s + 3 < NSEG) st16<SC1>(c.ws, xr_phys(L_MEM + (unsigned)(s + 3) * MEM_SZ + olo, c.lane & 1), 0, sv);
    if (s + 2 < NSEG) {
      const unsigned sa = m_phys((unsigned)(s + 2), (unsigned)j, mside) + (unsigned)c.wave * 2048;
      st16<SC1>(c.ws, l16, sa, sv); st16<SC1>(c.ws, l16 + 1024, sa, sv);
    }
  }
}

__device__ __forceinline__ void load_proj_w(Ctx& c, const void* Wm, int h, u32x4 (&W)[3][20]) {
  rsrc_t rw = mk_rsrc(Wm, DM * NH * DH * 2);
  const unsigned src = stage_src<0>(c, NH * DH), rd = stage_rd(c.lane, c.wave);
  stage_weights<true, 5, 0, 12, 3>(c,
    [&](auto ci, LAS unsigned char* dst) {
      constexpr int id = ci, kap = id / 3, pn = id % 3;
      dma16<0>(rw, dst, src, (unsigned)(((32 * kap) * NH * DH + h * DH + pn * 64) * 2));
    },
    [&](auto ci, LAS unsigned char* chunk) {
      constexpr int id = ci;
      W[id % 3][id / 3] = stage_frag(chunk + rd);
    },
    [&](auto ci) { constexpr int id = ci; pin_a(W[id % 3][id / 3]); }, [](auto) {});
}
__device__ __forceinline__ void dma_hb(const Ctx& c, int s, LAS unsigned char* slot) {
#pragma unroll
  for (int i = 0; i < 10; ++i)
    dma16<SC1>(c.ws, slot + (c.wave * 10 + i) * 1024, (unsigned)c.lane * 16, OFF_HB + (unsigned)s * HB_SZ + (unsigned)(c.wave * 10 + i) * 1024);
}
template <bool WA, bool WFIRST, bool ZERO>
__device__ __forceinline__ void mfma_v(f32x4& d, const u32x4& w, const u32x4& x) {
  if constexpr (ZERO) {
    if constexpr (WA && WFIRST) asm volatile("v_mfma_f32_16x16x32_bf16 %0, %1, %2, 0" : "=&v"(d) : "a"(w), "v"(x));
    else if constexpr (WA) asm volatile("v_mfma_f32_16x16x32_bf16 %0, %2, %1, 0" : "=&v"(d) : "a"(w), "v"(x));
    else if constexpr (WFIRST) asm volatile("v_mfma_f32_16x16x32_bf16 %0, %1, %2, 0" : "=&v"(d) : "v"(w), "v"(x));
    else asm volatile("v_mfma_f32_16x16x32_bf16 %0, %2, %1, 0" : "=&v"(d) : "v"(w), "v"(x));
  } else {
    if constexpr (WA && WFIRST) asm volatile("v_mfma_f32_16x16x32_bf16 %0, %1, %2, %0" : "+v"(d) : "a"(w), "v"(x));
    else if constexpr (WA) asm volatile("v_mfma_f32_16x16x32_bf16 %0, %2, %1, %0" : "+v"(d) : "a"(w), "v"(x));
    else if constexpr (WFIRST) asm volatile("v_mfma_f32_16x16x32_bf16 %0, %1, %2, %0" : "+v"(d) : "v"(w), "v"(x));
    else asm volatile("v_mfma_f32_16x16x32_bf16 %0, %2, %1, %0" : "+v"(d) : "v"(w), "v"(x));
  }
}
__device__ __forceinline__ void mfma_vn(f32x4& d, const u32x4& a, const u32x4& b) {
  asm volatile("s_nop 1\n\tv_mfma_f32_16x16x32_bf16 %0, %1, %2, %0" : "+v"(d) : "v"(a), "v"(b));
}
__device__ __forceinline__ void mfma_fence() { sb(); asm volatile("s_nop 7"); sb(); }

template <int NT, int K0, int K1, class Hook>
__device__ __forceinline__ void gemm_part(const u32x4 (&W)[NT][20], LAS unsigned char* hb, f32x4 (&acc)[NT][2], Hook&& hook) {
  u32x4 b[K1 - K0][2];
#pragma unroll
  for (int k = 0; k < 2; ++k) { b[k][0] = lds16(hb + (2 * (K0 + k)) * 1024); b[k][1] = lds16(hb + (2 * (K0 + k) + 1) * 1024); }
  sb();
  sfor<0, K1 - K0>([&](auto kc) {
    constexpr int k = kc, ka = K0 + k;
    sfor<0, 2 * NT>([&](auto ic) {
      constexpr int t = ic / NT, i = ic % NT;
      mfma_v<(ka * NT + i < 64), true, ka == 0>(acc[i][t], W[i][ka], b[k][t]);
      sb();
      if constexpr (ic < 2 && k + 2 < K1 - K0) { b[k + 2][ic] = lds16(hb + (2 * (ka + 2) + ic) * 1024); sb(); }
      if constexpr (ic == 2) { hook(kc); sb(); }
    });
  });
}
__device__ __forceinline__ void dma_hb1(const Ctx& c, int s, LAS unsigned char* slot, int i) {
  dma16<SC1>(c.ws, slot + (c.wave * 10 + i) * 1024, (unsigned)c.lane * 16, OFF_HB + (unsigned)s * HB_SZ + (unsigned)(c.wave * 10 + i) * 1024);
}
__device__ __forceinline__ u32x4 pack2(const f32x4& a, const f32x4& b) { return u32x4{pk(a[0], a[1]), pk(a[2], a[3]), pk(b[0], b[1]), pk(b[2], b[3])}; }

__device__ __forceinline__ void need_hb(const Ctx& c, unsigned long long& mask, int s) {
  if (c.wave != 0 || ((mask >> s) & 1ull)) return;
  unsigned sp = 0;
  for (;;) {
    mask = __ballot(ld_flag(c, FL_H + FS * (unsigned)c.lane) >= 0);
    if ((mask >> s) & 1ull) return;
    __builtin_amdgcn_s_sleep(4);
    spin_check(c, sp);
  }
}

__device__ __forceinline__ void role_KV(const Params& p, Ctx& c, int h, bool isV) {
  u32x4 W[3][20];
  load_proj_w(c, isV ? p.Wv : p.Wk, h, W);
  unsigned long long hm = 0;
  need_hb(c, hm, 0);
  bar();
  const unsigned obase = isV ? OFF_VX : OFF_KX, fl = (isV ? FL_V : FL_K) + FS * h;
  dma_hb(c, 0, c.lds);
  need_hb(c, hm, 1);
  bar();
  dma_hb(c, 1, c.lds + HB_SZ);
  auto kst = [&](int s, int i, const f32x4 (&a)[3][2]) {
    st16<SC1>(c.ws, (unsigned)c.lane * 16, obase + (unsigned)(s * NH + h) * KX_SZ + (unsigned)(4 * i + c.wave) * 1024, pack2(a[i][0], a[i][1]));
  };
  f32x4 accA[3][2], accB[3][2];
#pragma unroll 1
  for (int s = 0; s < 2; ++s) {
    need_hb(c, hm, s + 2);
    if (s == 0) wait_vm<10>();
    bar();
    gemm_part<3, 0, 20>(W, c.lds + s * HB_SZ + c.lane * 16, accA, [&](auto k) { if constexpr (k < 10) dma_hb1(c, s + 2, c.lds + (s == 0 ? 2 : 0) * HB_SZ, k); });
    mfma_fence();
#pragma unroll
    for (int i = 0; i < 3; ++i) kst(s, i, accA);
    publish(c, fl, s);
  }
  int sl = 2;
  auto seg = [&](int s, f32x4 (&cur)[3][2], const f32x4 (&prev)[3][2], bool hasprev) {
    const int s2 = min(s + 2, NSEG - 1);
    need_hb(c, hm, s2);
    wait_vm<10>();
    bar();
    if (c.tid == 0 && s >= 4) st_flag(c, fl, s - 2);
    const int sl2 = sl == 0 ? 2 : sl - 1;
    gemm_part<3, 0, 20>(W, c.lds + sl * HB_SZ + c.lane * 16, cur, [&](auto k) {
      if constexpr (k >= 2 && k < 5) { if (hasprev) kst(s - 1, k - 2, prev); }
      if constexpr (k >= 6 && k < 16) dma_hb1(c, s2, c.lds + sl2 * HB_SZ, k - 6);
    });
    sl = sl == 2 ? 0 : sl + 1;
  };
#pragma unroll 1
  for (int s = 2; s < NSEG; s += 2) { seg(s, accA, accB, s > 2); seg(s + 1, accB, accA, true); }
  mfma_fence();
#pragma unroll
  for (int i = 0; i < 3; ++i) kst(NSEG - 1, i, accB);
  publish(c, fl, NSEG - 1);
}

__device__ __forceinline__ void role_U(const Params& p, Ctx& c, int h) {
  u32x4 W[10][6];
  {
    rsrc_t ro = mk_rsrc(p.Wo, DM * NH * DH * 2);
    const unsigned src = stage_src<2>(c, DM), rd = stage_rd(c.lane, c.wave);
    stage_weights<true, 5, 0, 12, 3>(c,
      [&](auto ci, LAS unsigned char* dst) {
        constexpr int id = ci, a = id / 10, pn = id % 10;
        dma16<0>(ro, dst, src, (unsigned)(((h * DH + 32 * a) * DM + pn * 64) * 2));
      },
      [&](auto ci, LAS unsigned char* chunk) {
        constexpr int id = ci;
        W[id % 10][id / 10] = stage_frag(chunk + rd);
      },
      [&](auto ci) { constexpr int id = ci; pin_a(W[id % 10][id / 10]); }, [](auto) {});
  }
  const int g = c.lane >> 4, rp = c.lane & 15;
  const int gs = rp >> 2, i4 = rp & 3;
  const unsigned aoff = (unsigned)((g * 16 + 8 * (i4 >> 1) + 4 * (gs & 1) + (i4 & 1)) * 16 + (gs >> 1) * 8);
  const unsigned uvo = (unsigned)((h >> 1) * 2048 + (g >> 1) * 1024 + ((h & 1) * 2 + (g & 1)) * 256 + rp * 16);
  int vready = -1, issued = -1;
  auto dma_v = [&](int s) {
    LAS unsigned char* slot = c.lds + (s % 3) * KX_SZ;
#pragma unroll
    for (int i = 0; i < 3; ++i)
      dma16<SC1>(c.ws, slot + (c.wave * 3 + i) * 1024, (unsigned)c.lane * 16, OFF_VX + (unsigned)(s * NH + h) * KX_SZ + (unsigned)(c.wave * 3 + i) * 1024);
  };
  for (int s = 0; s < NSEG; ++s) {
    if (issued < s) {
      unsigned sp = 0;
      while ((vready = __builtin_amdgcn_readfirstlane(ld_flag(c, FL_V + FS * h))) < s) { __builtin_amdgcn_s_sleep(2); spin_check(c, sp); }
      dma_v(s); issued = s;
      wait_vm<0>();
    } else {
      wait_vm<10>();
    }
    bar();
    if (c.tid == 0 && s >= U_EAGER + 2) st_flag(c, FL_U + FS * h, s - 2);
    if (s + 1 < NSEG && vready >= s + 1) { dma_v(s + 1); issued = s + 1; }
    const u32x2 fv = ld_flag_raw(c, vready < min(s + 2, NSEG - 1) ? FL_V + FS * h : 0x80000000u);
    LAS unsigned char* sa0 = c.lds + (s % 3) * KX_SZ + aoff;
    LAS unsigned char* sa1 = sa0 + 32;
    pin_v(sa0); pin_v(sa1);
    u32x4 a[2][6];
#pragma unroll
    for (int k = 0; k < 6; ++k) {
      a[0][k] = cat(*(LAS u32x2*)(sa0 + (2 * k) * 1024), *(LAS u32x2*)(sa0 + (2 * k + 1) * 1024));
      a[1][k] = cat(*(LAS u32x2*)(sa1 + (2 * k) * 1024), *(LAS u32x2*)(sa1 + (2 * k + 1) * 1024));
    }
    sb();
    f32x4 d[10][2];
    u32x4 o;
    sfor<0, 10>([&](auto pc) {
      constexpr int pn = pc;
      sfor<0, 12>([&](auto ic) {
        constexpr int k = ic / 2, tp = ic % 2;
        mfma_v<true, false, k == 0>(d[pn][tp], W[pn][k], a[tp][k]);
        sb();
        if constexpr (pn > 0 && ic >= 7 && ic < 11) { constexpr int q = ic - 7; o[q] = pk(d[pn - 1][q / 2][2 * (q % 2)], d[pn - 1][q / 2][2 * (q % 2) + 1]); sb(); }
        if constexpr (pn > 0 && ic == 11) {
          st16<SC1>(c.ws, uvo, OFF_U + (unsigned)s * U_SZ + (unsigned)(4 * (pn - 1) + c.wave) * 32768, o);
          sb();
        }
      });
    });
    mfma_fence();
    st16<SC1>(c.ws, uvo, OFF_U + (unsigned)s * U_SZ + (unsigned)(36 + c.wave) * 32768, pack2(d[9][0], d[9][1]));
    {
      vready = max(vready, __builtin_amdgcn_readfirstlane(flag_val(c, fv)));
    }
    if (s < U_EAGER) {
      if (issued == s && vready >= s + 1) { dma_v(s + 1); issued = s + 1; }
      publish(c, FL_U + FS * h, s);
    }
  }
  publish(c, FL_U + FS * h, NSEG - 1);
}

__device__ __forceinline__ void role_Q(const Params& p, Ctx& c, int h) {
  const int g = c.lane >> 4, n = c.lane & 15;
  const int qt = c.wave * 8 + (c.lane >> 3), tau = qt >> 4, rho = qt & 15, it0 = c.lane & 7, it = min(it0, 4);
  const int T = it >> 1;
  const bool dead = (it0 > 4) || (tau == 0 && it >= 2 && it < 4);
  const unsigned pvo = dead ? 0x80000000u : it < 4 ? (unsigned)(((h >> 1) * 3 + tau + (tau ? T : 0)) * 1024 + (((h & 1) * 2 + (it & 1)) * 16 + rho) * 16)
                                                   : (unsigned)(65536 + ((h >> 2) * 2 + tau) * 1024 + ((h & 3) * 16 + rho) * 16);
  const u32x4 sv = {SENT, SENT, SENT, SENT};
  st16<SC1>(c.ws, pvo, OFF_PX, sv); st16<SC1>(c.ws, pvo, OFF_PX + PX_SZ, sv);
  u32x4 W[3][20];
  load_proj_w(c, p.Wq, h, W);
  if (c.tid == 0) st_flag(c, FL_PX + FS * h, 0);
  {
    u32x4 t[45];
#pragma unroll
    for (int m = 0; m < 3; ++m) {
      rsrc_t r = mk_rsrc(m == 0 ? p.wg : m == 1 ? p.wu : p.wd, DM * DFF * 2);
#pragma unroll
      for (int i = 0; i < 15; ++i) t[m * 15 + i] = ld16<0>(r, (unsigned)c.tid * 16u + i * 4096u, (unsigned)h * 61440u);
    }
#pragma unroll
    for (int i = 0; i < 45; ++i) asm volatile("" :: "v"(t[i]));
  }
  LAS unsigned char* PART = c.lds + 2 * HB_SZ;
  constexpr int QS = 336, WS_ = 16 * QS, BS_ = 4 * WS_;
  const int tile = it == 4 ? (tau ? 4 : 1) : (tau ? 2 + T : 0);
  const int ga = it == 4 ? 0 : (it & 1), gb = it == 4 ? 1 : (it & 1) + 2;
  const int pao = rho * QS + tile * 64 + ga * 16, pbo = rho * QS + tile * 64 + gb * 16;
  LAS unsigned char* PW = PART + c.wave * WS_ + n * QS + g * 16;
  f32x4 ba, bb;
#pragma unroll
  for (int r = 0; r < 4; ++r) {
    ba[r] = (dead || (it < 4 && 16 * T + 4 * ga + r > qt)) ? -__builtin_inff() : 0.f;
    bb[r] = (dead || (it < 4 && 16 * T + 4 * gb + r > qt)) ? -__builtin_inff() : 0.f;
  }
  if (c.wave == 0) wait_flags<8>(c, FL_H, 64, 0);
  if (c.wave == 1) wait_flags<8>(c, FL_KM + FS * h, 1, 0);
  bar();
  dma_hb(c, 0, c.lds); dma_hb(c, 1, c.lds + HB_SZ);
  wait_vm<10>();
  bar();
  u32x4 q[3], kx[3];
  int kready = -1;
  auto load_kx = [&](int s, int i) {
    kx[i] = ld16<SC1>(c.ws, (unsigned)c.lane * 16, OFF_KX + (unsigned)(s * NH + h) * KX_SZ + (unsigned)(4 * i + c.wave) * 1024);
  };
  auto xscores = [&](int s) {
    f32x4 s0 = {}, s2 = {}, s3 = {};
#pragma unroll
    for (int i = 0; i < 3; ++i) {
      const u32x2 q0 = {q[i].x, q[i].y}, q1 = {q[i].z, q[i].w}, k0 = {kx[i].x, kx[i].y}, k1 = {kx[i].z, kx[i].w};
      s0 = mfma16(k0, q0, s0); s2 = mfma16(k0, q1, s2); s3 = mfma16(k1, q1, s3);
    }
    LAS unsigned char* pw = PW + (s & 1) * BS_;
    *(LAS f32x4*)(pw) = s0; *(LAS f32x4*)(pw + 128) = s2; *(LAS f32x4*)(pw + 192) = s3;
  };
  {
    f32x4 acc[3][2];
    gemm_part<3, 0, 20>(W, c.lds + c.lane * 16, acc, [](auto) {});
    mfma_fence();
#pragma unroll
    for (int i = 0; i < 3; ++i) q[i] = pack2(acc[i][0], acc[i][1]);
    kready = wait_flags<8>(c, FL_K + FS * h, 1, 0);
#pragma unroll
    for (int i = 0; i < 3; ++i) load_kx(0, i);
    xscores(0);
  }
  const unsigned kmvo = (unsigned)(g * 64 + (n & 7) * 8 + c.wave * 256);
#pragma unroll 1
  for (int s = 0; s < NSEG; ++s) {
    u32x2 km[3];
    unsigned sp = 0;
    for (;;) {
#pragma unroll
      for (int i = 0; i < 3; ++i) km[i] = ld8<SC1>(c.ws, kmvo, OFF_KM + (unsigned)(s * NH + h) * 3072 + (unsigned)i * 1024);
      if (!__any(umax3(umax3(km[0].x, km[0].y, km[1].x), umax3(km[1].y, km[2].x, km[2].y), 0u) == SENT)) break;
      __builtin_amdgcn_s_sleep(3);
      spin_check(c, sp);
    }
    wait_vm<0>();
    LAS unsigned char* PB = PART + (s & 1) * BS_;
    {
      f32x4 s1 = {}, s4 = {};
#pragma unroll
      for (int i = 0; i < 3; ++i) { s1 = mfma16(km[i], u32x2{q[i].x, q[i].y}, s1); s4 = mfma16(km[i], u32x2{q[i].z, q[i].w}, s4); }
      LAS unsigned char* pw = PW + (s & 1) * BS_;
      *(LAS f32x4*)(pw + 64) = s1; *(LAS f32x4*)(pw + 256) = s4;
    }
    bar();
    {
      LAS unsigned char* pa = PB + pao;
      LAS unsigned char* pb = PB + pbo;
      f32x4 va = ((*(LAS f32x4*)pa + ba) + *(LAS f32x4*)(pa + WS_)) + (*(LAS f32x4*)(pa + 2 * WS_) + *(LAS f32x4*)(pa + 3 * WS_));
      f32x4 vb = ((*(LAS f32x4*)pb + bb) + *(LAS f32x4*)(pb + WS_)) + (*(LAS f32x4*)(pb + 2 * WS_) + *(LAS f32x4*)(pb + 3 * WS_));
      float m = fmaxf(fmaxf(fmaxf(va[0], va[1]), fmaxf(va[2], va[3])), fmaxf(fmaxf(vb[0], vb[1]), fmaxf(vb[2], vb[3])));
      asm volatile("s_nop 1\n\tv_max_f32_dpp %0, %0, %0 quad_perm:[1,0,3,2] row_mask:0xf bank_mask:0xf\n\t"
                   "s_nop 1\n\tv_max_f32_dpp %0, %0, %0 quad_perm:[2,3,0,1] row_mask:0xf bank_mask:0xf\n\t"
                   "s_nop 1\n\tv_max_f32_dpp %0, %0, %0 row_half_mirror row_mask:0xf bank_mask:0xf" : "+v"(m));
      const float cs = 0.07216878364870322f * 1.4426950408889634f;
      float l = 0.f;
#pragma unroll
      for (int r = 0; r < 4; ++r) {
        va[r] = __builtin_amdgcn_exp2f(add1(va[r], -m) * cs);
        vb[r] = __builtin_amdgcn_exp2f(add1(vb[r], -m) * cs);
        l += va[r] + vb[r];
      }
      l += dpp<0xB1>(l); l += dpp<0x4E>(l); l += dpp<0x141>(l);
      const float inv = __builtin_amdgcn_rcpf(l);
      st16<SC1>(c.ws, pvo, OFF_PX + (unsigned)s * PX_SZ,
                u32x4{pks(va[0] * inv, va[1] * inv), pks(vb[0] * inv, vb[1] * inv), pks(va[2] * inv, va[3] * inv), pks(vb[2] * inv, vb[3] * inv)});
    }
    if (c.tid == 0) st_flag(c, FL_PX + FS * h, s + 1);
    if (s + 1 < NSEG) {
      if (s + 2 < NSEG) st16<SC1>(c.ws, pvo, OFF_PX + (unsigned)(s + 2) * PX_SZ, sv);
      if (kready < s + 1) kready = wait_flags<8>(c, FL_K + FS * h, 1, s + 1);
      const int s2 = min(s + 2, NSEG - 1);
      f32x4 acc[3][2];
      gemm_part<3, 0, 20>(W, c.lds + ((s + 1) & 1) * HB_SZ + c.lane * 16, acc, [&](auto k) {
        if constexpr (k < 3) load_kx(s + 1, k);
        if constexpr (k >= 3 && k < 13) dma_hb1(c, s2, c.lds + (s & 1) * HB_SZ, k - 3);
      });
      mfma_fence();
#pragma unroll
      for (int i = 0; i < 3; ++i) q[i] = pack2(acc[i][0], acc[i][1]);
      xscores(s + 1);
    }
  }
}

constexpr int X1_ARM = 6;
__device__ __forceinline__ void role_PX(const Params& p, Ctx& c, int jx) {
  LAS float* ga = (LAS float*)c.lds;
  LAS unsigned char* R = c.lds + 4096;
  const int g = c.lane >> 4, n = c.lane & 15;
  const unsigned l16 = (unsigned)c.lane * 16;
  const int nt = c.wave & 1, tau = c.wave >> 1;
  const int col = 32 * jx + 16 * nt + 4 * g;
  const unsigned xo = (unsigned)(((tau * 16 + n) * DM + col) * 2);
  const unsigned x1o = (unsigned)((jx * 2 + tau) * 1024 + ((2 * nt + (g >> 1)) * 16 + n) * 16 + (g & 1) * 8);
#pragma unroll
  for (int i = 0; i < X1_ARM; ++i) st8<SC1>(c.ws, x1o, OFF_X1 + (unsigned)i * HB_SZ, u32x2{SENT, SENT});
  publish(c, FL_X1 + FS * jx, 0);
  Sides sd;
  get_sides(c, sd);
  load_mod(c, 2 * DM, ga, false);
  wait_flags<8>(c, FL_PX + 8 * FS * c.wave, 8, 0);
  bar();
  const f32x4 gav = *(LAS f32x4*)(ga + col);
  rsrc_t rx = mk_rsrc(p.x, 2048 * DM * 2);
  const unsigned ms0 = pz_side(sd, 2 * jx), ms1 = pz_side(sd, 2 * jx + 1);
  int uready = -1, seen = -1;
  auto ld_u = [&](unsigned vo, int s, int i) -> u32x4 {
    return ld16<SC1>(c.ws, vo, OFF_U + (unsigned)s * U_SZ + (unsigned)((2 * jx + (i & 1)) * 32 + 8 * c.wave + (i >> 1)) * 1024);
  };
  auto step = [&](int s, const u32x4 (&z)[16], u32x4 (&zn)[16]) {
    const int sn = min(s + 1, NSEG - 1);
    if (uready < sn) uready = wait_flags<8>(c, FL_U + 8 * FS * c.wave, 8, sn);
    const unsigned spx = OFF_PX + (unsigned)s * PX_SZ;
    const unsigned uvo = s + 1 < NSEG ? l16 : 0x80000000u;
    if (seen < s + 1) seen = doorbell<4, PX_DELAY>(c, FL_PX, s + 1);
    const unsigned m0 = m_phys((unsigned)s, (unsigned)(2 * jx), ms0) + (unsigned)c.wave * 2048, m1 = m_phys((unsigned)s, (unsigned)(2 * jx + 1), ms1) + (unsigned)c.wave * 2048;
    u32x4 pa[12], pm[2][2], zm[2][2];
    f32x4 acc[2][2];
    const u32x2 xv = ld8<0>(rx, xo, (unsigned)s * HB_SZ);
    unsigned sp = 0;
    for (;;) {
#pragma unroll
      for (int i = 0; i < 2; ++i) {
        zm[i][0] = ld16<SC1>(c.ws, l16 + i * 1024, m0); zm[i][1] = ld16<SC1>(c.ws, l16 + i * 1024, m1);
#pragma unroll
        for (int t = 0; t < 2; ++t) pm[i][t] = ld16<SC1>(c.ws, l16, spx + 65536 + (unsigned)((2 * c.wave + i) * 2 + t) * 1024);
      }
#pragma unroll
      for (int i = 0; i < 12; ++i) pa[i] = ld16<SC1>(c.ws, l16, spx + (unsigned)(12 * c.wave + i) * 1024);
      sb();
      unsigned m = 0;
      sfor<0, 8>([&](auto ic) {
        constexpr int k = ic >> 2, t = (ic >> 1) & 1, u = ic & 1;
        mfma_v<false, true, k == 0>(acc[t][u], zm[k][t], pm[k][u]);
        sb();
        if constexpr (u == 0) m = umax4(m, zm[k][t]);
        if constexpr (t == 1) m = umax4(m, pm[k][u]);
        sb();
      });
      sfor<0, 24>([&](auto ic) {
        constexpr int f = ic >> 1, t = ic & 1, pr = f / 3, w = f % 3;
        mfma_v<false, true, false>(acc[t][w ? 1 : 0], z[2 * (2 * pr + (w == 2 ? 1 : 0)) + t], pa[f]);
        sb();
        if constexpr (t == 0) m = umax4(m, pa[f]);
        if constexpr (ic >= 8) zn[ic - 8] = ld_u(uvo, sn, ic - 8);
        sb();
      });
      if (!__any(m == SENT)) break;
      wait_flags<4>(c, FL_PX + 8 * FS * c.wave, 8, s + 1);
      __builtin_amdgcn_s_sleep(2);
      spin_check(c, sp);
    }
    const u32x2 fnext = ld_flag_raw(c, FL_PX);
    mfma_fence();
    LAS unsigned char* Rb = R + (s & 1) * 16384;
#pragma unroll
    for (int t = 0; t < 2; ++t)
#pragma unroll
      for (int u = 0; u < 2; ++u) *(LAS f32x4*)(Rb + c.wave * 4096 + (u * 2 + t) * 1024 + l16) = acc[t][u];
    bar();
    LAS unsigned char* rp = Rb + c.wave * 1024 + l16;
    const f32x4 v = (*(LAS f32x4*)rp + *(LAS f32x4*)(rp + 4096)) + (*(LAS f32x4*)(rp + 8192) + *(LAS f32x4*)(rp + 12288));
    const float o0 = bf_lo(xv.x) + rbf(gav[0] * rbf(v[0])), o1 = bf_hi(xv.x) + rbf(gav[1] * rbf(v[1]));
    const float o2 = bf_lo(xv.y) + rbf(gav[2] * rbf(v[2])), o3 = bf_hi(xv.y) + rbf(gav[3] * rbf(v[3]));
    st8<SC1>(c.ws, x1o, OFF_X1 + (unsigned)s * HB_SZ, u32x2{pks(o0, o1), pks(o2, o3)});
    if (c.tid == 0) st_flag(c, FL_X1 + FS * jx, s + 1);
    if (s + X1_ARM < NSEG) st8<SC1>(c.ws, x1o, OFF_X1 + (unsigned)(s + X1_ARM) * HB_SZ, u32x2{SENT, SENT});
    seen = max(seen, __builtin_amdgcn_readfirstlane(flag_val(c, fnext)));
    if (s == 1 && jx == 0 && c.tid < 2) { const unsigned eo = c.tid ? FL_EPOCH2 : FL_EPOCH; st4<SC1>(c.ws, eo, 0, ld4<SC1>(c.ws, eo, 0) + 1u); }
  };
  u32x4 za[16], zb[16];
  uready = wait_flags<8>(c, FL_U + 8 * FS * c.wave, 8, 0);
#pragma unroll
  for (int i = 0; i < 16; ++i) za[i] = ld_u(l16, 0, i);
#pragma unroll 1
  for (int s = 0; s < NSEG; s += 2) { step(s, za, zb); step(s + 1, zb, za); }
}

__device__ __forceinline__ int ffn_seg(int rep, int i) { return (i >> 1) * 4 + (rep & 2) + (i & 1); }

__device__ __forceinline__ void role_F1(const Params& p, Ctx& c, int rep, int f1) {
  const u32x4 sv = {SENT, SENT, SENT, SENT};
  const int tau = rep & 1, g = c.lane >> 4, rho = c.lane & 15;
  const unsigned avo = (unsigned)(c.lane * 16 + (f1 * 4 + c.wave) * 2048 + tau * 1024);
  st16<SC1>(c.ws, avo, OFF_ACT + (unsigned)ffn_seg(rep, 0) * ACT_SZ, sv);
  st16<SC1>(c.ws, avo, OFF_ACT + (unsigned)ffn_seg(rep, 1) * ACT_SZ, sv);
  LAS float* shiftv = (LAS float*)(c.lds + 131072);
  LAS float* scalev = (LAS float*)(c.lds + 131072 + 4096);
  bar();
  load_mod(c, 3 * DM, shiftv, false);
  load_mod(c, 4 * DM, scalev, true);
  u32x4 W[4][20];
  f32x4 bias[4] = {}, csum[4] = {};
  {
    rsrc_t rg = mk_rsrc(p.wg, DM * DFF * 2), ru = mk_rsrc(p.wu, DM * DFF * 2);
    const unsigned src = stage_src<0>(c, DFF), rd = stage_rd(c.lane, c.wave);
    const u32x4 ones = {0x3F803F80u, 0x3F803F80u, 0x3F803F80u, 0x3F803F80u};
    stage_weights<false, 10, 0, 8, 4>(c,
      [&](auto ci, LAS unsigned char* dst) {
        constexpr int id = ci, kap = id / 4, pn = id % 4;
        dma16<0>(pn < 2 ? rg : ru, dst, src, (unsigned)(((32 * kap) * DFF + f1 * 128 + (pn & 1) * 64) * 2));
      },
      [&](auto ci, LAS unsigned char* chunk) {
        constexpr int id = ci;
        W[id % 4][id / 4] = stage_frag(chunk + rd);
      },
      [&](auto) {},
      [&](auto ci) {
        constexpr int id = ci, kap = id / 4, i = id % 4;
        const f32x4 s0 = *(LAS f32x4*)(scalev + 32 * kap + 8 * g), s1 = *(LAS f32x4*)(scalev + 32 * kap + 8 * g + 4);
        const f32x4 h0 = *(LAS f32x4*)(shiftv + 32 * kap + 8 * g), h1 = *(LAS f32x4*)(shiftv + 32 * kap + 8 * g + 4);
        const u32x4 w = W[i][kap];
        mfma_vn(bias[i], w, pack2(h0, h1));
        W[i][kap] = u32x4{pk(bf_lo(w.x) * s0[0], bf_hi(w.x) * s0[1]), pk(bf_lo(w.y) * s0[2], bf_hi(w.y) * s0[3]),
                          pk(bf_lo(w.z) * s1[0], bf_hi(w.z) * s1[1]), pk(bf_lo(w.w) * s1[2], bf_hi(w.w) * s1[3])};
        mfma_vn(csum[i], W[i][kap], ones);
        if constexpr (id < 64) pin_a(W[i][kap]); else pin_v(W[i][kap]);
      });
  }
  if (c.tid == 0) st_flag(c, FL_ACT + FS * (16 * rep + f1), 0);
  LAS unsigned char* HB = c.lds;
  LAS float* red = (LAS float*)(c.lds + 40960);
  LAS float* rw = red + rho * 20 + c.wave * 4 + g;
  LAS float* rr = red + rho * 20;
  wait_flags<8>(c, FL_X1, 20, 0);
  const unsigned vo = (unsigned)((10 * c.wave + tau) * 1024 + c.lane * 16);
  unsigned ones_u = 0x3F803F80u;
  pin_v(ones_u);
  auto sum16 = [&](LAS float* q) {
    const f32x4 t = (*(LAS f32x4*)q + *(LAS f32x4*)(q + 4)) + (*(LAS f32x4*)(q + 8) + *(LAS f32x4*)(q + 12));
    return (t[0] + t[1]) + (t[2] + t[3]);
  };
  u32x4 rawn[5];
  bool pre = false;
  int seen = -1;
#pragma unroll 1
  for (int it = 0; it < 32; ++it) {
    const int s = ffn_seg(rep, it);
    const unsigned sx = OFF_X1 + (unsigned)s * HB_SZ;
    LAS unsigned char* hb = HB + (it & 1) * 20480 + c.lane * 16;
    u32x4 raw[5];
    if (!pre) {
      seen = doorbell<4, F1_DELAY>(c, FL_X1, s + 1);
#pragma unroll
      for (int i = 0; i < 5; ++i) rawn[i] = ld16<SC1>(c.ws, vo, sx + i * 2048);
      sb();
#pragma unroll
      for (int i = 0; i < 5; ++i) pin_v(rawn[i]);
    }
    unsigned sp = 0;
    for (;;) {
      unsigned m = 0;
      float s0 = 0.f, s1 = 0.f;
      sb();
#pragma unroll
      for (int i = 0; i < 5; ++i) {
        raw[i] = rawn[i];
        *(LAS u32x4*)(hb + (5 * c.wave + i) * 1024) = raw[i];
        m = umax4(m, raw[i]);
        s0 = dot2(raw[i].x, ones_u, s0); s1 = dot2(raw[i].y, ones_u, s1);
        s0 = dot2(raw[i].z, ones_u, s0); s1 = dot2(raw[i].w, ones_u, s1);
      }
      rw[0] = s0 + s1;
      if (!__any(m == SENT)) break;
      wait_flags<4>(c, FL_X1 + 5 * FS * c.wave, 5, s + 1);
      __builtin_amdgcn_s_sleep(2);
      spin_check(c, sp);
#pragma unroll
      for (int i = 0; i < 5; ++i) rawn[i] = ld16<SC1>(c.ws, vo, sx + i * 2048);
      sb();
#pragma unroll
      for (int i = 0; i < 5; ++i) pin_v(rawn[i]);
    }
    const int sn = ffn_seg(rep, it + 1);
    pre = it + 1 < 32 && seen >= sn + 2;
    const unsigned von = pre ? vo : 0x80000000u;
    bar();
    const float mean = sum16(rr) * (1.f / DM);
    f32x4 acc[4];
    float v0 = 0.f, v1 = 0.f;
    {
      u32x4 b[20];
#pragma unroll
      for (int k = 0; k < 3; ++k) b[k] = lds16(hb + k * 1024);
      sb();
      sfor<0, 20>([&](auto kc) {
        constexpr int k = kc;
        const unsigned d = raw[k / 4][k % 4];
        float a0, a1;
        mfma_v<(k * 4 + 0 < 64), true, k == 0>(acc[0], W[0][k], b[k]); sb();
        if constexpr (k + 3 < 20) b[k + 3] = lds16(hb + (k + 3) * 1024);
        a0 = bf_lo(d); a1 = bf_hi(d); sb();
        mfma_v<(k * 4 + 1 < 64), true, k == 0>(acc[1], W[1][k], b[k]); sb();
        a0 = add1(a0, -mean); a1 = add1(a1, -mean); sb();
        mfma_v<(k * 4 + 2 < 64), true, k == 0>(acc[2], W[2][k], b[k]); sb();
        asm("v_fmac_f32 %0, %1, %1" : "+v"(v0) : "v"(a0)); asm("v_fmac_f32 %0, %1, %1" : "+v"(v1) : "v"(a1)); sb();
        mfma_v<(k * 4 + 3 < 64), true, k == 0>(acc[3], W[3][k], b[k]); sb();
        if constexpr (k < 5) { rawn[k] = ld16<SC1>(c.ws, von, OFF_X1 + (unsigned)sn * HB_SZ + k * 2048); sb(); }
      });
    }
    rw[320] = v0 + v1;
    mfma_fence();
    bar();
    sb();
    const float rstd = __builtin_amdgcn_rsqf(sum16(rr + 320) * (1.f / DM) + 1e-5f);
    float a[8];
#pragma unroll
    for (int i = 0; i < 2; ++i)
#pragma unroll
      for (int r = 0; r < 4; ++r) {
        const float gv = __builtin_fmaf(__builtin_fmaf(-mean, csum[i][r], acc[i][r]), rstd, bias[i][r]);
        const float uv = __builtin_fmaf(__builtin_fmaf(-mean, csum[2 + i][r], acc[2 + i][r]), rstd, bias[2 + i][r]);
        const float z2 = gv * __builtin_fmaf(gv * gv, -2.f * 1.4426950408889634f * 0.7978845608028654f * 0.044715f, -2.f * 1.4426950408889634f * 0.7978845608028654f);
        a[4 * i + r] = gv * uv * __builtin_amdgcn_rcpf(1.f + __builtin_amdgcn_exp2f(z2));
      }
#pragma unroll
    for (int i = 0; i < 5; ++i) pin_v(rawn[i]);
    wait_vm<0>();
    st16<SC1>(c.ws, avo, OFF_ACT + (unsigned)s * ACT_SZ, u32x4{pks(a[0], a[1]), pks(a[2], a[3]), pks(a[4], a[5]), pks(a[6], a[7])});
    if (c.tid == 0) st_flag(c, FL_ACT + FS * (16 * rep + f1), it + 1);
    if (s + 4 < NSEG) st16<SC1>(c.ws, avo, OFF_ACT + (unsigned)(s + 4) * ACT_SZ, sv);
  }
}

__device__ __forceinline__ void role_F2(const Params& p, Ctx& c, int rep, int f2) {
  bar();
  u32x4 W[12][4];
  {
    rsrc_t rd_ = mk_rsrc(p.wd, DFF * DM * 2);
    const unsigned src = stage_src<3>(c, DM);
    unsigned rd[4];
#pragma unroll
    for (int u = 0; u < 4; ++u) rd[u] = stage_rd(c.lane, u) + (unsigned)c.wave * 4096;
    stage_weights<false, 4, 0, 12, 3>(c,
      [&](auto ci, LAS unsigned char* dst) {
        constexpr int id = ci;
        dma16<0>(rd_, dst, src, (unsigned)(((128 * (id / 4) + 16 * (id % 4)) * DM + f2 * 64) * 2));
      },
      [&](auto ci, LAS unsigned char* chunk) {
        constexpr int id = ci;
        if constexpr (id % 4 == 0) {
#pragma unroll
          for (int u = 0; u < 4; ++u) W[id / 4][u] = stage_frag(chunk + rd[u]);
        }
      },
      [&](auto ci) { constexpr int id = ci; pin_a(W[id / 4][id % 4]); }, [](auto) {});
  }
  LAS float* gb = (LAS float*)c.lds;
  LAS unsigned char* R = c.lds + 4096;
  load_mod(c, 5 * DM, gb, false);
  wait_flags<8>(c, FL_ACT + FS * 16 * rep, 12, 0);
  bar();
  const int tau = rep & 1, g = c.lane >> 4, n = c.lane & 15;
  const unsigned l16 = (unsigned)c.lane * 16;
  const int col = 64 * f2 + 16 * c.wave + 4 * g;
  const f32x4 gbv = *(LAS f32x4*)(gb + col);
  rsrc_t ro = mk_rsrc(p.out, 2048 * DM * 2);
  const unsigned xo = (unsigned)(((tau * 16 + n) * DM + col) * 2);
  const unsigned x1o = (unsigned)((col >> 5) * 2048 + tau * 1024 + (((col >> 3) & 3) * 16 + n) * 16 + (g & 1) * 8);
  bool pre = false;
  int seen = -1;
  auto ld_b = [&](unsigned vo, int s, int i) { return ld16<SC1>(c.ws, vo, OFF_ACT + (unsigned)s * ACT_SZ + (unsigned)((4 * i + c.wave) * 2 + tau) * 1024); };
  auto item = [&](int it, u32x4 (&b)[12], u32x4 (&bn)[12]) {
    const int s = ffn_seg(rep, it), sn = ffn_seg(rep, it + 1);
    if (!pre) {
      seen = doorbell<4, F2_DELAY>(c, FL_ACT + FS * 16 * rep, it + 1);
#pragma unroll
      for (int i = 0; i < 12; ++i) b[i] = ld_b(l16, s, i);
    }
    const u32x2 xv = ld8<SC1>(c.ws, x1o, OFF_X1 + (unsigned)s * HB_SZ);
    f32x4 acc[4];
    unsigned sp = 0;
    for (;;) {
      sb();
      unsigned m = 0;
      sfor<0, 48>([&](auto ic) {
        constexpr int i = ic >> 2, u = ic & 3;
        mfma_v<true, true, i == 0>(acc[u], W[i][u], b[i]);
        sb();
        if constexpr (u == 0) { m = umax4(m, b[i]); sb(); }
      });
      if (!__any(m == SENT)) break;
      wait_flags<4>(c, FL_ACT + FS * 16 * rep, 12, it + 1);
      __builtin_amdgcn_s_sleep(2);
      spin_check(c, sp);
      sfor<0, 12>([&](auto i) { b[i] = ld_b(l16, s, i); });
    }
    pre = it + 1 < 32 && seen >= it + 3;
    if (pre) sfor<0, 12>([&](auto i) { bn[i] = ld_b(l16, sn, i); });
    mfma_fence();
    LAS unsigned char* Rb = R + (it & 1) * 16384;
#pragma unroll
    for (int u = 0; u < 4; ++u) *(LAS f32x4*)(Rb + c.wave * 4096 + u * 1024 + l16) = acc[u];
    bar();
    LAS unsigned char* rp = Rb + c.wave * 1024 + l16;
    const f32x4 v = (*(LAS f32x4*)rp + *(LAS f32x4*)(rp + 4096)) + (*(LAS f32x4*)(rp + 8192) + *(LAS f32x4*)(rp + 12288));
    const float o0 = bf_lo(xv.x) + rbf(gbv[0] * rbf(v[0])), o1 = bf_hi(xv.x) + rbf(gbv[1] * rbf(v[1]));
    const float o2 = bf_lo(xv.y) + rbf(gbv[2] * rbf(v[2])), o3 = bf_hi(xv.y) + rbf(gbv[3] * rbf(v[3]));
    st8<0>(ro, xo, (unsigned)s * HB_SZ, u32x2{pk(o0, o1), pk(o2, o3)});
  };
  u32x4 b0[12];
#pragma unroll 1
  for (int it = 0; it < 32; ++it) item(it, b0, b0);
}

__global__ void __launch_bounds__(256, 1) mega_kernel(Params p) {
  extern __shared__ __attribute__((aligned(16))) unsigned char smem[];
  Ctx c;
  const uintptr_t wsa = ((uintptr_t)p.ws + ((2u << 20) - 1)) & ~(uintptr_t)((2u << 20) - 1);
  c.wsp = (const void*)wsa;
  c.ws = mk_rsrc(c.wsp, WS_BYTES);
  c.srd = u32x4{(unsigned)wsa, (unsigned)(wsa >> 32) & 0xFFFFu, WS_BYTES, 0x00020000u};
  c.tag = 0;
  c.lds = (LAS unsigned char*)smem;
  c.tid = threadIdx.x; c.lane = c.tid & 63; c.wave = __builtin_amdgcn_readfirstlane(c.tid >> 6);
  const int grp = blockIdx.x & 7, ix = blockIdx.x >> 3;
  int role, h, rep, r;
  rep = grp - 4; r = ix;
  if (grp < 4) {
    if (ix < 16) { role = ix >> 3; h = grp * 8 + (ix & 7); }
    else if (ix < 26) { role = 6; h = grp * 10 + ix - 16; }
    else if (ix < 31) { role = 6; h = 40 + grp * 5 + ix - 26; }
    else { role = 2; h = 28 + grp; }
  } else {
    if (ix < 24) { role = 3 + (ix >> 3); h = rep * 8 + (ix & 7); }
    else if (ix < 31) { role = 2; h = rep * 7 + ix - 24; }
    else { role = 6; h = 60 + rep; }
  }
  if (role == 0) role_H(p, c, h);
  else if (role == 1) role_V(p, c, h);
  else if (role == 2) role_Q(p, c, h);
  else if (role < 6) {
    if (role == 5) role_U(p, c, h);
    else role_KV(p, c, h, role == 4);
    if (r < 12) role_F1(p, c, rep, r);
    else if (r < 22) role_F2(p, c, rep, r - 12);
  } else {
    role_pre(p, c, h, h < 40 ? h : -1);
    if (h < 40) role_PZ(p, c, h);
    else if (h < 60) role_PX(p, c, h - 40);
  }
}

}

extern "C" void kernel_launch(void* const* d_in, const int* in_sizes, int n_in, void* d_out, int out_size, void* d_ws,
                              size_t ws_size, hipStream_t stream) {
  Params p;
  p.x = d_in[0]; p.mem0 = d_in[1]; p.Wq = d_in[2]; p.Wk = d_in[3]; p.Wv = d_in[4]; p.Wo = d_in[5];
  p.wg = d_in[8]; p.wu = d_in[9]; p.wd = d_in[10]; p.c = d_in[11]; p.wmod = d_in[12];
  p.out = d_out; p.ws = d_ws;
  (void)hipFuncSetAttribute((const void*)mega_kernel, hipFuncAttributeMaxDynamicSharedMemorySize, LDS_BYTES);
  hipLaunchKernelGGL(mega_kernel, dim3(256), dim3(256), LDS_BYTES, stream, p);
}
```
